```python
import torch

def _softmax(x, axis=-1):
    return torch.softmax(x, dim=axis)

def _gelu(x):
    return torch.nn.functional.gelu(x, approximate='tanh')

def _silu(x):
    return torch.nn.functional.silu(x)

def _sigmoid(x):
    return torch.sigmoid(x)

def _layernorm(x, axis=-1, eps=1e-05):
    m = x.mean(dim=axis, keepdim=True)
    v = x.var(dim=axis, keepdim=True, unbiased=False)
    return (x - m) / torch.sqrt(v + eps)

def _scatter_add(tgt, idx, src, axis=0):
    return tgt.clone().index_add_(axis, idx, src)

def _segment_sum(src, seg, n):
    out = torch.zeros((n,) + src.shape[1:], dtype=src.dtype)
    return out.index_add_(0, seg, src)

def _pad(x, pw):
    flat = tuple((v for pair in reversed(pw) for v in pair))
    return torch.nn.functional.pad(x, flat)
kernel_name = 'adaLN_cond__spectral_gfnet__dense_activation_silu__prompt'

def setup_inputs():

    _w = lambda a: (lambda t: t.to(torch.bfloat16) if t.is_floating_point() else t)(torch.as_tensor(a))

    def _A_setup():
        B, S, D = (1, 2048, 512)
        Df = D // 2 + 1
        return {'x': _w((lambda *s: torch.randn(*s, dtype=torch.bfloat16) * 0.05)(B, S, D)), 'w_r': _w((lambda *s: torch.randn(*s, dtype=torch.bfloat16) * 1.6)(S, Df) * 0.02), 'w_i': _w((lambda *s: torch.randn(*s, dtype=torch.bfloat16) * 1.6)(S, Df) * 0.02)}
    _A_in = _A_setup()

    def _B_setup():
        B, S, D, F = (1, 2048, 512, 1792)
        return {'x': _w((lambda *s: torch.randn(*s, dtype=torch.bfloat16) * 0.05)(B, S, D)), 'w1': _w((lambda *s: torch.randn(*s, dtype=torch.bfloat16) * 0.8)(D, F) * D ** (-0.5)), 'w2': _w((lambda *s: torch.randn(*s, dtype=torch.bfloat16) * 0.2)(F, D) * F ** (-0.5))}
    _B_in = _B_setup()
    return {'A_x': _A_in['x'], 'A_w_r': _A_in['w_r'], 'A_w_i': _A_in['w_i'], 'B_x': _B_in['x'], 'B_w1': _B_in['w1'], 'B_w2': _B_in['w2'], 'c': _w((lambda *s: torch.randn(*s, dtype=torch.bfloat16) * 0.2)(1, 512) * 8.0), 'w_mod': _w((lambda *s: torch.randn(*s, dtype=torch.bfloat16) * 0.2)(512, 6 * 512) * 512 ** (-0.5) * 8.0)}

def reference(A_x, A_w_r, A_w_i, B_x, B_w1, B_w2, c, w_mod):

    def _A_ref(x, w_r, w_i):
        B, S, D = x.shape
        X = (lambda x, axes=(-2, -1): torch.fft.rfft2(x.float(), dim=axes))(x, axes=(-2, -1))
        W = w_r + 1j * w_i
        X = X * W
        y = (lambda x, s, axes=(-2, -1): torch.fft.irfft2(x, s=s, dim=axes).to(torch.bfloat16))(X, (S, D), axes=(-2, -1))
        return y

    def _B_ref(x, w1, w2):
        h = torch.einsum('bsd,df->bsf', x, w1)
        h = _silu(h)
        out = torch.einsum('bsf,fd->bsd', h, w2)
        return out
    x = A_x
    mod = (_silu(c) @ w_mod).reshape(c.shape[0], 6, 1, -1)
    sa, ca, ga, sb, cb, gb = (mod[:, 0].clone(), mod[:, 1].clone(), mod[:, 2].clone(), mod[:, 3].clone(), mod[:, 4].clone(), mod[:, 5].clone())
    h = _layernorm(x, axis=-1) * (1 + ca) + sa
    x = x + ga * _A_ref(h, A_w_r, A_w_i)
    h = _layernorm(x, axis=-1) * (1 + cb) + sb
    x = x + gb * _B_ref(h, B_w1, B_w2)
    return x
```

```cpp
#include <hip/hip_runtime.h>
#include <hip/hip_bf16.h>
#include <stdint.h>

namespace {

typedef float f2 __attribute__((ext_vector_type(2)));
typedef float f4 __attribute__((ext_vector_type(4)));
typedef unsigned u2 __attribute__((ext_vector_type(2)));
typedef unsigned u4 __attribute__((ext_vector_type(4)));
typedef short s8 __attribute__((ext_vector_type(8)));
typedef __bf16 bf2 __attribute__((ext_vector_type(2)));

constexpr int S = 2048, D = 512, DF = 257, FF = 1792;

constexpr size_t OFF_FLAGS = 0;
constexpr size_t OFF_MOD   = 65536;
constexpr size_t OFF_TW    = OFF_MOD + 16384;
constexpr size_t OFF_PQ    = OFF_TW + 16384;
constexpr size_t OFF_WF    = OFF_PQ + 32768;
constexpr size_t OFF_T     = OFF_WF + (size_t)256 * 2048 * 4;
constexpr size_t OFF_H2F   = OFF_T + (size_t)256 * 2048 * 8;
constexpr size_t OFF_X1    = OFF_H2F + (size_t)S * D * 2;
constexpr size_t OFF_AF    = OFF_X1 + (size_t)S * D * 2;
constexpr size_t OFF_W1F   = OFF_AF + (size_t)S * FF * 2;
constexpr size_t OFF_W2F   = OFF_W1F + (size_t)D * FF * 2;
constexpr size_t WS_BYTES  = OFF_W2F + (size_t)D * FF * 2;

__device__ __forceinline__ float bflo(unsigned u) { return __builtin_bit_cast(float, u << 16); }
__device__ __forceinline__ float bfhi(unsigned u) { return __builtin_bit_cast(float, u & 0xffff0000u); }
__device__ __forceinline__ f2 unpk(unsigned u) { return f2{bflo(u), bfhi(u)}; }
__device__ __forceinline__ unsigned pk(f2 v) {
  bf2 b = __builtin_convertvector(v, bf2);
  return __builtin_bit_cast(unsigned, b);
}
__device__ __forceinline__ f2 rb2(f2 v) { return unpk(pk(v)); }
__device__ __forceinline__ float rb1(float v) { return bflo(pk(f2{v, v})); }

__device__ __forceinline__ f2 cmul(f2 a, f2 b) {
  f2 r = a.xx * b;
  f2 bs = f2{-b.y, b.x};
  return a.yy * bs + r;
}
__device__ __forceinline__ f2 cmulc(f2 a, f2 b) {
  f2 r = a * b.xx;
  f2 as = f2{a.y, -a.x};
  return as * b.yy + r;
}
template <bool INV> __device__ __forceinline__ f2 cmuld(f2 a, f2 b) { return INV ? cmulc(a, b) : cmul(a, b); }
template <bool INV> __device__ __forceinline__ f2 rot90(f2 a) { return INV ? f2{-a.y, a.x} : f2{a.y, -a.x}; }

template <bool INV> __device__ __forceinline__ void bfly4(f2& a0, f2& a1, f2& a2, f2& a3) {
  f2 t0 = a0 + a2, t1 = a0 - a2, t2 = a1 + a3, t3 = rot90<INV>(a1 - a3);
  a0 = t0 + t2; a1 = t1 + t3; a2 = t0 - t2; a3 = t1 - t3;
}
template <bool INV> __device__ __forceinline__ void bfly8(f2 (&a)[8]) {
  constexpr float R = 0.70710678118654752440f;
  bfly4<INV>(a[0], a[2], a[4], a[6]);
  bfly4<INV>(a[1], a[3], a[5], a[7]);
  f2 e0 = a[0], e1 = a[2], e2 = a[4], e3 = a[6];
  f2 o0 = a[1], o1 = a[3], o2 = a[5], o3 = a[7];
  o1 = (o1 + rot90<INV>(o1)) * R;
  o2 = rot90<INV>(o2);
  o3 = (rot90<INV>(o3) - o3) * R;
  a[0] = e0 + o0; a[4] = e0 - o0;
  a[1] = e1 + o1; a[5] = e1 - o1;
  a[2] = e2 + o2; a[6] = e2 - o2;
  a[3] = e3 + o3; a[7] = e3 - o3;
}

__device__ __forceinline__ void swap32(f2& a, f2& b) {
  const u2 au = __builtin_bit_cast(u2, a), bu = __builtin_bit_cast(u2, b);
  const unsigned ax = au[0], ay = au[1], bx = bu[0], by = bu[1];
  auto rx = __builtin_amdgcn_permlane32_swap(ax, bx, false, false);
  auto ry = __builtin_amdgcn_permlane32_swap(ay, by, false, false);
  a = f2{__builtin_bit_cast(float, (unsigned)rx[0]), __builtin_bit_cast(float, (unsigned)ry[0])};
  b = f2{__builtin_bit_cast(float, (unsigned)rx[1]), __builtin_bit_cast(float, (unsigned)ry[1])};
}
__device__ __forceinline__ void swap16(f2& a, f2& b) {
  const u2 au = __builtin_bit_cast(u2, a), bu = __builtin_bit_cast(u2, b);
  const unsigned ax = au[0], ay = au[1], bx = bu[0], by = bu[1];
  auto rx = __builtin_amdgcn_permlane16_swap(ax, bx, false, false);
  auto ry = __builtin_amdgcn_permlane16_swap(ay, by, false, false);
  a = f2{__builtin_bit_cast(float, (unsigned)rx[0]), __builtin_bit_cast(float, (unsigned)ry[0])};
  b = f2{__builtin_bit_cast(float, (unsigned)rx[1]), __builtin_bit_cast(float, (unsigned)ry[1])};
}
__device__ __forceinline__ float swap8_1(float& a, float& b) {
  int ai = __builtin_bit_cast(int, a), bi = __builtin_bit_cast(int, b);
  int na = __builtin_amdgcn_update_dpp(ai, bi, 0x128, 0xf, 0xc, false);
  int nb = __builtin_amdgcn_update_dpp(bi, ai, 0x128, 0xf, 0x3, false);
  a = __builtin_bit_cast(float, na); b = __builtin_bit_cast(float, nb);
  return 0.f;
}
__device__ __forceinline__ void swap8(f2& a, f2& b) {
  float ax = a.x, ay = a.y, bx = b.x, by = b.y;
  swap8_1(ax, bx); swap8_1(ay, by);
  a = f2{ax, ay}; b = f2{bx, by};
}

__device__ __forceinline__ float dpp_add(float v, const int ctrl_unused) { return v; }
template <int CTRL> __device__ __forceinline__ float dppmov(float v) {
  return __builtin_bit_cast(float, __builtin_amdgcn_update_dpp(0, __builtin_bit_cast(int, v), CTRL, 0xf, 0xf, true));
}
__device__ __forceinline__ float wave_sum(float v) {
  v += dppmov<0xB1>(v);
  v += dppmov<0x4E>(v);
  v += dppmov<0x141>(v);
  v += dppmov<0x140>(v);
  {
    unsigned u = __builtin_bit_cast(unsigned, v);
    auto r = __builtin_amdgcn_permlane16_swap(u, u, false, false);
    v = __builtin_bit_cast(float, (unsigned)r[0]) + __builtin_bit_cast(float, (unsigned)r[1]);
  }
  {
    unsigned u = __builtin_bit_cast(unsigned, v);
    auto r = __builtin_amdgcn_permlane32_swap(u, u, false, false);
    v = __builtin_bit_cast(float, (unsigned)r[0]) + __builtin_bit_cast(float, (unsigned)r[1]);
  }
  return v;
}

__device__ __forceinline__ f4 mfma(u4 a, u4 b, f4 c) {
  return __builtin_amdgcn_mfma_f32_16x16x32_bf16(__builtin_bit_cast(s8, a), __builtin_bit_cast(s8, b), c, 0, 0, 0);
}
__device__ __forceinline__ float silu_f(float v) {
  return v * __builtin_amdgcn_rcpf(1.0f + __expf(-v));
}

__device__ __forceinline__ void ln_mod(const f2 (&xv)[4], const f2 (&sc1)[4], const f2 (&sh)[4], f2 (&h)[4]) {
  f2 s2 = (xv[0] + xv[1]) + (xv[2] + xv[3]);
  float mean = wave_sum(s2.x + s2.y) * (1.0f / 512.0f);
  f2 q2 = f2{0.f, 0.f};
#pragma unroll
  for (int i = 0; i < 4; ++i) { f2 d = xv[i] - mean; q2 += d * d; }
  float var = wave_sum(q2.x + q2.y) * (1.0f / 512.0f);
  float mb = rb1(mean);
  float sd = rb1(__builtin_sqrtf(rb1(rb1(var) + 1e-5f)));
  float rs = 1.0f / sd;
#pragma unroll
  for (int i = 0; i < 4; ++i) {
    f2 d = rb2(xv[i] - mb);
    f2 n = rb2(d * rs);
    h[i] = rb2(rb2(n * sc1[i]) + sh[i]);
  }
}

__device__ __forceinline__ int xpos2(int k0, int k1, int n1, int n0) {
  return k0 * 64 + k1 * 16 + ((n1 ^ ((k1 >> 1) | ((k0 & 1) << 1))) << 2) + n0;
}
__device__ __forceinline__ int xpos3(int k0, int k1, int k2, int n0) {
  return k0 * 64 + k1 * 16 + k2 * 4 + (n0 ^ ((k1 >> 1) | ((k0 & 1) << 1)));
}

struct Tw256 { f2 a[3], b[3], c[3]; };
__device__ __forceinline__ void load_tw256(Tw256& t, const f2* __restrict__ TW, int l) {
#pragma unroll
  for (int r = 1; r < 4; ++r) {
    t.a[r - 1] = TW[(l * r * 8) & 2047];
    t.b[r - 1] = TW[((l & 15) * r * 32) & 2047];
    t.c[r - 1] = TW[((l & 3) * r * 128) & 2047];
  }
}

__device__ __forceinline__ void fft256_fwd(f2 (&v)[4], const Tw256& t, f2* reg, int l) {
  bfly4<false>(v[0], v[1], v[2], v[3]);
#pragma unroll
  for (int r = 1; r < 4; ++r) v[r] = cmul(v[r], t.a[r - 1]);
  swap32(v[0], v[2]); swap32(v[1], v[3]);
  swap16(v[0], v[1]); swap16(v[2], v[3]);
  bfly4<false>(v[0], v[1], v[2], v[3]);
#pragma unroll
  for (int r = 1; r < 4; ++r) v[r] = cmul(v[r], t.b[r - 1]);
  const int d2 = l >> 4, d1 = (l >> 2) & 3, d0 = l & 3;
#pragma unroll
  for (int r = 0; r < 4; ++r) reg[xpos2(d2, r, d1, d0)] = v[r];
  __builtin_amdgcn_wave_barrier();
#pragma unroll
  for (int r = 0; r < 4; ++r) v[r] = reg[xpos2(d2, d1, r, d0)];
  __builtin_amdgcn_wave_barrier();
  bfly4<false>(v[0], v[1], v[2], v[3]);
#pragma unroll
  for (int r = 1; r < 4; ++r) v[r] = cmul(v[r], t.c[r - 1]);
#pragma unroll
  for (int r = 0; r < 4; ++r) reg[xpos3(d2, d1, r, d0)] = v[r];
  __builtin_amdgcn_wave_barrier();
#pragma unroll
  for (int r = 0; r < 4; ++r) v[r] = reg[xpos3(d2, d1, d0, r)];
  __builtin_amdgcn_wave_barrier();
  bfly4<false>(v[0], v[1], v[2], v[3]);
}
__device__ __forceinline__ void fft256_inv(f2 (&v)[4], const Tw256& t, f2* reg, int l) {
  const int d2 = l >> 4, d1 = (l >> 2) & 3, d0 = l & 3;
  bfly4<true>(v[0], v[1], v[2], v[3]);
#pragma unroll
  for (int r = 0; r < 4; ++r) reg[xpos3(d2, d1, d0, r)] = v[r];
  __builtin_amdgcn_wave_barrier();
#pragma unroll
  for (int r = 0; r < 4; ++r) v[r] = reg[xpos3(d2, d1, r, d0)];
  __builtin_amdgcn_wave_barrier();
#pragma unroll
  for (int r = 1; r < 4; ++r) v[r] = cmulc(v[r], t.c[r - 1]);
  bfly4<true>(v[0], v[1], v[2], v[3]);
#pragma unroll
  for (int r = 0; r < 4; ++r) reg[xpos2(d2, d1, r, d0)] = v[r];
  __builtin_amdgcn_wave_barrier();
#pragma unroll
  for (int r = 0; r < 4; ++r) v[r] = reg[xpos2(d2, r, d1, d0)];
  __builtin_amdgcn_wave_barrier();
#pragma unroll
  for (int r = 1; r < 4; ++r) v[r] = cmulc(v[r], t.b[r - 1]);
  bfly4<true>(v[0], v[1], v[2], v[3]);
  swap16(v[0], v[1]); swap16(v[2], v[3]);
  swap32(v[0], v[2]); swap32(v[1], v[3]);
#pragma unroll
  for (int r = 1; r < 4; ++r) v[r] = cmulc(v[r], t.a[r - 1]);
  bfly4<true>(v[0], v[1], v[2], v[3]);
}

__device__ __host__ __forceinline__ int k2_k1(int tt, int kb) {
  int seq = (tt >> 6) * 8 + (tt & 7);
  int q = seq >> 2, k0 = seq & 3, ka = (tt >> 3) & 7;
  return 8 * (k0 + 4 * ka + 32 * kb) + q;
}

constexpr int K0_MOD = 96, K0_WF = 64, K0_MISC = 8, K0_W1 = 56, K0_W2 = 56;
constexpr int K0_BLOCKS = K0_MOD + K0_WF + K0_MISC + K0_W1 + K0_W2;

__device__ __forceinline__ void transpose8x8(const u4 (&in)[8], u4 (&out)[8]) {
#pragma unroll
  for (int c2 = 0; c2 < 4; ++c2) {
#pragma unroll
    for (int r2 = 0; r2 < 4; ++r2) {
      unsigned a = in[2 * r2][c2], b = in[2 * r2 + 1][c2];
      out[2 * c2][r2] = __builtin_amdgcn_perm(b, a, 0x05040100u);
      out[2 * c2 + 1][r2] = __builtin_amdgcn_perm(b, a, 0x07060302u);
    }
  }
}

__global__ __launch_bounds__(256) void k0_prep(const unsigned short* __restrict__ cvec,
                                               const unsigned short* __restrict__ wmod,
                                               const unsigned short* __restrict__ wr,
                                               const unsigned short* __restrict__ wi,
                                               const unsigned short* __restrict__ w1,
                                               const unsigned short* __restrict__ w2,
                                               unsigned char* __restrict__ ws) {
  __shared__ float red[64][33];
  const int tid = threadIdx.x;
  int b = blockIdx.x;
  if (b < K0_MOD) {
    const int co = tid & 3, rg = tid >> 2;
    u4 wv[8];
    float sv[8];
#pragma unroll
    for (int i = 0; i < 8; ++i)
      wv[i] = *(const u4*)(wmod + (size_t)(rg + 64 * i) * 3072 + b * 32 + co * 8);
#pragma unroll
    for (int i = 0; i < 8; ++i) {
      float cv = bflo(cvec[rg + 64 * i]);
      sv[i] = rb1(cv / (1.0f + __expf(-cv)));
    }
    float acc[8];
#pragma unroll
    for (int e = 0; e < 8; ++e) acc[e] = 0.f;
#pragma unroll
    for (int i = 0; i < 8; ++i) {
#pragma unroll
      for (int e2 = 0; e2 < 4; ++e2) {
        acc[2 * e2] += sv[i] * bflo(wv[i][e2]);
        acc[2 * e2 + 1] += sv[i] * bfhi(wv[i][e2]);
      }
    }
#pragma unroll
    for (int e = 0; e < 8; ++e) red[rg][co * 8 + e] = acc[e];
    __syncthreads();
    if (tid < 32) {
      float s = 0.f;
      for (int r = 0; r < 64; ++r) s += red[r][tid];
      ((float*)(ws + OFF_MOD))[b * 32 + tid] = rb1(s);
    }
    return;
  }
  b -= K0_MOD;
  if (b < K0_WF) {
    const int k2 = tid;
    unsigned* wf = (unsigned*)(ws + OFF_WF) + (size_t)k2 * 2048 + b * 32;
#pragma unroll
    for (int tq = 0; tq < 4; ++tq) {
      unsigned v[8];
#pragma unroll
      for (int kb = 0; kb < 8; ++kb) {
        int k1 = k2_k1(4 * b + tq, kb);
        v[kb] = (unsigned)wr[k1 * DF + k2] | ((unsigned)wi[k1 * DF + k2] << 16);
      }
      *(u4*)(wf + tq * 8) = u4{v[0], v[1], v[2], v[3]};
      *(u4*)(wf + tq * 8 + 4) = u4{v[4], v[5], v[6], v[7]};
    }
    return;
  }
  b -= K0_WF;
  if (b < K0_MISC) {
    const int e = b * 256 + tid;
    {
      double sv, cv;
      sincospi((double)e * (1.0 / 1024.0), &sv, &cv);
      ((f2*)(ws + OFF_TW))[e] = f2{(float)cv, (float)(-sv)};
    }
    {
      const int tt = e >> 3, kb = e & 7;
      const int k1 = k2_k1(tt, kb), nk = (2048 - k1) & 2047;
      f2 a0 = f2{bflo(wr[k1 * DF]), bflo(wi[k1 * DF])};
      f2 a1 = f2{bflo(wr[nk * DF]), -bflo(wi[nk * DF])};
      f2 b0 = f2{bflo(wr[k1 * DF + 256]), bflo(wi[k1 * DF + 256])};
      f2 b1 = f2{bflo(wr[nk * DF + 256]), -bflo(wi[nk * DF + 256])};
      f2 w0h = (a0 + a1) * 0.5f, w256h = (b0 + b1) * 0.5f;
      f2 P = (w0h + w256h) * 0.5f, Q = (w0h - w256h) * 0.5f;
      ((f4*)(ws + OFF_PQ))[e] = f4{P.x, P.y, Q.x, Q.y};
    }
    return;
  }
  b -= K0_MISC;
  if (b < K0_W1) {
    const int g = b * 256 + tid;
    const int no = g % 224, ko = g / 224;
    u4 in[8], out[8];
#pragma unroll
    for (int r = 0; r < 8; ++r) in[r] = *(const u4*)(w1 + (size_t)(ko * 8 + r) * FF + no * 8);
    transpose8x8(in, out);
    const int n0 = no * 8, c = n0 >> 5, nbl = (n0 >> 4) & 1, kb = ko >> 2, kg = ko & 3;
    u4* dst = (u4*)(ws + OFF_W1F) + (((size_t)c * 16 + kb) * 2 + nbl) * 64 + (n0 & 15) + 16 * kg;
#pragma unroll
    for (int cc = 0; cc < 8; ++cc) dst[cc] = out[cc];
    return;
  }
  b -= K0_W1;
  {
    const int g = b * 256 + tid;
    const int co = g & 63, g4 = (g >> 6) & 3, c = g >> 8;
    u4 in[8], out[8];
#pragma unroll
    for (int e = 0; e < 8; ++e) {
      int hid = 32 * c + 16 * (e >> 2) + 4 * g4 + (e & 3);
      in[e] = *(const u4*)(w2 + (size_t)hid * D + co * 8);
    }
    transpose8x8(in, out);
    const int jj = co >> 3, h = (co >> 2) & 1, i2 = co & 3;
    u4* dst = (u4*)(ws + OFF_W2F) + (((size_t)jj * 56 + c) * 4 + 2 * h) * 64 + 16 * g4 + 4 * i2;
#pragma unroll
    for (int cc = 0; cc < 8; ++cc) dst[(cc >> 2) * 64 + (cc & 3)] = out[cc];
  }
}

constexpr int ROWP = 256 + 8;

__global__ __launch_bounds__(512) void k1_rows(const unsigned* __restrict__ x32, unsigned char* __restrict__ ws) {
  __shared__ __attribute__((aligned(16))) f2 lds[8 * ROWP];
  const int b = blockIdx.x;
  const int j = (b & 7) * 32 + (b >> 3);
  const int tid = threadIdx.x, w = tid >> 6, l = tid & 63;
  const f2* TW = (const f2*)(ws + OFF_TW);
  const f2* mod2 = (const f2*)(ws + OFF_MOD);
  const int s = j + 256 * w;
  f2 xv[4], sc1[4], sh[4], v[4];
#pragma unroll
  for (int i = 0; i < 4; ++i) xv[i] = unpk(x32[(size_t)s * 256 + l + 64 * i]);
#pragma unroll
  for (int i = 0; i < 4; ++i) {
    sh[i] = mod2[l + 64 * i];
    sc1[i] = rb2(mod2[256 + l + 64 * i] + 1.0f);
  }
  Tw256 tw;
  load_tw256(tw, TW, l);
  ln_mod(xv, sc1, sh, v);
  f2* reg = lds + w * ROWP;
  fft256_fwd(v, tw, reg, l);
  {
    const int kl = (l >> 4) + 4 * ((l >> 2) & 3) + 16 * (l & 3);
#pragma unroll
    for (int r = 0; r < 4; ++r) reg[kl + 64 * r] = v[r];
  }
  __syncthreads();
  if (tid < 256) {
    const int k2 = tid, kn = (256 - k2) & 255;
    const f2 t512 = TW[4 * k2];
    f2 a[8];
#pragma unroll
    for (int m = 0; m < 8; ++m) {
      f2 z = lds[m * ROWP + k2], zr = lds[m * ROWP + kn];
      zr.y = -zr.y;
      f2 e = z + zr, o = rot90<false>(z - zr);
      f2 xx = e + cmul(o, t512);
      if (k2 == 0) xx.y = e.x - o.x;
      a[m] = xx;
    }
    bfly8<false>(a);
#pragma unroll
    for (int q = 1; q < 8; ++q) a[q] = cmul(a[q], TW[j * q]);
    f4* dst = (f4*)(ws + OFF_T) + ((size_t)k2 * 256 + j) * 4;
#pragma unroll
    for (int q = 0; q < 4; ++q) dst[q] = f4{a[2 * q].x, a[2 * q].y, a[2 * q + 1].x, a[2 * q + 1].y};
  }
}

constexpr int SEQP = 64 + 1;
__global__ __launch_bounds__(256) void k2_cols(unsigned char* __restrict__ ws) {
  __shared__ __attribute__((aligned(16))) f2 lds[2048 + 64];
  const int k2 = blockIdx.x;
  const int t = threadIdx.x, l = t & 63;
  const f2* TW = (const f2*)(ws + OFF_TW);
  f4* col = (f4*)(ws + OFF_T) + (size_t)k2 * 1024;
  const int jr = t >> 2, qp = t & 3;
  f2 a0[4], a1[4];
#pragma unroll
  for (int i = 0; i < 4; ++i) {
    f4 c = col[t + 256 * i];
    a0[i] = c.xy; a1[i] = c.zw;
  }
  f2 twA[3], twB[7];
#pragma unroll
  for (int r = 1; r < 4; ++r) twA[r - 1] = TW[jr * r * 8];
  const int bb = l >> 3;
#pragma unroll
  for (int r = 1; r < 8; ++r) twB[r - 1] = TW[bb * r * 32];
  u4 wf0, wf1;
  if (k2 != 0) {
    const u4* wf = (const u4*)(ws + OFF_WF) + ((size_t)k2 * 256 + t) * 2;
    wf0 = wf[0]; wf1 = wf[1];
  }
  bfly4<false>(a0[0], a0[1], a0[2], a0[3]);
  bfly4<false>(a1[0], a1[1], a1[2], a1[3]);
#pragma unroll
  for (int r = 1; r < 4; ++r) { a0[r] = cmul(a0[r], twA[r - 1]); a1[r] = cmul(a1[r], twA[r - 1]); }
#pragma unroll
  for (int r = 0; r < 4; ++r) {
    lds[((2 * qp) * 4 + r) * SEQP + jr] = a0[r];
    lds[((2 * qp + 1) * 4 + r) * SEQP + jr] = a1[r];
  }
  __syncthreads();
  const int seq = (t >> 6) * 8 + (t & 7);
  f2 v[8];
#pragma unroll
  for (int a = 0; a < 8; ++a) v[a] = lds[seq * SEQP + 8 * a + bb];
  bfly8<false>(v);
#pragma unroll
  for (int r = 1; r < 8; ++r) v[r] = cmul(v[r], twB[r - 1]);
#pragma unroll
  for (int i = 0; i < 4; ++i) swap32(v[i], v[i + 4]);
  swap16(v[0], v[2]); swap16(v[1], v[3]); swap16(v[4], v[6]); swap16(v[5], v[7]);
#pragma unroll
  for (int i = 0; i < 8; i += 2) swap8(v[i], v[i + 1]);
  bfly8<false>(v);
  if (k2 != 0) {
#pragma unroll
    for (int i = 0; i < 4; ++i) {
      v[i] = cmul(v[i], unpk(wf0[i]));
      v[i + 4] = cmul(v[i + 4], unpk(wf1[i]));
    }
  } else {
    __syncthreads();
#pragma unroll
    for (int kb = 0; kb < 8; ++kb) lds[k2_k1(t, kb)] = v[kb];
    __syncthreads();
    const f4* pq = (const f4*)(ws + OFF_PQ) + t * 8;
#pragma unroll
    for (int kb = 0; kb < 8; ++kb) {
      f2 fn = lds[(2048 - k2_k1(t, kb)) & 2047];
      fn.y = -fn.y;
      f4 c = pq[kb];
      v[kb] = cmul(v[kb], c.xy) + cmul(fn, c.zw);
    }
  }
  bfly8<true>(v);
#pragma unroll
  for (int i = 0; i < 8; i += 2) swap8(v[i], v[i + 1]);
  swap16(v[0], v[2]); swap16(v[1], v[3]); swap16(v[4], v[6]); swap16(v[5], v[7]);
#pragma unroll
  for (int i = 0; i < 4; ++i) swap32(v[i], v[i + 4]);
#pragma unroll
  for (int r = 1; r < 8; ++r) v[r] = cmulc(v[r], twB[r - 1]);
  bfly8<true>(v);
  __syncthreads();
#pragma unroll
  for (int a = 0; a < 8; ++a) lds[seq * SEQP + 8 * a + bb] = v[a];
  __syncthreads();
#pragma unroll
  for (int r = 0; r < 4; ++r) {
    a0[r] = lds[((2 * qp) * 4 + r) * SEQP + jr];
    a1[r] = lds[((2 * qp + 1) * 4 + r) * SEQP + jr];
  }
#pragma unroll
  for (int r = 1; r < 4; ++r) { a0[r] = cmulc(a0[r], twA[r - 1]); a1[r] = cmulc(a1[r], twA[r - 1]); }
  bfly4<true>(a0[0], a0[1], a0[2], a0[3]);
  bfly4<true>(a1[0], a1[1], a1[2], a1[3]);
#pragma unroll
  for (int i = 0; i < 4; ++i) col[t + 256 * i] = f4{a0[i].x, a0[i].y, a1[i].x, a1[i].y};
}

__device__ __forceinline__ void gmap(int b, int& g, int& jj) {
  g = (b & 7) + 8 * (b >> 6);
  jj = (b >> 3) & 7;
}

__global__ __launch_bounds__(512) void k3_rows(const unsigned* __restrict__ x32, unsigned char* __restrict__ ws) {
  __shared__ __attribute__((aligned(16))) f2 lds[8 * ROWP];
  int g, jj;
  gmap(blockIdx.x, g, jj);
  const int j = 8 * g + jj;
  const int tid = threadIdx.x, w = tid >> 6, l = tid & 63;
  const f2* TW = (const f2*)(ws + OFF_TW);
  const f2* mod2 = (const f2*)(ws + OFF_MOD);
  const int s = j + 256 * w;
  f2 xv[4], ga[4], sc1[4], sh[4], v[4], t512[4];
  const int kl = (l >> 4) + 4 * ((l >> 2) & 3) + 16 * (l & 3);
#pragma unroll
  for (int i = 0; i < 4; ++i) xv[i] = unpk(x32[(size_t)s * 256 + l + 64 * i]);
#pragma unroll
  for (int i = 0; i < 4; ++i) {
    ga[i] = mod2[512 + l + 64 * i];
    sh[i] = mod2[768 + l + 64 * i];
    sc1[i] = rb2(mod2[1024 + l + 64 * i] + 1.0f);
    t512[i] = TW[4 * (kl + 64 * i)];
  }
  Tw256 tw;
  load_tw256(tw, TW, l);
  if (tid < 256) {
    const int k2 = tid;
    const f4* src = (const f4*)(ws + OFF_T) + ((size_t)k2 * 256 + j) * 4;
    f2 a[8];
#pragma unroll
    for (int q = 0; q < 4; ++q) { f4 c = src[q]; a[2 * q] = c.xy; a[2 * q + 1] = c.zw; }
#pragma unroll
    for (int q = 1; q < 8; ++q) a[q] = cmulc(a[q], TW[j * q]);
    bfly8<true>(a);
#pragma unroll
    for (int m = 0; m < 8; ++m) lds[m * ROWP + k2] = a[m];
  }
  __syncthreads();
  f2* reg = lds + w * ROWP;
#pragma unroll
  for (int r = 0; r < 4; ++r) {
    const int k = kl + 64 * r, kn = (256 - k) & 255;
    f2 xk = reg[k], xn = reg[kn];
    xn.y = -xn.y;
    f2 e = xk + xn, o = cmulc(xk - xn, t512[r]);
    f2 zp = e + rot90<true>(o);
    if (k == 0) zp = f2{xk.x + xk.y, xk.x - xk.y};
    v[r] = zp;
  }
  __builtin_amdgcn_wave_barrier();
  fft256_inv(v, tw, reg, l);
  f2 x1[4], h2[4];
#pragma unroll
  for (int i = 0; i < 4; ++i) {
    f2 y = rb2(v[i] * 4.76837158203125e-07f);
    x1[i] = rb2(xv[i] + rb2(ga[i] * y));
  }
  ln_mod(x1, sc1, sh, h2);
  unsigned* st = (unsigned*)reg;
#pragma unroll
  for (int i = 0; i < 4; ++i) {
    st[l + 64 * i] = pk(x1[i]);
    st[256 + l + 64 * i] = pk(h2[i]);
  }
  __syncthreads();
  {
    u4 xo = *(const u4*)(st + 4 * l);
    *((u4*)(ws + OFF_X1) + (size_t)(64 * g + 8 * jj + w) * 64 + l) = xo;
    const int ch = 8 * w + (l >> 3), m = l & 7;
    u4 ho = *(const u4*)((const unsigned*)(lds + m * ROWP) + 256 + 4 * ch);
    const int tb = jj >> 1, tok = 8 * (jj & 1) + m;
    *((u4*)(ws + OFF_H2F) + (((size_t)g * 4 + tb) * 16 + (ch >> 2)) * 64 + tok + 16 * (ch & 3)) = ho;
  }
}

__global__ __launch_bounds__(512) void k4_gemm1(unsigned char* __restrict__ ws) {
  __shared__ u4 hs[64 * 64];
  int g, jj;
  gmap(blockIdx.x, g, jj);
  const int tid = threadIdx.x, w = __builtin_amdgcn_readfirstlane(tid >> 6), l = tid & 63;
  const u4* h2f = (const u4*)(ws + OFF_H2F) + (size_t)g * 4096;
  u4 hv[8];
#pragma unroll
  for (int i = 0; i < 8; ++i) hv[i] = h2f[(w * 8 + i) * 64 + l];
  u4 wv[32];
  const int c = 7 * jj + (w < 7 ? w : 6);
  const u4* w1f = (const u4*)(ws + OFF_W1F) + (size_t)c * 2048;
  if (w < 7) {
#pragma unroll
    for (int i = 0; i < 32; ++i) wv[i] = w1f[i * 64 + l];
  }
#pragma unroll
  for (int i = 0; i < 8; ++i) hs[(w * 8 + i) * 64 + l] = hv[i];
  __syncthreads();
  if (w < 7) {
    f4 acc[2][4];
#pragma unroll
    for (int n = 0; n < 2; ++n)
#pragma unroll
      for (int tb = 0; tb < 4; ++tb) acc[n][tb] = f4{0.f, 0.f, 0.f, 0.f};
#pragma unroll
    for (int kb = 0; kb < 16; ++kb) {
#pragma unroll
      for (int tb = 0; tb < 4; ++tb) {
        u4 hb = hs[(tb * 16 + kb) * 64 + l];
        acc[0][tb] = mfma(wv[2 * kb], hb, acc[0][tb]);
        acc[1][tb] = mfma(wv[2 * kb + 1], hb, acc[1][tb]);
      }
    }
    u4* af = (u4*)(ws + OFF_AF) + ((size_t)g * 8 + jj) * 28 * 64;
#pragma unroll
    for (int tb = 0; tb < 4; ++tb) {
      u4 o;
#pragma unroll
      for (int n = 0; n < 2; ++n) {
        f2 p0 = rb2(acc[n][tb].xy), p1 = rb2(acc[n][tb].zw);
        o[2 * n] = pk(f2{silu_f(p0.x), silu_f(p0.y)});
        o[2 * n + 1] = pk(f2{silu_f(p1.x), silu_f(p1.y)});
      }
      af[(tb * 7 + w) * 64 + l] = o;
    }
  }
}

__global__ __launch_bounds__(512) void k5_gemm2(unsigned char* __restrict__ ws, unsigned short* __restrict__ out) {
  __shared__ f4 red[8 * 16 * 64];
  int g, jj;
  gmap(blockIdx.x, g, jj);
  const int tid = threadIdx.x, w = __builtin_amdgcn_readfirstlane(tid >> 6), l = tid & 63;
  const u4* w2f = (const u4*)(ws + OFF_W2F) + ((size_t)jj * 56 + 7 * w) * 256;
  const u4* af = (const u4*)(ws + OFF_AF) + ((size_t)g * 8 + w) * 28 * 64;
  u4 wv[28], av[28];
#pragma unroll
  for (int i = 0; i < 28; ++i) wv[i] = w2f[i * 64 + l];
#pragma unroll
  for (int i = 0; i < 28; ++i) av[i] = af[i * 64 + l];
  const int h = w & 1, tbf = w >> 1;
  const int tok = 16 * tbf + (l & 15);
  const int col = 64 * jj + 32 * h + 8 * (l >> 4);
  const u4 x1v = *((const u4*)(ws + OFF_X1) + (size_t)(64 * g + tok) * 64 + (col >> 3));
  const f4 gb0 = *(const f4*)((const float*)(ws + OFF_MOD) + 2560 + col);
  const f4 gb1 = *(const f4*)((const float*)(ws + OFF_MOD) + 2560 + col + 4);
#pragma unroll
  for (int tb = 0; tb < 4; ++tb) {
    f4 acc[4];
#pragma unroll
    for (int nb = 0; nb < 4; ++nb) acc[nb] = f4{0.f, 0.f, 0.f, 0.f};
#pragma unroll
    for (int c7 = 0; c7 < 7; ++c7)
#pragma unroll
      for (int nb = 0; nb < 4; ++nb) acc[nb] = mfma(wv[c7 * 4 + nb], av[tb * 7 + c7], acc[nb]);
#pragma unroll
    for (int nb = 0; nb < 4; ++nb) red[(w * 16 + tb * 4 + nb) * 64 + l] = acc[nb];
  }
  __syncthreads();
  f4 s0 = f4{0.f, 0.f, 0.f, 0.f}, s1 = s0;
#pragma unroll
  for (int ww = 0; ww < 8; ++ww) {
    s0 += red[(ww * 16 + tbf * 4 + 2 * h) * 64 + l];
    s1 += red[(ww * 16 + tbf * 4 + 2 * h + 1) * 64 + l];
  }
  u4 o;
  o[0] = pk(unpk(x1v[0]) + rb2(gb0.xy * rb2(s0.xy)));
  o[1] = pk(unpk(x1v[1]) + rb2(gb0.zw * rb2(s0.zw)));
  o[2] = pk(unpk(x1v[2]) + rb2(gb1.xy * rb2(s1.xy)));
  o[3] = pk(unpk(x1v[3]) + rb2(gb1.zw * rb2(s1.zw)));
  const int srow = 8 * g + (tok >> 3) + 256 * (tok & 7);
  *(u4*)(out + (size_t)srow * D + col) = o;
}

}

extern "C" void kernel_launch(void* const* d_in, const int* in_sizes, int n_in, void* d_out, int out_size,
                              void* d_ws, size_t ws_size, hipStream_t stream) {
  const unsigned short* x = (const unsigned short*)d_in[0];
  const unsigned short* wr = (const unsigned short*)d_in[1];
  const unsigned short* wi = (const unsigned short*)d_in[2];
  const unsigned short* w1 = (const unsigned short*)d_in[4];
  const unsigned short* w2 = (const unsigned short*)d_in[5];
  const unsigned short* cv = (const unsigned short*)d_in[6];
  const unsigned short* wm = (const unsigned short*)d_in[7];
  unsigned char* ws = (unsigned char*)d_ws;
  k0_prep<<<K0_BLOCKS, 256, 0, stream>>>(cv, wm, wr, wi, w1, w2, ws);
  k1_rows<<<256, 512, 0, stream>>>((const unsigned*)x, ws);
  k2_cols<<<256, 256, 0, stream>>>(ws);
  k3_rows<<<256, 512, 0, stream>>>((const unsigned*)x, ws);
  k4_gemm1<<<256, 512, 0, stream>>>(ws);
  k5_gemm2<<<256, 512, 0, stream>>>(ws, (unsigned short*)d_out);
}
```

```cpp
#include <hip/hip_runtime.h>
#include <hip/hip_bf16.h>
#include <stdint.h>
#include <type_traits>

namespace {

typedef float f2 __attribute__((ext_vector_type(2)));
typedef float f4 __attribute__((ext_vector_type(4)));
typedef unsigned u2 __attribute__((ext_vector_type(2)));
typedef unsigned u4 __attribute__((ext_vector_type(4)));
typedef short s8 __attribute__((ext_vector_type(8)));
typedef __bf16 bf2 __attribute__((ext_vector_type(2)));
#define GAS __attribute__((address_space(1)))
typedef GAS unsigned gu32;
#define RLX_AGENT __ATOMIC_RELAXED, __HIP_MEMORY_SCOPE_AGENT
#define SCHED_FENCE() __builtin_amdgcn_sched_barrier(0)

constexpr int S = 2048, D = 512, DF = 257, FF = 1792;

constexpr size_t OFF_F1    = 0;
constexpr size_t OFF_F2    = 32768;
constexpr size_t FLAG_BYTES = 32768 + 65536;
constexpr size_t OFF_MODK1 = 131072;
constexpr size_t OFF_MODK3 = OFF_MODK1 + 2048;
constexpr size_t OFF_GB    = OFF_MODK3 + 3072;
constexpr size_t OFF_PQ    = OFF_MODK1 + 8192;
constexpr size_t OFF_WF    = OFF_PQ + 32768;
constexpr size_t OFF_T     = OFF_WF + (size_t)256 * 2048 * 4;
constexpr size_t OFF_H2F   = OFF_T + (size_t)256 * 2048 * 8;
constexpr size_t OFF_X1    = OFF_H2F + (size_t)S * D * 2;
constexpr size_t OFF_AF    = OFF_X1 + (size_t)S * D * 2;
constexpr size_t OFF_W1F   = OFF_AF + (size_t)S * FF * 2;
constexpr size_t OFF_W2F   = OFF_W1F + (size_t)D * FF * 2;
constexpr size_t WS_BYTES  = OFF_W2F + (size_t)D * FF * 2;

#define STAMP(kid, i) do {} while (0)
#define STAMPT(kid, i, thr) do {} while (0)
#define STAMP_DRAIN() do {} while (0)

__device__ __forceinline__ float bflo(unsigned u) { return __builtin_bit_cast(float, u << 16); }
__device__ __forceinline__ float bfhi(unsigned u) { return __builtin_bit_cast(float, u & 0xffff0000u); }
__device__ __forceinline__ f2 unpk(unsigned u) { return f2{bflo(u), bfhi(u)}; }
__device__ __forceinline__ unsigned pk(f2 v) {
  bf2 b = __builtin_convertvector(v, bf2);
  return __builtin_bit_cast(unsigned, b);
}
__device__ __forceinline__ f2 rb2(f2 v) { return unpk(pk(v)); }
__device__ __forceinline__ float rb1(float v) { return bflo(pk(f2{v, v})); }

__device__ __forceinline__ f2 tw2048(int t) {
  const float f = (float)t * (1.0f / 2048.0f);
  return f2{__builtin_amdgcn_cosf(f), -__builtin_amdgcn_sinf(f)};
}

__device__ __forceinline__ f2 cmul(f2 a, f2 w) {
  f2 t, d;
  asm("v_pk_mul_f32 %0, %1, %2 op_sel:[1,1] op_sel_hi:[1,0] neg_lo:[0,1]" : "=v"(t) : "v"(a), "v"(w));
  asm("v_pk_fma_f32 %0, %1, %2, %3 op_sel:[0,0,0] op_sel_hi:[0,1,1]" : "=v"(d) : "v"(a), "v"(w), "v"(t));
  return d;
}
__device__ __forceinline__ f2 cmulc(f2 a, f2 w) {
  f2 t, d;
  asm("v_pk_mul_f32 %0, %1, %2 op_sel:[0,0] op_sel_hi:[1,0]" : "=v"(t) : "v"(a), "v"(w));
  asm("v_pk_fma_f32 %0, %1, %2, %3 op_sel:[1,1,0] op_sel_hi:[0,1,1] neg_hi:[1,0,0]" : "=v"(d) : "v"(a), "v"(w), "v"(t));
  return d;
}
template <bool CONJ> __device__ __forceinline__ void cmul3(f2& a0, f2& a1, f2& a2, f2 w0, f2 w1, f2 w2) {
  f2 t0, t1, t2;
  if (!CONJ) {
    asm("v_pk_mul_f32 %3, %0, %6 op_sel:[1,1] op_sel_hi:[1,0] neg_lo:[0,1]\n\t"
        "v_pk_mul_f32 %4, %1, %7 op_sel:[1,1] op_sel_hi:[1,0] neg_lo:[0,1]\n\t"
        "v_pk_mul_f32 %5, %2, %8 op_sel:[1,1] op_sel_hi:[1,0] neg_lo:[0,1]\n\t"
        "v_pk_fma_f32 %0, %0, %6, %3 op_sel:[0,0,0] op_sel_hi:[0,1,1]\n\t"
        "v_pk_fma_f32 %1, %1, %7, %4 op_sel:[0,0,0] op_sel_hi:[0,1,1]\n\t"
        "v_pk_fma_f32 %2, %2, %8, %5 op_sel:[0,0,0] op_sel_hi:[0,1,1]"
        : "+v"(a0), "+v"(a1), "+v"(a2), "=&v"(t0), "=&v"(t1), "=&v"(t2) : "v"(w0), "v"(w1), "v"(w2));
  } else {
    asm("v_pk_mul_f32 %3, %0, %6 op_sel:[0,0] op_sel_hi:[1,0]\n\t"
        "v_pk_mul_f32 %4, %1, %7 op_sel:[0,0] op_sel_hi:[1,0]\n\t"
        "v_pk_mul_f32 %5, %2, %8 op_sel:[0,0] op_sel_hi:[1,0]\n\t"
        "v_pk_fma_f32 %0, %0, %6, %3 op_sel:[1,1,0] op_sel_hi:[0,1,1] neg_hi:[1,0,0]\n\t"
        "v_pk_fma_f32 %1, %1, %7, %4 op_sel:[1,1,0] op_sel_hi:[0,1,1] neg_hi:[1,0,0]\n\t"
        "v_pk_fma_f32 %2, %2, %8, %5 op_sel:[1,1,0] op_sel_hi:[0,1,1] neg_hi:[1,0,0]"
        : "+v"(a0), "+v"(a1), "+v"(a2), "=&v"(t0), "=&v"(t1), "=&v"(t2) : "v"(w0), "v"(w1), "v"(w2));
  }
}
template <bool CONJ> __device__ __forceinline__ void cmul4(f2& a0, f2& a1, f2& a2, f2& a3, f2 w0, f2 w1, f2 w2, f2 w3) {
  f2 t0, t1, t2, t3;
  if (!CONJ) {
    asm("v_pk_mul_f32 %4, %0, %8 op_sel:[1,1] op_sel_hi:[1,0] neg_lo:[0,1]\n\t"
        "v_pk_mul_f32 %5, %1, %9 op_sel:[1,1] op_sel_hi:[1,0] neg_lo:[0,1]\n\t"
        "v_pk_mul_f32 %6, %2, %10 op_sel:[1,1] op_sel_hi:[1,0] neg_lo:[0,1]\n\t"
        "v_pk_mul_f32 %7, %3, %11 op_sel:[1,1] op_sel_hi:[1,0] neg_lo:[0,1]\n\t"
        "v_pk_fma_f32 %0, %0, %8, %4 op_sel:[0,0,0] op_sel_hi:[0,1,1]\n\t"
        "v_pk_fma_f32 %1, %1, %9, %5 op_sel:[0,0,0] op_sel_hi:[0,1,1]\n\t"
        "v_pk_fma_f32 %2, %2, %10, %6 op_sel:[0,0,0] op_sel_hi:[0,1,1]\n\t"
        "v_pk_fma_f32 %3, %3, %11, %7 op_sel:[0,0,0] op_sel_hi:[0,1,1]"
        : "+v"(a0), "+v"(a1), "+v"(a2), "+v"(a3), "=&v"(t0), "=&v"(t1), "=&v"(t2), "=&v"(t3)
        : "v"(w0), "v"(w1), "v"(w2), "v"(w3));
  } else {
    asm("v_pk_mul_f32 %4, %0, %8 op_sel:[0,0] op_sel_hi:[1,0]\n\t"
        "v_pk_mul_f32 %5, %1, %9 op_sel:[0,0] op_sel_hi:[1,0]\n\t"
        "v_pk_mul_f32 %6, %2, %10 op_sel:[0,0] op_sel_hi:[1,0]\n\t"
        "v_pk_mul_f32 %7, %3, %11 op_sel:[0,0] op_sel_hi:[1,0]\n\t"
        "v_pk_fma_f32 %0, %0, %8, %4 op_sel:[1,1,0] op_sel_hi:[0,1,1] neg_hi:[1,0,0]\n\t"
        "v_pk_fma_f32 %1, %1, %9, %5 op_sel:[1,1,0] op_sel_hi:[0,1,1] neg_hi:[1,0,0]\n\t"
        "v_pk_fma_f32 %2, %2, %10, %6 op_sel:[1,1,0] op_sel_hi:[0,1,1] neg_hi:[1,0,0]\n\t"
        "v_pk_fma_f32 %3, %3, %11, %7 op_sel:[1,1,0] op_sel_hi:[0,1,1] neg_hi:[1,0,0]"
        : "+v"(a0), "+v"(a1), "+v"(a2), "+v"(a3), "=&v"(t0), "=&v"(t1), "=&v"(t2), "=&v"(t3)
        : "v"(w0), "v"(w1), "v"(w2), "v"(w3));
  }
}
template <bool CONJ> __device__ __forceinline__ void cmul7(f2 (&a)[8], const f2 (&w)[7]) {
  cmul3<CONJ>(a[1], a[2], a[3], w[0], w[1], w[2]);
  cmul4<CONJ>(a[4], a[5], a[6], a[7], w[3], w[4], w[5], w[6]);
}
__device__ __forceinline__ void herm2(f2& r0, f2& r1, f2 z0, f2 q0, f2 w0, f2 z1, f2 q1, f2 w1) {
  f2 d0, d1;
  asm("v_pk_add_f32 %0, %4, %5 neg_hi:[0,1]\n\t"
      "v_pk_add_f32 %2, %4, %5 neg_lo:[0,1]\n\t"
      "v_pk_add_f32 %1, %7, %8 neg_hi:[0,1]\n\t"
      "v_pk_add_f32 %3, %7, %8 neg_lo:[0,1]\n\t"
      "v_pk_fma_f32 %0, %2, %6, %0 op_sel:[1,1,0] op_sel_hi:[1,0,1] neg_lo:[0,1,0]\n\t"
      "v_pk_fma_f32 %1, %3, %9, %1 op_sel:[1,1,0] op_sel_hi:[1,0,1] neg_lo:[0,1,0]\n\t"
      "v_pk_fma_f32 %0, %2, %6, %0 op_sel:[0,0,0] op_sel_hi:[0,1,1]\n\t"
      "v_pk_fma_f32 %1, %3, %9, %1 op_sel:[0,0,0] op_sel_hi:[0,1,1]"
      : "=&v"(r0), "=&v"(r1), "=&v"(d0), "=&v"(d1) : "v"(z0), "v"(q0), "v"(w0), "v"(z1), "v"(q1), "v"(w1));
}
__device__ __forceinline__ f2 cfma(f2 a, f2 w, f2 c) {
  f2 t, d;
  asm("v_pk_fma_f32 %0, %1, %2, %3 op_sel:[1,1,0] op_sel_hi:[1,0,1] neg_lo:[0,1,0]" : "=v"(t) : "v"(a), "v"(w), "v"(c));
  asm("v_pk_fma_f32 %0, %1, %2, %3 op_sel:[0,0,0] op_sel_hi:[0,1,1]" : "=v"(d) : "v"(a), "v"(w), "v"(t));
  return d;
}
__device__ __forceinline__ f2 add_mi(f2 a, f2 b) {
  f2 d;
  asm("v_pk_add_f32 %0, %1, %2 op_sel:[0,1] op_sel_hi:[1,0] neg_hi:[0,1]" : "=v"(d) : "v"(a), "v"(b));
  return d;
}
__device__ __forceinline__ f2 add_pi(f2 a, f2 b) {
  f2 d;
  asm("v_pk_add_f32 %0, %1, %2 op_sel:[0,1] op_sel_hi:[1,0] neg_lo:[0,1]" : "=v"(d) : "v"(a), "v"(b));
  return d;
}
__device__ __forceinline__ f2 add_cj(f2 a, f2 b) {
  f2 d;
  asm("v_pk_add_f32 %0, %1, %2 neg_hi:[0,1]" : "=v"(d) : "v"(a), "v"(b));
  return d;
}
__device__ __forceinline__ f2 sub_cj(f2 a, f2 b) {
  f2 d;
  asm("v_pk_add_f32 %0, %1, %2 neg_lo:[0,1]" : "=v"(d) : "v"(a), "v"(b));
  return d;
}
template <bool INV> __device__ __forceinline__ f2 addr(f2 a, f2 b) { return INV ? add_pi(a, b) : add_mi(a, b); }
template <bool INV> __device__ __forceinline__ f2 subr(f2 a, f2 b) { return INV ? add_mi(a, b) : add_pi(a, b); }

template <bool INV> __device__ __forceinline__ void bfly4(f2& a0, f2& a1, f2& a2, f2& a3) {
  const f2 t0 = a0 + a2, t1 = a0 - a2, t2 = a1 + a3, u = a1 - a3;
  a0 = t0 + t2; a2 = t0 - t2; a1 = addr<INV>(t1, u); a3 = subr<INV>(t1, u);
}
template <bool INV> __device__ __forceinline__ void bfly8(f2 (&a)[8]) {
  constexpr float R = 0.70710678118654752440f;
  bfly4<INV>(a[0], a[2], a[4], a[6]);
  bfly4<INV>(a[1], a[3], a[5], a[7]);
  const f2 e0 = a[0], e1 = a[2], e2 = a[4], e3 = a[6];
  const f2 o0 = a[1], o2 = a[5];
  const f2 p1 = addr<INV>(a[3], a[3]);
  const f2 p3 = subr<INV>(a[7], a[7]);
  a[0] = e0 + o0; a[4] = e0 - o0;
  a[1] = e1 + R * p1; a[5] = e1 - R * p1;
  a[2] = addr<INV>(e2, o2); a[6] = subr<INV>(e2, o2);
  a[3] = e3 - R * p3; a[7] = e3 + R * p3;
}
#define XL_PAD4(v) asm volatile("s_nop 1" : "+v"(v[0]), "+v"(v[1]), "+v"(v[2]), "+v"(v[3]))
#define XL_PAD8(v) asm volatile("s_nop 1" : "+v"(v[0]), "+v"(v[1]), "+v"(v[2]), "+v"(v[3]), "+v"(v[4]), "+v"(v[5]), "+v"(v[6]), "+v"(v[7]))

__device__ __forceinline__ void swap32(f2& a, f2& b) {
  const u2 au = __builtin_bit_cast(u2, a), bu = __builtin_bit_cast(u2, b);
  const unsigned ax = au[0], ay = au[1], bx = bu[0], by = bu[1];
  auto rx = __builtin_amdgcn_permlane32_swap(ax, bx, false, false);
  auto ry = __builtin_amdgcn_permlane32_swap(ay, by, false, false);
  a = __builtin_bit_cast(f2, u2{(unsigned)rx[0], (unsigned)ry[0]});
  b = __builtin_bit_cast(f2, u2{(unsigned)rx[1], (unsigned)ry[1]});
}
__device__ __forceinline__ void swap16(f2& a, f2& b) {
  const u2 au = __builtin_bit_cast(u2, a), bu = __builtin_bit_cast(u2, b);
  const unsigned ax = au[0], ay = au[1], bx = bu[0], by = bu[1];
  auto rx = __builtin_amdgcn_permlane16_swap(ax, bx, false, false);
  auto ry = __builtin_amdgcn_permlane16_swap(ay, by, false, false);
  a = __builtin_bit_cast(f2, u2{(unsigned)rx[0], (unsigned)ry[0]});
  b = __builtin_bit_cast(f2, u2{(unsigned)rx[1], (unsigned)ry[1]});
}
__device__ __forceinline__ void swap8(f2& a, f2& b) {
  const u2 au = __builtin_bit_cast(u2, a), bu = __builtin_bit_cast(u2, b);
  const int ax = au[0], ay = au[1], bx = bu[0], by = bu[1];
  const int nax = __builtin_amdgcn_update_dpp(ax, bx, 0x128, 0xf, 0xc, false);
  const int nbx = __builtin_amdgcn_update_dpp(bx, ax, 0x128, 0xf, 0x3, false);
  const int nay = __builtin_amdgcn_update_dpp(ay, by, 0x128, 0xf, 0xc, false);
  const int nby = __builtin_amdgcn_update_dpp(by, ay, 0x128, 0xf, 0x3, false);
  a = __builtin_bit_cast(f2, u2{(unsigned)nax, (unsigned)nay});
  b = __builtin_bit_cast(f2, u2{(unsigned)nbx, (unsigned)nby});
}

template <int CTRL> __device__ __forceinline__ float dppmov(float v) {
  return __builtin_bit_cast(float, __builtin_amdgcn_update_dpp(0, __builtin_bit_cast(int, v), CTRL, 0xf, 0xf, true));
}
__device__ __forceinline__ float wave_sum(float v) {
  v += dppmov<0xB1>(v);
  v += dppmov<0x4E>(v);
  v += dppmov<0x141>(v);
  v += dppmov<0x140>(v);
  {
    unsigned u = __builtin_bit_cast(unsigned, v);
    auto r = __builtin_amdgcn_permlane16_swap(u, u, false, false);
    v = __builtin_bit_cast(float, (unsigned)r[0]) + __builtin_bit_cast(float, (unsigned)r[1]);
  }
  {
    unsigned u = __builtin_bit_cast(unsigned, v);
    auto r = __builtin_amdgcn_permlane32_swap(u, u, false, false);
    v = __builtin_bit_cast(float, (unsigned)r[0]) + __builtin_bit_cast(float, (unsigned)r[1]);
  }
  return v;
}

__device__ __forceinline__ f4 mfma(u4 a, u4 b, f4 c) {
  return __builtin_amdgcn_mfma_f32_16x16x32_bf16(__builtin_bit_cast(s8, a), __builtin_bit_cast(s8, b), c, 0, 0, 0);
}
__device__ __forceinline__ float silu_f(float v) {
  return v * __builtin_amdgcn_rcpf(1.0f + __expf(-v));
}

__device__ __forceinline__ void wave_sum2(float& a, float& b) {
  a += dppmov<0xB1>(a);  b += dppmov<0xB1>(b);
  a += dppmov<0x4E>(a);  b += dppmov<0x4E>(b);
  a += dppmov<0x141>(a); b += dppmov<0x141>(b);
  a += dppmov<0x140>(a); b += dppmov<0x140>(b);
  unsigned ua = __builtin_bit_cast(unsigned, a), ub = __builtin_bit_cast(unsigned, b);
  auto r = __builtin_amdgcn_permlane16_swap(ua, ub, false, false);
  float c = __builtin_bit_cast(float, (unsigned)r[0]) + __builtin_bit_cast(float, (unsigned)r[1]);
  unsigned uc = __builtin_bit_cast(unsigned, c);
  auto r2 = __builtin_amdgcn_permlane32_swap(uc, uc, false, false);
  c = __builtin_bit_cast(float, (unsigned)r2[0]) + __builtin_bit_cast(float, (unsigned)r2[1]);
  a = __builtin_bit_cast(float, __builtin_amdgcn_readlane(__builtin_bit_cast(int, c), 0));
  b = __builtin_bit_cast(float, __builtin_amdgcn_readlane(__builtin_bit_cast(int, c), 16));
}
__device__ __forceinline__ void ln_mod(const f2 (&xv)[4], const unsigned (&sc1)[4], const unsigned (&sh)[4], f2 (&h)[4]) {
  const float x00 = xv[0].x;
  const float piv = __builtin_bit_cast(float, __builtin_amdgcn_readfirstlane(__builtin_bit_cast(int, x00)));
  f2 d[4];
#pragma unroll
  for (int i = 0; i < 4; ++i) d[i] = xv[i] - piv;
  const f2 s2 = (d[0] + d[1]) + (d[2] + d[3]);
  const f2 q2 = (d[0] * d[0] + d[1] * d[1]) + (d[2] * d[2] + d[3] * d[3]);
  float s1 = s2.x + s2.y, sq = q2.x + q2.y;
  wave_sum2(s1, sq);
  const float dm = s1 * (1.0f / 512.0f);
  const float var = __builtin_fmaxf(sq * (1.0f / 512.0f) - dm * dm, 0.0f);
  const float mbp = rb1(piv + dm) - piv;
  const float sd = rb1(__builtin_amdgcn_sqrtf(rb1(rb1(var) + 1e-5f)));
  const float rs = __builtin_amdgcn_rcpf(sd);
#pragma unroll
  for (int i = 0; i < 4; ++i) h[i] = ((d[i] - mbp) * rs) * unpk(sc1[i]) + unpk(sh[i]);
}

struct Tw256 { f2 a[3], b[3], c[3]; int x2a, x2b, x3a, x3b; };
__device__ __forceinline__ void make_tw256(Tw256& t, int l) {
#pragma unroll
  for (int r = 1; r < 4; ++r) {
    t.a[r - 1] = tw2048(l * r * 8);
    t.b[r - 1] = tw2048((l & 15) * r * 32);
    t.c[r - 1] = tw2048((l & 3) * r * 128);
  }
  const int d2 = l >> 4, d1 = (l >> 2) & 3, d0 = l & 3;
  t.x2a = 80 * d2 + 4 * d1 + d0;
  t.x2b = 80 * d2 + 20 * d1 + d0;
  t.x3a = 80 * d2 + 4 * d1 + d0;
  t.x3b = 80 * d2 + 4 * d1 + 17 * d0;
}
#define PIN_TW(t) do { \
  asm volatile("" : "+v"(t.a[0]), "+v"(t.a[1]), "+v"(t.a[2]), "+v"(t.b[0]), "+v"(t.b[1]), "+v"(t.b[2]), "+v"(t.c[0]), "+v"(t.c[1]), "+v"(t.c[2])); \
  asm volatile("" : "+v"(t.x2a), "+v"(t.x2b), "+v"(t.x3a), "+v"(t.x3b)); } while (0)

__device__ __forceinline__ void fft256_fwd(f2 (&v)[4], const Tw256& t, f2* lds) {
  bfly4<false>(v[0], v[1], v[2], v[3]);
  cmul3<false>(v[1], v[2], v[3], t.a[0], t.a[1], t.a[2]);
  XL_PAD4(v);
  swap32(v[0], v[2]); swap32(v[1], v[3]);
  swap16(v[0], v[1]); swap16(v[2], v[3]);
  bfly4<false>(v[0], v[1], v[2], v[3]);
  cmul3<false>(v[1], v[2], v[3], t.b[0], t.b[1], t.b[2]);
#pragma unroll
  for (int r = 0; r < 4; ++r) lds[t.x2a + 20 * r] = v[r];
  __builtin_amdgcn_wave_barrier();
#pragma unroll
  for (int r = 0; r < 4; ++r) v[r] = lds[t.x2b + 4 * r];
  __builtin_amdgcn_wave_barrier();
  bfly4<false>(v[0], v[1], v[2], v[3]);
  cmul3<false>(v[1], v[2], v[3], t.c[0], t.c[1], t.c[2]);
#pragma unroll
  for (int r = 0; r < 4; ++r) lds[t.x3a + 17 * r] = v[r];
  __builtin_amdgcn_wave_barrier();
#pragma unroll
  for (int r = 0; r < 4; ++r) v[r] = lds[t.x3b + r];
  __builtin_amdgcn_wave_barrier();
  bfly4<false>(v[0], v[1], v[2], v[3]);
}
template <class H> __device__ __forceinline__ void fft256_inv(f2 (&v)[4], const Tw256& t, f2* lds, H hook) {
  bfly4<true>(v[0], v[1], v[2], v[3]);
#pragma unroll
  for (int r = 0; r < 4; ++r) lds[t.x3b + r] = v[r];
  __builtin_amdgcn_wave_barrier();
#pragma unroll
  for (int r = 0; r < 4; ++r) v[r] = lds[t.x3a + 17 * r];
  __builtin_amdgcn_wave_barrier();
  hook(std::integral_constant<int, 0>{});
  cmul3<true>(v[1], v[2], v[3], t.c[0], t.c[1], t.c[2]);
  bfly4<true>(v[0], v[1], v[2], v[3]);
#pragma unroll
  for (int r = 0; r < 4; ++r) lds[t.x2b + 4 * r] = v[r];
  __builtin_amdgcn_wave_barrier();
#pragma unroll
  for (int r = 0; r < 4; ++r) v[r] = lds[t.x2a + 20 * r];
  __builtin_amdgcn_wave_barrier();
  hook(std::integral_constant<int, 1>{});
  cmul3<true>(v[1], v[2], v[3], t.b[0], t.b[1], t.b[2]);
  bfly4<true>(v[0], v[1], v[2], v[3]);
  XL_PAD4(v);
  swap16(v[0], v[1]); swap16(v[2], v[3]);
  swap32(v[0], v[2]); swap32(v[1], v[3]);
  hook(std::integral_constant<int, 2>{});
  cmul3<true>(v[1], v[2], v[3], t.a[0], t.a[1], t.a[2]);
  bfly4<true>(v[0], v[1], v[2], v[3]);
}
__device__ __forceinline__ int zpos(int k) { return k + (k >> 4); }

__device__ __host__ __forceinline__ int k2_k1(int tt, int kb) {
  int seq = (tt >> 6) * 8 + (tt & 7);
  int q = seq >> 2, k0 = seq & 3, ka = (tt >> 3) & 7;
  return 8 * (k0 + 4 * ka + 32 * kb) + q;
}

__device__ __forceinline__ void transpose8x8(const u4 (&in)[8], u4 (&out)[8]) {
#pragma unroll
  for (int c2 = 0; c2 < 4; ++c2) {
#pragma unroll
    for (int r2 = 0; r2 < 4; ++r2) {
      unsigned a = in[2 * r2][c2], b = in[2 * r2 + 1][c2];
      out[2 * c2][r2] = __builtin_amdgcn_perm(b, a, 0x05040100u);
      out[2 * c2 + 1][r2] = __builtin_amdgcn_perm(b, a, 0x07060302u);
    }
  }
}

#define PREP_AUX 0
#define T_AUX 0
constexpr int AUX_SC1 = 16;
constexpr int OOB = 0x40000000;
__device__ __forceinline__ __amdgpu_buffer_rsrc_t ws_rsrc(unsigned char* ws) {
  return __builtin_amdgcn_make_buffer_rsrc((void*)ws, 0, (int)WS_BYTES, 0x00020000);
}

__device__ __forceinline__ void prep_w1(const unsigned short* __restrict__ w1, __amdgpu_buffer_rsrc_t rs, int g) {
  const int no = g % 224, ko = g / 224;
  u4 in[8], out[8];
#pragma unroll
  for (int r = 0; r < 8; ++r) in[r] = *(const u4*)(w1 + (size_t)(ko * 8 + r) * FF + no * 8);
  transpose8x8(in, out);
  const int n0 = no * 8, c = n0 >> 5, nbl = (n0 >> 4) & 1, kb = ko >> 2, kg = ko & 3;
  const int dst = (int)OFF_W1F + (((c * 16 + kb) * 2 + nbl) * 64 + (n0 & 15) + 16 * kg) * 16;
#pragma unroll
  for (int cc = 0; cc < 8; ++cc) __builtin_amdgcn_raw_buffer_store_b128(out[cc], rs, dst + cc * 16, 0, PREP_AUX);
}
__device__ __forceinline__ void prep_w2(const unsigned short* __restrict__ w2, __amdgpu_buffer_rsrc_t rs, int g) {
  const int co = g & 63, g4 = (g >> 6) & 3, c = g >> 8;
  u4 in[8], out[8];
#pragma unroll
  for (int e = 0; e < 8; ++e) {
    int hid = 32 * c + 16 * (e >> 2) + 4 * g4 + (e & 3);
    in[e] = *(const u4*)(w2 + (size_t)hid * D + co * 8);
  }
  transpose8x8(in, out);
  const int jj = co >> 3, h = (co >> 2) & 1, i2 = co & 3;
  const int dst = (int)OFF_W2F + (((jj * 56 + c) * 4 + 2 * h) * 64 + 16 * g4 + 4 * i2) * 16;
#pragma unroll
  for (int cc = 0; cc < 8; ++cc)
    __builtin_amdgcn_raw_buffer_store_b128(out[cc], rs, dst + ((cc >> 2) * 64 + (cc & 3)) * 16, 0, PREP_AUX);
}

constexpr int K0_MOD = 96, K0_WF = 128, K0_MISC = 32;
constexpr int K0_BLOCKS = K0_MOD + K0_WF + K0_MISC;

__global__ __launch_bounds__(256) void k0_prep(const unsigned short* __restrict__ cvec,
                                               const unsigned short* __restrict__ wmod,
                                               const unsigned short* __restrict__ wr,
                                               const unsigned short* __restrict__ wi,
                                               unsigned char* __restrict__ ws) {
  __shared__ float red[4][32];
  const int tid = threadIdx.x;
  int b = blockIdx.x;
  STAMP(0, 0);
  if (b < K0_MOD) {
    const int co = tid & 3, rg = tid >> 2;
    u4 wv[8];
    unsigned short cs[8];
#pragma unroll
    for (int i = 0; i < 8; ++i) cs[i] = cvec[rg + 64 * i];
#pragma unroll
    for (int i = 0; i < 8; ++i)
      wv[i] = *(const u4*)(wmod + (size_t)(rg + 64 * i) * 3072 + b * 32 + co * 8);
    SCHED_FENCE();
    f2 acc[4];
#pragma unroll
    for (int e = 0; e < 4; ++e) acc[e] = f2{0.f, 0.f};
#pragma unroll
    for (int i = 0; i < 8; ++i) {
      const float cv = bflo(cs[i]);
      const float sv = rb1(silu_f(cv));
#pragma unroll
      for (int e2 = 0; e2 < 4; ++e2) acc[e2] += sv * unpk(wv[i][e2]);
    }
    STAMP(0, 2);
#pragma unroll
    for (int e2 = 0; e2 < 4; ++e2) {
      float v0 = acc[e2].x, v1 = acc[e2].y;
      v0 += dppmov<0x124>(v0); v1 += dppmov<0x124>(v1);
      v0 += dppmov<0x128>(v0); v1 += dppmov<0x128>(v1);
      unsigned u0 = __builtin_bit_cast(unsigned, v0), u1 = __builtin_bit_cast(unsigned, v1);
      auto r = __builtin_amdgcn_permlane16_swap(u0, u1, false, false);
      float c = __builtin_bit_cast(float, (unsigned)r[0]) + __builtin_bit_cast(float, (unsigned)r[1]);
      unsigned uc = __builtin_bit_cast(unsigned, c);
      auto r2 = __builtin_amdgcn_permlane32_swap(uc, uc, false, false);
      c = __builtin_bit_cast(float, (unsigned)r2[0]) + __builtin_bit_cast(float, (unsigned)r2[1]);
      if ((tid & 0x2c) == 0) red[tid >> 6][co * 8 + e2 * 2 + ((tid >> 4) & 1)] = c;
    }
    __syncthreads();
    if (tid < 32) {
      float s = rb1((red[0][tid] + red[1][tid]) + (red[2][tid] + red[3][tid]));
      const int col = b * 32 + tid, p = col >> 9, e = col & 511;
      if (p == 1 || p == 4) s = rb1(s + 1.0f);
      const unsigned short sb = (unsigned short)(__builtin_bit_cast(unsigned, s) >> 16);
      const int n = e >> 1, l = n & 63, i = n >> 6, hf = e & 1;
      if (p < 2) ((unsigned short*)(ws + OFF_MODK1))[((l * 4 + i) * 2 + p) * 2 + hf] = sb;
      else if (p < 5) ((unsigned short*)(ws + OFF_MODK3))[((l * 4 + i) * 3 + (p == 2 ? 0 : (p == 4 ? 1 : 2))) * 2 + hf] = sb;
      else ((unsigned short*)(ws + OFF_GB))[e] = sb;
    }
    STAMP(0, 1);
    return;
  }
  b -= K0_MOD;
  const __amdgpu_buffer_rsrc_t rs = ws_rsrc(ws);
  if (b < K0_WF) {
    const int tp = (b & 7) * 16 + (b >> 3);
    const int k2 = tid;
    unsigned short vr[16], vi[16];
#pragma unroll
    for (int e = 0; e < 16; ++e) {
      const int k1 = k2_k1(2 * tp + (e >> 3), e & 7);
      vr[e] = wr[k1 * DF + k2];
      vi[e] = wi[k1 * DF + k2];
    }
    SCHED_FENCE();
#pragma unroll
    for (int e4 = 0; e4 < 4; ++e4) {
      u4 o;
#pragma unroll
      for (int e = 0; e < 4; ++e) o[e] = (unsigned)vr[e4 * 4 + e] | ((unsigned)vi[e4 * 4 + e] << 16);
      __builtin_amdgcn_raw_buffer_store_b128(o, rs, (int)OFF_WF + (k2 * 512 + tp * 4 + e4) * 16, 0, PREP_AUX);
    }
    STAMP(0, 3);
    STAMP_DRAIN();
    STAMP(0, 5);
    return;
  }
  b -= K0_WF;
  if (tid < 64) {
    const int e = b * 64 + tid;
    const int tt = e & 255, kb = e >> 8;
    const int k1 = k2_k1(tt, kb), nk = (2048 - k1) & 2047;
    f2 a0 = f2{bflo(wr[k1 * DF]), bflo(wi[k1 * DF])};
    f2 a1 = f2{bflo(wr[nk * DF]), -bflo(wi[nk * DF])};
    f2 b0 = f2{bflo(wr[k1 * DF + 256]), bflo(wi[k1 * DF + 256])};
    f2 b1 = f2{bflo(wr[nk * DF + 256]), -bflo(wi[nk * DF + 256])};
    f2 w0h = (a0 + a1) * 0.5f, w256h = (b0 + b1) * 0.5f;
    f2 P = (w0h + w256h) * 0.5f, Q = (w0h - w256h) * 0.5f;
    ((f4*)(ws + OFF_PQ))[e] = f4{P.x, P.y, Q.x, Q.y};
    STAMP(0, 4);
  } else {
    ((u4*)ws)[b * 192 + (tid - 64)] = u4{0u, 0u, 0u, 0u};
  }
}

constexpr int ROWP = 322;

__global__ __launch_bounds__(640) void k1_rows(const unsigned* __restrict__ x32, const unsigned short* __restrict__ w1,
                                               const unsigned short* __restrict__ w2, unsigned char* __restrict__ ws) {
  __shared__ __attribute__((aligned(16))) f2 lds[8 * ROWP];
  const int j = blockIdx.x;
  const int tid = threadIdx.x, w = __builtin_amdgcn_readfirstlane(tid >> 6), l = tid & 63;
  const __amdgpu_buffer_rsrc_t rs = ws_rsrc(ws);
  STAMP(1, 0);
  if (w >= 8) {
    if (l < 56) {
      if (w == 8) prep_w1(w1, rs, j * 56 + l);
      else prep_w2(w2, rs, j * 56 + l);
    }
    STAMPT(1, 5, 512);
    STAMP_DRAIN();
    STAMPT(1, 6, 512);
    return;
  }
  const int s = j + 256 * w;
  unsigned xr[4], sc1[4], sh[4];
#pragma unroll
  for (int i = 0; i < 4; ++i) xr[i] = x32[(size_t)s * 256 + l + 64 * i];
  {
    const u4* mp = (const u4*)(ws + OFF_MODK1) + l * 2;
    const u4 m0 = mp[0], m1 = mp[1];
    sh[0] = m0[0]; sc1[0] = m0[1]; sh[1] = m0[2]; sc1[1] = m0[3];
    sh[2] = m1[0]; sc1[2] = m1[1]; sh[3] = m1[2]; sc1[3] = m1[3];
  }
  SCHED_FENCE();
  Tw256 tw;
  make_tw256(tw, l);
  tw.x2a += w * ROWP; tw.x2b += w * ROWP; tw.x3a += w * ROWP; tw.x3b += w * ROWP;
  const int k2 = tid & 255;
  f2 t512 = tw2048(4 * k2 + 512);
  f2 twq[7];
#pragma unroll
  for (int q = 1; q < 8; ++q) twq[q - 1] = tw2048(j * q);
  int zw = w * ROWP + (l >> 4) + 4 * ((l >> 2) & 3) + 17 * (l & 3);
  int za = zpos(k2), zb = zpos((256 - k2) & 255);
  int to = (int)OFF_T + (j * 256 + k2) * 64;
  asm volatile("" : "+v"(twq[0]), "+v"(twq[1]), "+v"(twq[2]), "+v"(twq[3]), "+v"(twq[4]), "+v"(twq[5]), "+v"(twq[6]), "+v"(t512));
  asm volatile("" : "+v"(zw), "+v"(za), "+v"(zb), "+v"(to));
  PIN_TW(tw);
  SCHED_FENCE();
  f2 xv[4], v[4];
#pragma unroll
  for (int i = 0; i < 4; ++i) xv[i] = unpk(xr[i]);
  ln_mod(xv, sc1, sh, v);
  STAMP(1, 1);
  fft256_fwd(v, tw, lds);
  STAMP(1, 2);
#pragma unroll
  for (int r = 0; r < 4; ++r) lds[zw + 68 * r] = v[r];
  __syncthreads();
  if (w < 4) {
    f2 a[8], z[8], zr[8];
#pragma unroll
    for (int m = 0; m < 8; ++m) { z[m] = lds[m * ROWP + za]; zr[m] = lds[m * ROWP + zb]; }
#pragma unroll
    for (int m = 0; m < 8; m += 2) herm2(a[m], a[m + 1], z[m], zr[m], t512, z[m + 1], zr[m + 1], t512);
    if (w == 0) {
#pragma unroll
      for (int m = 0; m < 8; ++m) a[m].y = k2 == 0 ? 2.0f * (z[m].x - z[m].y) : a[m].y;
    }
    bfly8<false>(a);
    cmul7<false>(a, twq);
#pragma unroll
    for (int q = 0; q < 4; ++q)
      __builtin_amdgcn_raw_buffer_store_b128(__builtin_bit_cast(u4, f4{a[2 * q].x, a[2 * q].y, a[2 * q + 1].x, a[2 * q + 1].y}), rs,
                                             to + q * 16, 0, T_AUX);
    STAMP(1, 3);
    STAMP_DRAIN();
    STAMP(1, 4);
  }
}

constexpr int SEQP = 64 + 1;
__global__ __launch_bounds__(256) void k2_cols(unsigned char* __restrict__ ws) {
  __shared__ __attribute__((aligned(16))) f2 lds[2048 + 64];
  const int b = blockIdx.x;
  const int t = threadIdx.x, l = t & 63;
  const __amdgpu_buffer_rsrc_t rs = ws_rsrc(ws);
  STAMP(2, 0);
  const int k2 = (b & 7) * 32 + (b >> 3);
  const int jr = t >> 2, qp = t & 3;
  const int co = (int)OFF_T + (jr * 1024 + k2 * 4 + qp) * 16;
  u4 cin[4];
#pragma unroll
  for (int i = 0; i < 4; ++i) cin[i] = __builtin_amdgcn_raw_buffer_load_b128(rs, co + i * (1 << 20), 0, 0);
  u4 wq[8];
  {
    const int wo = k2 != 0 ? (int)OFF_WF + (k2 * 256 + t) * 32 : (int)OFF_PQ + t * 16;
    const int ws1 = k2 != 0 ? 16 : 4096;
    wq[0] = __builtin_amdgcn_raw_buffer_load_b128(rs, wo, 0, 0);
    wq[1] = __builtin_amdgcn_raw_buffer_load_b128(rs, wo + ws1, 0, 0);
    const int po = k2 != 0 ? OOB : wo;
#pragma unroll
    for (int kb = 2; kb < 8; ++kb) wq[kb] = __builtin_amdgcn_raw_buffer_load_b128(rs, po + kb * 4096, 0, 0);
  }
  SCHED_FENCE();
  f2 twA[3], twB[7];
#pragma unroll
  for (int r = 1; r < 4; ++r) twA[r - 1] = tw2048(jr * r * 8);
  const int bb = l >> 3;
#pragma unroll
  for (int r = 1; r < 8; ++r) twB[r - 1] = tw2048(bb * r * 32);
  int pa = (8 * qp) * SEQP + jr;
  int pb = ((t >> 6) * 8 + (t & 7)) * SEQP + bb;
  asm volatile("" : "+v"(twB[0]), "+v"(twB[1]), "+v"(twB[2]), "+v"(twB[3]), "+v"(twB[4]), "+v"(twB[5]), "+v"(twB[6]), "+v"(twA[0]), "+v"(twA[1]), "+v"(twA[2]));
  asm volatile("" : "+v"(pa), "+v"(pb));
  SCHED_FENCE();
  f2 a0[4], a1[4];
#pragma unroll
  for (int i = 0; i < 4; ++i) { const f4 c = __builtin_bit_cast(f4, cin[i]); a0[i] = c.xy; a1[i] = c.zw; }
  bfly4<false>(a0[0], a0[1], a0[2], a0[3]);
  bfly4<false>(a1[0], a1[1], a1[2], a1[3]);
  cmul3<false>(a0[1], a0[2], a0[3], twA[0], twA[1], twA[2]);
  cmul3<false>(a1[1], a1[2], a1[3], twA[0], twA[1], twA[2]);
#pragma unroll
  for (int r = 0; r < 4; ++r) {
    lds[pa + r * SEQP] = a0[r];
    lds[pa + (4 + r) * SEQP] = a1[r];
  }
  __syncthreads();
  STAMP(2, 1);
  f2 v[8];
#pragma unroll
  for (int a = 0; a < 8; ++a) v[a] = lds[pb + 8 * a];
  bfly8<false>(v);
  cmul7<false>(v, twB);
  XL_PAD8(v);
#pragma unroll
  for (int i = 0; i < 4; ++i) swap32(v[i], v[i + 4]);
  swap16(v[0], v[2]); swap16(v[1], v[3]); swap16(v[4], v[6]); swap16(v[5], v[7]);
#pragma unroll
  for (int i = 0; i < 8; i += 2) swap8(v[i], v[i + 1]);
  bfly8<false>(v);
  if (k2 != 0) {
    cmul4<false>(v[0], v[1], v[2], v[3], unpk(wq[0][0]), unpk(wq[0][1]), unpk(wq[0][2]), unpk(wq[0][3]));
    cmul4<false>(v[4], v[5], v[6], v[7], unpk(wq[1][0]), unpk(wq[1][1]), unpk(wq[1][2]), unpk(wq[1][3]));
  } else {
    __syncthreads();
#pragma unroll
    for (int kb = 0; kb < 8; ++kb) lds[k2_k1(t, kb)] = v[kb];
    __syncthreads();
#pragma unroll
    for (int kb = 0; kb < 8; ++kb) {
      f2 fn = lds[(2048 - k2_k1(t, kb)) & 2047];
      fn.y = -fn.y;
      const f4 c = __builtin_bit_cast(f4, wq[kb]);
      v[kb] = cfma(fn, c.zw, cmul(v[kb], c.xy));
    }
  }
  bfly8<true>(v);
  XL_PAD8(v);
#pragma unroll
  for (int i = 0; i < 8; i += 2) swap8(v[i], v[i + 1]);
  swap16(v[0], v[2]); swap16(v[1], v[3]); swap16(v[4], v[6]); swap16(v[5], v[7]);
#pragma unroll
  for (int i = 0; i < 4; ++i) swap32(v[i], v[i + 4]);
  cmul7<true>(v, twB);
  bfly8<true>(v);
  if (k2 == 0) __syncthreads();
#pragma unroll
  for (int a = 0; a < 8; ++a) lds[pb + 8 * a] = v[a];
  __syncthreads();
#pragma unroll
  for (int r = 0; r < 4; ++r) {
    a0[r] = lds[pa + r * SEQP];
    a1[r] = lds[pa + (4 + r) * SEQP];
  }
  cmul3<true>(a0[1], a0[2], a0[3], twA[0], twA[1], twA[2]);
  cmul3<true>(a1[1], a1[2], a1[3], twA[0], twA[1], twA[2]);
  bfly4<true>(a0[0], a0[1], a0[2], a0[3]);
  bfly4<true>(a1[0], a1[1], a1[2], a1[3]);
#pragma unroll
  for (int i = 0; i < 4; ++i)
    __builtin_amdgcn_raw_buffer_store_b128(__builtin_bit_cast(u4, f4{a0[i].x, a0[i].y, a1[i].x, a1[i].y}), rs, co + i * (1 << 20), 0, T_AUX);
  STAMP(2, 2);
  STAMP_DRAIN();
  STAMP(2, 3);
}

constexpr unsigned SPIN_CAP = 1u << 20;
constexpr int L_H2 = 0;
constexpr int L_STG = 65536;
constexpr int L_RED = 0;
constexpr int LDS3_BYTES = 131072;
#define W1A 10
#define W1S 3
#define W2A 6
#define W2B 14
#define W1LD(i) wv[i] = __builtin_amdgcn_raw_buffer_load_b128(rs, wo + (i) * 1024, 0, 0)
#define W1SPRINKLE(k) do { SCHED_FENCE(); _Pragma("unroll") for (int i_ = 0; i_ < W1S; ++i_) W1LD(W1A + (k) * W1S + i_); SCHED_FENCE(); } while (0)

__device__ __forceinline__ void poll_flags(gu32* f, int n, int l) {
  for (unsigned s = 0; s < SPIN_CAP; ++s) {
    const unsigned v = l < n ? __hip_atomic_load(f + l, RLX_AGENT) : 1u;
    if (__all(v != 0u)) break;
    __builtin_amdgcn_s_sleep(1);
  }
  __builtin_amdgcn_fence(__ATOMIC_ACQUIRE, "wavefront");
}

__global__ __launch_bounds__(512, 2) void k3_fused(const unsigned* __restrict__ x32, unsigned char* __restrict__ ws,
                                                   unsigned short* __restrict__ out) {
  extern __shared__ __attribute__((aligned(16))) unsigned char smem[];
  const int b = blockIdx.x;
  const int g = (b & 7) + 8 * (b >> 6), jj = (b >> 3) & 7;
  const int j = 8 * g + jj;
  const int tid = threadIdx.x, w = __builtin_amdgcn_readfirstlane(tid >> 6), l = tid & 63;
  const __amdgpu_buffer_rsrc_t rs = ws_rsrc(ws);
  STAMP(3, 0);
  const int s = j + 256 * w;
  u4 tin[4];
  {
    const int to = w < 4 ? (int)OFF_T + (j * 256 + tid) * 64 : OOB;
#pragma unroll
    for (int q = 0; q < 4; ++q) tin[q] = __builtin_amdgcn_raw_buffer_load_b128(rs, to + q * 16, 0, 0);
  }
  unsigned xr[4];
#pragma unroll
  for (int i = 0; i < 4; ++i) xr[i] = x32[(size_t)s * 256 + l + 64 * i];
  u4 mk[3];
#pragma unroll
  for (int i = 0; i < 3; ++i) mk[i] = ((const u4*)(ws + OFF_MODK3))[l * 3 + i];
  SCHED_FENCE();
  const int kl = (l >> 4) + 4 * ((l >> 2) & 3) + 16 * (l & 3);
  Tw256 tw;
  make_tw256(tw, l);
  tw.x2a += w * ROWP; tw.x2b += w * ROWP; tw.x3a += w * ROWP; tw.x3b += w * ROWP;
  f2 t512[4];
  int zk[4], zn[4];
#pragma unroll
  for (int i = 0; i < 4; ++i) {
    t512[i] = tw2048(-4 * (kl + 64 * i) - 512);
    zk[i] = w * ROWP + zpos(kl + 64 * i);
    zn[i] = w * ROWP + zpos((256 - kl - 64 * i) & 255);
  }
  f2 twq[7];
#pragma unroll
  for (int q = 1; q < 8; ++q) twq[q - 1] = tw2048(j * q);
  int zc = zpos(tid & 255);
  asm volatile("" : "+v"(twq[0]), "+v"(twq[1]), "+v"(twq[2]), "+v"(twq[3]), "+v"(twq[4]), "+v"(twq[5]), "+v"(twq[6]));
  asm volatile("" : "+v"(t512[0]), "+v"(t512[1]), "+v"(t512[2]), "+v"(t512[3]), "+v"(zc));
  asm volatile("" : "+v"(zk[0]), "+v"(zk[1]), "+v"(zk[2]), "+v"(zk[3]), "+v"(zn[0]), "+v"(zn[1]), "+v"(zn[2]), "+v"(zn[3]));
  PIN_TW(tw);
  SCHED_FENCE();
  u4 wv[32];
  const int wo = w < 7 ? (int)OFF_W1F + ((7 * jj + w) * 2048 + l) * 16 : OOB;
#pragma unroll
  for (int i = 0; i < W1A; ++i) W1LD(i);
  SCHED_FENCE();
  f2* lds = (f2*)(smem + L_STG);
  if (w < 4) {
    f2 a[8];
#pragma unroll
    for (int q = 0; q < 4; ++q) { const f4 c = __builtin_bit_cast(f4, tin[q]); a[2 * q] = c.xy; a[2 * q + 1] = c.zw; }
    cmul7<true>(a, twq);
    bfly8<true>(a);
#pragma unroll
    for (int m = 0; m < 8; ++m) lds[m * ROWP + zc] = a[m];
  }
  __syncthreads();
  STAMP(3, 1);
  f2 v[4];
  {
    f2 xk[4], xn[4];
#pragma unroll
    for (int r = 0; r < 4; ++r) { xk[r] = lds[zk[r]]; xn[r] = lds[zn[r]]; }
    W1SPRINKLE(0);
    herm2(v[0], v[1], xk[0], xn[0], t512[0], xk[1], xn[1], t512[1]);
    herm2(v[2], v[3], xk[2], xn[2], t512[2], xk[3], xn[3], t512[3]);
    if (l == 0) v[0] = f2{xk[0].x + xk[0].y, xk[0].x - xk[0].y};
  }
  __builtin_amdgcn_wave_barrier();
  fft256_inv(v, tw, lds, [&](auto k) { W1SPRINKLE(1 + decltype(k)::value); });
  W1SPRINKLE(4);
  {
    f2 x1[4], h2[4];
    unsigned sc1[4], sh[4], x1p[4];
#pragma unroll
    for (int i = 0; i < 4; ++i) {
      const unsigned ga = mk[(3 * i) >> 2][(3 * i) & 3];
      sc1[i] = mk[(3 * i + 1) >> 2][(3 * i + 1) & 3];
      sh[i] = mk[(3 * i + 2) >> 2][(3 * i + 2) & 3];
      x1p[i] = pk(unpk(xr[i]) + unpk(ga) * (v[i] * 4.76837158203125e-07f));
      x1[i] = unpk(x1p[i]);
    }
    ln_mod(x1, sc1, sh, h2);
    unsigned* st = (unsigned*)(lds + w * ROWP);
#pragma unroll
    for (int i = 0; i < 4; ++i) {
      st[l + 64 * i] = x1p[i];
      st[256 + l + 64 * i] = pk(h2[i]);
    }
  }
  __syncthreads();
  STAMP(3, 2);
  gu32* f1 = (gu32*)(ws + OFF_F1) + (size_t)g * 8 * 32;
  if (w == 7) {
    __builtin_amdgcn_s_setprio(3);
    const int tb = jj >> 1;
    u4 ho[8], xo[8];
#pragma unroll
    for (int r = 0; r < 8; ++r) {
      const int ch = 8 * r + (l >> 3), m = l & 7;
      ho[r] = *(const u4*)((const unsigned*)(lds + m * ROWP) + 256 + 4 * ch);
      xo[r] = *(const u4*)((const unsigned*)(lds + r * ROWP) + 4 * l);
    }
    const int ha = (int)OFF_H2F + ((((g * 4 + tb) * 16 + (l >> 5)) * 64) + 8 * (jj & 1) + (l & 7) + 16 * ((l >> 3) & 3)) * 16;
    const int xa = (int)OFF_X1 + ((64 * g + 8 * jj) * 64 + l) * 16;
#pragma unroll
    for (int r = 0; r < 8; ++r) __builtin_amdgcn_raw_buffer_store_b128(ho[r], rs, ha + r * 2048, 0, AUX_SC1);
#pragma unroll
    for (int r = 0; r < 8; ++r) __builtin_amdgcn_raw_buffer_store_b128(xo[r], rs, xa + r * 1024, 0, AUX_SC1);
    SCHED_FENCE();
    __builtin_amdgcn_s_barrier();
    asm volatile("s_waitcnt vmcnt(0)" ::: "memory");
    if (l == 0) __hip_atomic_store(f1 + jj * 32, 1u, RLX_AGENT);
    __builtin_amdgcn_s_setprio(0);
    STAMPT(3, 3, 448);
  } else {
    __builtin_amdgcn_s_barrier();
  }
  SCHED_FENCE();
#pragma unroll
  for (int i = W1A + 5 * W1S; i < 32; ++i) W1LD(i);
  u4 uv[28];
  const int uo = (int)OFF_W2F + ((jj * 56 + 7 * w) * 256 + l) * 16;
#pragma unroll
  for (int i = 0; i < W2A; ++i) uv[i] = __builtin_amdgcn_raw_buffer_load_b128(rs, uo + i * 1024, 0, 0);
  SCHED_FENCE();
  poll_flags(f1 + w * 32, 1, l);
  STAMP(3, 4);
  {
    u4 hv[8];
    const int tb = w >> 1;
    const int hoff = ((tb * 16 + (l >> 5)) * 64 + 8 * (w & 1) + (l & 7) + 16 * ((l >> 3) & 3)) * 16;
#pragma unroll
    for (int i = 0; i < 8; ++i)
      hv[i] = __builtin_amdgcn_raw_buffer_load_b128(rs, (int)OFF_H2F + g * 65536 + hoff + i * 2048, 0, AUX_SC1);
    SCHED_FENCE();
#pragma unroll
    for (int i = 0; i < 8; ++i) *(u4*)(smem + L_H2 + hoff + i * 2048) = hv[i];
  }
  __syncthreads();
  STAMP(3, 5);
  const int hcol = w & 1, tbf = w >> 1;
  const int tok = 16 * tbf + (l & 15);
  const int col = 64 * jj + 32 * hcol + 8 * (l >> 4);
  const u4 x1v = __builtin_amdgcn_raw_buffer_load_b128(rs, (int)OFF_X1 + ((64 * g + tok) * 64 + (col >> 3)) * 16, 0, AUX_SC1);
  const u4 gbv = __builtin_amdgcn_raw_buffer_load_b128(rs, (int)OFF_GB + col * 2, 0, 0);
  SCHED_FENCE();
  gu32* f2w = (gu32*)(ws + OFF_F2) + (size_t)(g * 8) * 64;
  if (w < 7) {
    const u4* hs = (const u4*)(smem + L_H2);
    const int afo = (int)OFF_AF + (((g * 8 + jj) * 28 + w) * 64 + l) * 16;
    u4 hb[3][2];
#pragma unroll
    for (int st = 0; st < 2; ++st)
#pragma unroll
      for (int t2 = 0; t2 < 2; ++t2) hb[st][t2] = hs[(t2 * 16 + st) * 64 + l];
#pragma unroll
    for (int hf = 0; hf < 2; ++hf) {
      f4 acc[2][2];
#pragma unroll
      for (int n = 0; n < 2; ++n)
#pragma unroll
        for (int t2 = 0; t2 < 2; ++t2) acc[n][t2] = f4{0.f, 0.f, 0.f, 0.f};
#pragma unroll
      for (int kb = 0; kb < 16; ++kb) {
        const int st = hf * 16 + kb;
        if (st + 2 < 32) {
#pragma unroll
          for (int t2 = 0; t2 < 2; ++t2) hb[(st + 2) % 3][t2] = hs[((2 * ((st + 2) >> 4) + t2) * 16 + ((st + 2) & 15)) * 64 + l];
        }
        {
          const int fi = hf == 0 ? (kb < W2B - W2A ? W2A + kb : 99) : W2B + kb;
          if (fi < 28) uv[fi] = __builtin_amdgcn_raw_buffer_load_b128(rs, uo + fi * 1024, 0, 0);
        }
        SCHED_FENCE();
#pragma unroll
        for (int t2 = 0; t2 < 2; ++t2) {
          acc[0][t2] = mfma(wv[2 * kb], hb[st % 3][t2], acc[0][t2]);
          acc[1][t2] = mfma(wv[2 * kb + 1], hb[st % 3][t2], acc[1][t2]);
        }
        SCHED_FENCE();
        if (hf == 1 && kb == 7) {
          asm volatile("s_waitcnt vmcnt(8)" ::: "memory");
          if (l == 0) __hip_atomic_store(f2w + (jj * 2 + 0) * 32 + w, 1u, RLX_AGENT);
          SCHED_FENCE();
        }
      }
#pragma unroll
      for (int t2 = 0; t2 < 2; ++t2) {
        u4 o;
#pragma unroll
        for (int n = 0; n < 2; ++n) {
          const f2 p0 = acc[n][t2].xy, p1 = acc[n][t2].zw;
          o[2 * n] = pk(f2{silu_f(p0.x), silu_f(p0.y)});
          o[2 * n + 1] = pk(f2{silu_f(p1.x), silu_f(p1.y)});
        }
        __builtin_amdgcn_raw_buffer_store_b128(o, rs, afo + (2 * hf + t2) * 7 * 1024, 0, AUX_SC1);
      }
      SCHED_FENCE();
    }
    asm volatile("s_waitcnt vmcnt(0)" ::: "memory");
    if (l == 0) __hip_atomic_store(f2w + (jj * 2 + 1) * 32 + w, 1u, RLX_AGENT);
  } else {
#pragma unroll
    for (int i = W2A; i < 28; ++i) uv[i] = __builtin_amdgcn_raw_buffer_load_b128(rs, uo + i * 1024, 0, 0);
  }
  SCHED_FENCE();
  STAMP(3, 6);
  const int aro = (int)OFF_AF + ((g * 8 + w) * 28 * 64 + l) * 16;
  u4 av[28];
  poll_flags(f2w + (w * 2 + 0) * 32, 7, l);
  STAMP(3, 7);
#pragma unroll
  for (int i = 0; i < 14; ++i) av[i] = __builtin_amdgcn_raw_buffer_load_b128(rs, aro + i * 1024, 0, AUX_SC1);
  SCHED_FENCE();
  poll_flags(f2w + (w * 2 + 1) * 32, 7, l);
  STAMP(3, 8);
#pragma unroll
  for (int i = 14; i < 21; ++i) av[i] = __builtin_amdgcn_raw_buffer_load_b128(rs, aro + i * 1024, 0, AUX_SC1);
  SCHED_FENCE();
  __syncthreads();
  f4* red = (f4*)(smem + L_RED);
#pragma unroll
  for (int tb = 0; tb < 4; ++tb) {
    f4 acc[4];
#pragma unroll
    for (int nb = 0; nb < 4; ++nb) acc[nb] = f4{0.f, 0.f, 0.f, 0.f};
#pragma unroll
    for (int c7 = 0; c7 < 7; ++c7)
#pragma unroll
      for (int nb = 0; nb < 4; ++nb) acc[nb] = mfma(uv[c7 * 4 + nb], av[tb * 7 + c7], acc[nb]);
    if (tb == 0) {
      SCHED_FENCE();
#pragma unroll
      for (int i = 21; i < 28; ++i) av[i] = __builtin_amdgcn_raw_buffer_load_b128(rs, aro + i * 1024, 0, AUX_SC1);
      SCHED_FENCE();
    }
#pragma unroll
    for (int nb = 0; nb < 4; ++nb) red[(w * 16 + tb * 4 + nb) * 64 + l] = acc[nb];
  }
  STAMP(3, 9);
  __syncthreads();
  f4 s0 = f4{0.f, 0.f, 0.f, 0.f}, s1 = s0;
#pragma unroll
  for (int ww = 0; ww < 8; ++ww) {
    s0 += red[(ww * 16 + tbf * 4 + 2 * hcol) * 64 + l];
    s1 += red[(ww * 16 + tbf * 4 + 2 * hcol + 1) * 64 + l];
  }
  u4 o;
  o[0] = pk(unpk(x1v[0]) + unpk(gbv[0]) * s0.xy);
  o[1] = pk(unpk(x1v[1]) + unpk(gbv[1]) * s0.zw);
  o[2] = pk(unpk(x1v[2]) + unpk(gbv[2]) * s1.xy);
  o[3] = pk(unpk(x1v[3]) + unpk(gbv[3]) * s1.zw);
  const int srow = 8 * g + (tok >> 3) + 256 * (tok & 7);
  *(u4*)(out + (size_t)srow * D + col) = o;
  STAMP(3, 10);
  STAMP_DRAIN();
  STAMP(3, 11);
}

}

extern "C" void kernel_launch(void* const* d_in, const int* in_sizes, int n_in, void* d_out, int out_size,
                              void* d_ws, size_t ws_size, hipStream_t stream) {
  const unsigned short* x = (const unsigned short*)d_in[0];
  const unsigned short* wr = (const unsigned short*)d_in[1];
  const unsigned short* wi = (const unsigned short*)d_in[2];
  const unsigned short* w1 = (const unsigned short*)d_in[4];
  const unsigned short* w2 = (const unsigned short*)d_in[5];
  const unsigned short* cv = (const unsigned short*)d_in[6];
  const unsigned short* wm = (const unsigned short*)d_in[7];
  unsigned char* ws = (unsigned char*)d_ws;
  (void)hipFuncSetAttribute((const void*)k3_fused, hipFuncAttributeMaxDynamicSharedMemorySize, LDS3_BYTES);
  k0_prep<<<K0_BLOCKS, 256, 0, stream>>>(cv, wm, wr, wi, ws);
  k1_rows<<<256, 640, 0, stream>>>((const unsigned*)x, w1, w2, ws);
  k2_cols<<<256, 256, 0, stream>>>(ws);
  k3_fused<<<256, 512, LDS3_BYTES, stream>>>((const unsigned*)x, ws, (unsigned short*)d_out);
}
```

```cpp
#include <hip/hip_runtime.h>
#include <hip/hip_bf16.h>
#include <stdint.h>
#include <type_traits>

namespace {

typedef float f2 __attribute__((ext_vector_type(2)));
typedef float f4 __attribute__((ext_vector_type(4)));
typedef unsigned u2 __attribute__((ext_vector_type(2)));
typedef unsigned u4 __attribute__((ext_vector_type(4)));
typedef short s8 __attribute__((ext_vector_type(8)));
typedef __bf16 bf2 __attribute__((ext_vector_type(2)));
#define GAS __attribute__((address_space(1)))
typedef GAS unsigned gu32;
#define RLX_AGENT __ATOMIC_RELAXED, __HIP_MEMORY_SCOPE_AGENT
#define SCHED_FENCE() __builtin_amdgcn_sched_barrier(0)

constexpr int S = 2048, D = 512, DF = 257, FF = 1792;

constexpr size_t OFF_F2    = 0;
constexpr size_t OFF_MODK1 = 262144;
constexpr size_t OFF_MODK3 = OFF_MODK1 + 2048;
constexpr size_t OFF_GB    = OFF_MODK3 + 3072;
constexpr size_t OFF_PQ    = OFF_MODK1 + 8192;
constexpr size_t OFF_WF    = OFF_PQ + 32768;
constexpr size_t OFF_T     = OFF_WF + (size_t)256 * 2048 * 4;
constexpr size_t OFF_H2F   = OFF_T + (size_t)256 * 2048 * 8;
constexpr size_t OFF_X1    = OFF_H2F + (size_t)S * D * 2;
constexpr size_t OFF_AF    = OFF_X1 + (size_t)S * D * 2;
constexpr size_t OFF_W1F   = OFF_AF + (size_t)S * FF * 2;
constexpr size_t OFF_W2F   = OFF_W1F + (size_t)D * FF * 2;
constexpr size_t WS_BYTES  = OFF_W2F + (size_t)D * FF * 2;

#define STAMP(kid, i) do {} while (0)
#define STAMPT(kid, i, thr) do {} while (0)
#define STAMP_DRAIN() do {} while (0)

__device__ __forceinline__ float bflo(unsigned u) { return __builtin_bit_cast(float, u << 16); }
__device__ __forceinline__ float bfhi(unsigned u) { return __builtin_bit_cast(float, u & 0xffff0000u); }
__device__ __forceinline__ f2 unpk(unsigned u) { return f2{bflo(u), bfhi(u)}; }
__device__ __forceinline__ unsigned pk(f2 v) {
  bf2 b = __builtin_convertvector(v, bf2);
  return __builtin_bit_cast(unsigned, b);
}
__device__ __forceinline__ f2 rb2(f2 v) { return unpk(pk(v)); }
__device__ __forceinline__ float rb1(float v) { return bflo(pk(f2{v, v})); }

__device__ __forceinline__ f2 tw2048(int t) {
  const float f = (float)t * (1.0f / 2048.0f);
  return f2{__builtin_amdgcn_cosf(f), -__builtin_amdgcn_sinf(f)};
}

__device__ __forceinline__ f2 cmul(f2 a, f2 w) {
  f2 t, d;
  asm("v_pk_mul_f32 %0, %1, %2 op_sel:[1,1] op_sel_hi:[1,0] neg_lo:[0,1]" : "=v"(t) : "v"(a), "v"(w));
  asm("v_pk_fma_f32 %0, %1, %2, %3 op_sel:[0,0,0] op_sel_hi:[0,1,1]" : "=v"(d) : "v"(a), "v"(w), "v"(t));
  return d;
}
__device__ __forceinline__ f2 cmulc(f2 a, f2 w) {
  f2 t, d;
  asm("v_pk_mul_f32 %0, %1, %2 op_sel:[0,0] op_sel_hi:[1,0]" : "=v"(t) : "v"(a), "v"(w));
  asm("v_pk_fma_f32 %0, %1, %2, %3 op_sel:[1,1,0] op_sel_hi:[0,1,1] neg_hi:[1,0,0]" : "=v"(d) : "v"(a), "v"(w), "v"(t));
  return d;
}
template <bool CONJ> __device__ __forceinline__ void cmul3(f2& a0, f2& a1, f2& a2, f2 w0, f2 w1, f2 w2) {
  f2 t0, t1, t2;
  if (!CONJ) {
    asm("v_pk_mul_f32 %3, %0, %6 op_sel:[1,1] op_sel_hi:[1,0] neg_lo:[0,1]\n\t"
        "v_pk_mul_f32 %4, %1, %7 op_sel:[1,1] op_sel_hi:[1,0] neg_lo:[0,1]\n\t"
        "v_pk_mul_f32 %5, %2, %8 op_sel:[1,1] op_sel_hi:[1,0] neg_lo:[0,1]\n\t"
        "v_pk_fma_f32 %0, %0, %6, %3 op_sel:[0,0,0] op_sel_hi:[0,1,1]\n\t"
        "v_pk_fma_f32 %1, %1, %7, %4 op_sel:[0,0,0] op_sel_hi:[0,1,1]\n\t"
        "v_pk_fma_f32 %2, %2, %8, %5 op_sel:[0,0,0] op_sel_hi:[0,1,1]"
        : "+v"(a0), "+v"(a1), "+v"(a2), "=&v"(t0), "=&v"(t1), "=&v"(t2) : "v"(w0), "v"(w1), "v"(w2));
  } else {
    asm("v_pk_mul_f32 %3, %0, %6 op_sel:[0,0] op_sel_hi:[1,0]\n\t"
        "v_pk_mul_f32 %4, %1, %7 op_sel:[0,0] op_sel_hi:[1,0]\n\t"
        "v_pk_mul_f32 %5, %2, %8 op_sel:[0,0] op_sel_hi:[1,0]\n\t"
        "v_pk_fma_f32 %0, %0, %6, %3 op_sel:[1,1,0] op_sel_hi:[0,1,1] neg_hi:[1,0,0]\n\t"
        "v_pk_fma_f32 %1, %1, %7, %4 op_sel:[1,1,0] op_sel_hi:[0,1,1] neg_hi:[1,0,0]\n\t"
        "v_pk_fma_f32 %2, %2, %8, %5 op_sel:[1,1,0] op_sel_hi:[0,1,1] neg_hi:[1,0,0]"
        : "+v"(a0), "+v"(a1), "+v"(a2), "=&v"(t0), "=&v"(t1), "=&v"(t2) : "v"(w0), "v"(w1), "v"(w2));
  }
}
template <bool CONJ> __device__ __forceinline__ void cmul4(f2& a0, f2& a1, f2& a2, f2& a3, f2 w0, f2 w1, f2 w2, f2 w3) {
  f2 t0, t1, t2, t3;
  if (!CONJ) {
    asm("v_pk_mul_f32 %4, %0, %8 op_sel:[1,1] op_sel_hi:[1,0] neg_lo:[0,1]\n\t"
        "v_pk_mul_f32 %5, %1, %9 op_sel:[1,1] op_sel_hi:[1,0] neg_lo:[0,1]\n\t"
        "v_pk_mul_f32 %6, %2, %10 op_sel:[1,1] op_sel_hi:[1,0] neg_lo:[0,1]\n\t"
        "v_pk_mul_f32 %7, %3, %11 op_sel:[1,1] op_sel_hi:[1,0] neg_lo:[0,1]\n\t"
        "v_pk_fma_f32 %0, %0, %8, %4 op_sel:[0,0,0] op_sel_hi:[0,1,1]\n\t"
        "v_pk_fma_f32 %1, %1, %9, %5 op_sel:[0,0,0] op_sel_hi:[0,1,1]\n\t"
        "v_pk_fma_f32 %2, %2, %10, %6 op_sel:[0,0,0] op_sel_hi:[0,1,1]\n\t"
        "v_pk_fma_f32 %3, %3, %11, %7 op_sel:[0,0,0] op_sel_hi:[0,1,1]"
        : "+v"(a0), "+v"(a1), "+v"(a2), "+v"(a3), "=&v"(t0), "=&v"(t1), "=&v"(t2), "=&v"(t3)
        : "v"(w0), "v"(w1), "v"(w2), "v"(w3));
  } else {
    asm("v_pk_mul_f32 %4, %0, %8 op_sel:[0,0] op_sel_hi:[1,0]\n\t"
        "v_pk_mul_f32 %5, %1, %9 op_sel:[0,0] op_sel_hi:[1,0]\n\t"
        "v_pk_mul_f32 %6, %2, %10 op_sel:[0,0] op_sel_hi:[1,0]\n\t"
        "v_pk_mul_f32 %7, %3, %11 op_sel:[0,0] op_sel_hi:[1,0]\n\t"
        "v_pk_fma_f32 %0, %0, %8, %4 op_sel:[1,1,0] op_sel_hi:[0,1,1] neg_hi:[1,0,0]\n\t"
        "v_pk_fma_f32 %1, %1, %9, %5 op_sel:[1,1,0] op_sel_hi:[0,1,1] neg_hi:[1,0,0]\n\t"
        "v_pk_fma_f32 %2, %2, %10, %6 op_sel:[1,1,0] op_sel_hi:[0,1,1] neg_hi:[1,0,0]\n\t"
        "v_pk_fma_f32 %3, %3, %11, %7 op_sel:[1,1,0] op_sel_hi:[0,1,1] neg_hi:[1,0,0]"
        : "+v"(a0), "+v"(a1), "+v"(a2), "+v"(a3), "=&v"(t0), "=&v"(t1), "=&v"(t2), "=&v"(t3)
        : "v"(w0), "v"(w1), "v"(w2), "v"(w3));
  }
}
template <bool CONJ> __device__ __forceinline__ void cmul7(f2 (&a)[8], const f2 (&w)[7]) {
  cmul3<CONJ>(a[1], a[2], a[3], w[0], w[1], w[2]);
  cmul4<CONJ>(a[4], a[5], a[6], a[7], w[3], w[4], w[5], w[6]);
}
__device__ __forceinline__ void herm2(f2& r0, f2& r1, f2 z0, f2 q0, f2 w0, f2 z1, f2 q1, f2 w1) {
  f2 d0, d1;
  asm("v_pk_add_f32 %0, %4, %5 neg_hi:[0,1]\n\t"
      "v_pk_add_f32 %2, %4, %5 neg_lo:[0,1]\n\t"
      "v_pk_add_f32 %1, %7, %8 neg_hi:[0,1]\n\t"
      "v_pk_add_f32 %3, %7, %8 neg_lo:[0,1]\n\t"
      "v_pk_fma_f32 %0, %2, %6, %0 op_sel:[1,1,0] op_sel_hi:[1,0,1] neg_lo:[0,1,0]\n\t"
      "v_pk_fma_f32 %1, %3, %9, %1 op_sel:[1,1,0] op_sel_hi:[1,0,1] neg_lo:[0,1,0]\n\t"
      "v_pk_fma_f32 %0, %2, %6, %0 op_sel:[0,0,0] op_sel_hi:[0,1,1]\n\t"
      "v_pk_fma_f32 %1, %3, %9, %1 op_sel:[0,0,0] op_sel_hi:[0,1,1]"
      : "=&v"(r0), "=&v"(r1), "=&v"(d0), "=&v"(d1) : "v"(z0), "v"(q0), "v"(w0), "v"(z1), "v"(q1), "v"(w1));
}
__device__ __forceinline__ f2 cfma(f2 a, f2 w, f2 c) {
  f2 t, d;
  asm("v_pk_fma_f32 %0, %1, %2, %3 op_sel:[1,1,0] op_sel_hi:[1,0,1] neg_lo:[0,1,0]" : "=v"(t) : "v"(a), "v"(w), "v"(c));
  asm("v_pk_fma_f32 %0, %1, %2, %3 op_sel:[0,0,0] op_sel_hi:[0,1,1]" : "=v"(d) : "v"(a), "v"(w), "v"(t));
  return d;
}
__device__ __forceinline__ f2 add_mi(f2 a, f2 b) {
  f2 d;
  asm("v_pk_add_f32 %0, %1, %2 op_sel:[0,1] op_sel_hi:[1,0] neg_hi:[0,1]" : "=v"(d) : "v"(a), "v"(b));
  return d;
}
__device__ __forceinline__ f2 add_pi(f2 a, f2 b) {
  f2 d;
  asm("v_pk_add_f32 %0, %1, %2 op_sel:[0,1] op_sel_hi:[1,0] neg_lo:[0,1]" : "=v"(d) : "v"(a), "v"(b));
  return d;
}
__device__ __forceinline__ f2 add_cj(f2 a, f2 b) {
  f2 d;
  asm("v_pk_add_f32 %0, %1, %2 neg_hi:[0,1]" : "=v"(d) : "v"(a), "v"(b));
  return d;
}
__device__ __forceinline__ f2 sub_cj(f2 a, f2 b) {
  f2 d;
  asm("v_pk_add_f32 %0, %1, %2 neg_lo:[0,1]" : "=v"(d) : "v"(a), "v"(b));
  return d;
}
template <bool INV> __device__ __forceinline__ f2 addr(f2 a, f2 b) { return INV ? add_pi(a, b) : add_mi(a, b); }
template <bool INV> __device__ __forceinline__ f2 subr(f2 a, f2 b) { return INV ? add_mi(a, b) : add_pi(a, b); }

template <bool INV> __device__ __forceinline__ void bfly4(f2& a0, f2& a1, f2& a2, f2& a3) {
  const f2 t0 = a0 + a2, t1 = a0 - a2, t2 = a1 + a3, u = a1 - a3;
  a0 = t0 + t2; a2 = t0 - t2; a1 = addr<INV>(t1, u); a3 = subr<INV>(t1, u);
}
template <bool INV> __device__ __forceinline__ void bfly8(f2 (&a)[8]) {
  constexpr float R = 0.70710678118654752440f;
  bfly4<INV>(a[0], a[2], a[4], a[6]);
  bfly4<INV>(a[1], a[3], a[5], a[7]);
  const f2 e0 = a[0], e1 = a[2], e2 = a[4], e3 = a[6];
  const f2 o0 = a[1], o2 = a[5];
  const f2 p1 = addr<INV>(a[3], a[3]);
  const f2 p3 = subr<INV>(a[7], a[7]);
  a[0] = e0 + o0; a[4] = e0 - o0;
  a[1] = e1 + R * p1; a[5] = e1 - R * p1;
  a[2] = addr<INV>(e2, o2); a[6] = subr<INV>(e2, o2);
  a[3] = e3 - R * p3; a[7] = e3 + R * p3;
}
#define XL_PAD4(v) asm volatile("s_nop 1" : "+v"(v[0]), "+v"(v[1]), "+v"(v[2]), "+v"(v[3]))
#define XL_PAD8(v) asm volatile("s_nop 1" : "+v"(v[0]), "+v"(v[1]), "+v"(v[2]), "+v"(v[3]), "+v"(v[4]), "+v"(v[5]), "+v"(v[6]), "+v"(v[7]))

__device__ __forceinline__ void swap32(f2& a, f2& b) {
  const u2 au = __builtin_bit_cast(u2, a), bu = __builtin_bit_cast(u2, b);
  const unsigned ax = au[0], ay = au[1], bx = bu[0], by = bu[1];
  auto rx = __builtin_amdgcn_permlane32_swap(ax, bx, false, false);
  auto ry = __builtin_amdgcn_permlane32_swap(ay, by, false, false);
  a = __builtin_bit_cast(f2, u2{(unsigned)rx[0], (unsigned)ry[0]});
  b = __builtin_bit_cast(f2, u2{(unsigned)rx[1], (unsigned)ry[1]});
}
__device__ __forceinline__ void swap16(f2& a, f2& b) {
  const u2 au = __builtin_bit_cast(u2, a), bu = __builtin_bit_cast(u2, b);
  const unsigned ax = au[0], ay = au[1], bx = bu[0], by = bu[1];
  auto rx = __builtin_amdgcn_permlane16_swap(ax, bx, false, false);
  auto ry = __builtin_amdgcn_permlane16_swap(ay, by, false, false);
  a = __builtin_bit_cast(f2, u2{(unsigned)rx[0], (unsigned)ry[0]});
  b = __builtin_bit_cast(f2, u2{(unsigned)rx[1], (unsigned)ry[1]});
}
__device__ __forceinline__ void swap8(f2& a, f2& b) {
  const u2 au = __builtin_bit_cast(u2, a), bu = __builtin_bit_cast(u2, b);
  const int ax = au[0], ay = au[1], bx = bu[0], by = bu[1];
  const int nax = __builtin_amdgcn_update_dpp(ax, bx, 0x128, 0xf, 0xc, false);
  const int nbx = __builtin_amdgcn_update_dpp(bx, ax, 0x128, 0xf, 0x3, false);
  const int nay = __builtin_amdgcn_update_dpp(ay, by, 0x128, 0xf, 0xc, false);
  const int nby = __builtin_amdgcn_update_dpp(by, ay, 0x128, 0xf, 0x3, false);
  a = __builtin_bit_cast(f2, u2{(unsigned)nax, (unsigned)nay});
  b = __builtin_bit_cast(f2, u2{(unsigned)nbx, (unsigned)nby});
}

template <int CTRL> __device__ __forceinline__ float dppmov(float v) {
  return __builtin_bit_cast(float, __builtin_amdgcn_update_dpp(0, __builtin_bit_cast(int, v), CTRL, 0xf, 0xf, true));
}
__device__ __forceinline__ float wave_sum(float v) {
  v += dppmov<0xB1>(v);
  v += dppmov<0x4E>(v);
  v += dppmov<0x141>(v);
  v += dppmov<0x140>(v);
  {
    unsigned u = __builtin_bit_cast(unsigned, v);
    auto r = __builtin_amdgcn_permlane16_swap(u, u, false, false);
    v = __builtin_bit_cast(float, (unsigned)r[0]) + __builtin_bit_cast(float, (unsigned)r[1]);
  }
  {
    unsigned u = __builtin_bit_cast(unsigned, v);
    auto r = __builtin_amdgcn_permlane32_swap(u, u, false, false);
    v = __builtin_bit_cast(float, (unsigned)r[0]) + __builtin_bit_cast(float, (unsigned)r[1]);
  }
  return v;
}

__device__ __forceinline__ f4 mfma(u4 a, u4 b, f4 c) {
  return __builtin_amdgcn_mfma_f32_16x16x32_bf16(__builtin_bit_cast(s8, a), __builtin_bit_cast(s8, b), c, 0, 0, 0);
}
typedef float f16v __attribute__((ext_vector_type(16)));
__device__ __forceinline__ f16v mfma32(u4 a, u4 b, f16v c) {
  return __builtin_amdgcn_mfma_f32_32x32x16_bf16(__builtin_bit_cast(s8, a), __builtin_bit_cast(s8, b), c, 0, 0, 0);
}
__device__ __forceinline__ float silu_f(float v) {
  return v * __builtin_amdgcn_rcpf(1.0f + __expf(-v));
}

__device__ __forceinline__ void wave_sum2(float& a, float& b) {
  a += dppmov<0xB1>(a);  b += dppmov<0xB1>(b);
  a += dppmov<0x4E>(a);  b += dppmov<0x4E>(b);
  a += dppmov<0x141>(a); b += dppmov<0x141>(b);
  a += dppmov<0x140>(a); b += dppmov<0x140>(b);
  unsigned ua = __builtin_bit_cast(unsigned, a), ub = __builtin_bit_cast(unsigned, b);
  auto r = __builtin_amdgcn_permlane16_swap(ua, ub, false, false);
  float c = __builtin_bit_cast(float, (unsigned)r[0]) + __builtin_bit_cast(float, (unsigned)r[1]);
  unsigned uc = __builtin_bit_cast(unsigned, c);
  auto r2 = __builtin_amdgcn_permlane32_swap(uc, uc, false, false);
  c = __builtin_bit_cast(float, (unsigned)r2[0]) + __builtin_bit_cast(float, (unsigned)r2[1]);
  a = __builtin_bit_cast(float, __builtin_amdgcn_readlane(__builtin_bit_cast(int, c), 0));
  b = __builtin_bit_cast(float, __builtin_amdgcn_readlane(__builtin_bit_cast(int, c), 16));
}
__device__ __forceinline__ void ln_mod(const f2 (&xv)[4], const unsigned (&sc1)[4], const unsigned (&sh)[4], f2 (&h)[4]) {
  const float x00 = xv[0].x;
  const float piv = __builtin_bit_cast(float, __builtin_amdgcn_readfirstlane(__builtin_bit_cast(int, x00)));
  f2 d[4];
#pragma unroll
  for (int i = 0; i < 4; ++i) d[i] = xv[i] - piv;
  const f2 s2 = (d[0] + d[1]) + (d[2] + d[3]);
  const f2 q2 = (d[0] * d[0] + d[1] * d[1]) + (d[2] * d[2] + d[3] * d[3]);
  float s1 = s2.x + s2.y, sq = q2.x + q2.y;
  wave_sum2(s1, sq);
  const float dm = s1 * (1.0f / 512.0f);
  const float var = __builtin_fmaxf(sq * (1.0f / 512.0f) - dm * dm, 0.0f);
  const float mbp = rb1(piv + dm) - piv;
  const float sd = rb1(__builtin_amdgcn_sqrtf(rb1(rb1(var) + 1e-5f)));
  const float rs = __builtin_amdgcn_rcpf(sd);
#pragma unroll
  for (int i = 0; i < 4; ++i) h[i] = ((d[i] - mbp) * rs) * unpk(sc1[i]) + unpk(sh[i]);
}

struct Tw256 { f2 a[3], b[3], c[3]; int x2a, x2b, x3a, x3b; };
__device__ __forceinline__ void make_tw256(Tw256& t, int l) {
#pragma unroll
  for (int r = 1; r < 4; ++r) {
    t.a[r - 1] = tw2048(l * r * 8);
    t.b[r - 1] = tw2048((l & 15) * r * 32);
    t.c[r - 1] = tw2048((l & 3) * r * 128);
  }
  const int d2 = l >> 4, d1 = (l >> 2) & 3, d0 = l & 3;
  t.x2a = 80 * d2 + 4 * d1 + d0;
  t.x2b = 80 * d2 + 20 * d1 + d0;
  t.x3a = 80 * d2 + 4 * d1 + d0;
  t.x3b = 80 * d2 + 4 * d1 + 17 * d0;
}
#define PIN_TW(t) do { \
  asm volatile("" : "+v"(t.a[0]), "+v"(t.a[1]), "+v"(t.a[2]), "+v"(t.b[0]), "+v"(t.b[1]), "+v"(t.b[2]), "+v"(t.c[0]), "+v"(t.c[1]), "+v"(t.c[2])); \
  asm volatile("" : "+v"(t.x2a), "+v"(t.x2b), "+v"(t.x3a), "+v"(t.x3b)); } while (0)

__device__ __forceinline__ void fft256_fwd(f2 (&v)[4], const Tw256& t, f2* lds) {
  bfly4<false>(v[0], v[1], v[2], v[3]);
  cmul3<false>(v[1], v[2], v[3], t.a[0], t.a[1], t.a[2]);
  XL_PAD4(v);
  swap32(v[0], v[2]); swap32(v[1], v[3]);
  swap16(v[0], v[1]); swap16(v[2], v[3]);
  bfly4<false>(v[0], v[1], v[2], v[3]);
  cmul3<false>(v[1], v[2], v[3], t.b[0], t.b[1], t.b[2]);
#pragma unroll
  for (int r = 0; r < 4; ++r) lds[t.x2a + 20 * r] = v[r];
  __builtin_amdgcn_wave_barrier();
#pragma unroll
  for (int r = 0; r < 4; ++r) v[r] = lds[t.x2b + 4 * r];
  __builtin_amdgcn_wave_barrier();
  bfly4<false>(v[0], v[1], v[2], v[3]);
  cmul3<false>(v[1], v[2], v[3], t.c[0], t.c[1], t.c[2]);
#pragma unroll
  for (int r = 0; r < 4; ++r) lds[t.x3a + 17 * r] = v[r];
  __builtin_amdgcn_wave_barrier();
#pragma unroll
  for (int r = 0; r < 4; ++r) v[r] = lds[t.x3b + r];
  __builtin_amdgcn_wave_barrier();
  bfly4<false>(v[0], v[1], v[2], v[3]);
}
template <class H> __device__ __forceinline__ void fft256_inv(f2 (&v)[4], const Tw256& t, f2* lds, H hook) {
  bfly4<true>(v[0], v[1], v[2], v[3]);
#pragma unroll
  for (int r = 0; r < 4; ++r) lds[t.x3b + r] = v[r];
  __builtin_amdgcn_wave_barrier();
#pragma unroll
  for (int r = 0; r < 4; ++r) v[r] = lds[t.x3a + 17 * r];
  __builtin_amdgcn_wave_barrier();
  hook(std::integral_constant<int, 0>{});
  cmul3<true>(v[1], v[2], v[3], t.c[0], t.c[1], t.c[2]);
  bfly4<true>(v[0], v[1], v[2], v[3]);
#pragma unroll
  for (int r = 0; r < 4; ++r) lds[t.x2b + 4 * r] = v[r];
  __builtin_amdgcn_wave_barrier();
#pragma unroll
  for (int r = 0; r < 4; ++r) v[r] = lds[t.x2a + 20 * r];
  __builtin_amdgcn_wave_barrier();
  hook(std::integral_constant<int, 1>{});
  cmul3<true>(v[1], v[2], v[3], t.b[0], t.b[1], t.b[2]);
  bfly4<true>(v[0], v[1], v[2], v[3]);
  XL_PAD4(v);
  swap16(v[0], v[1]); swap16(v[2], v[3]);
  swap32(v[0], v[2]); swap32(v[1], v[3]);
  hook(std::integral_constant<int, 2>{});
  cmul3<true>(v[1], v[2], v[3], t.a[0], t.a[1], t.a[2]);
  bfly4<true>(v[0], v[1], v[2], v[3]);
}
template <int RP> __device__ __forceinline__ void fft256_inv2(f2 (&v)[2][4], const Tw256& t, f2* lds) {
#pragma unroll
  for (int u = 0; u < 2; ++u) bfly4<true>(v[u][0], v[u][1], v[u][2], v[u][3]);
#pragma unroll
  for (int u = 0; u < 2; ++u)
#pragma unroll
    for (int r = 0; r < 4; ++r) lds[t.x3b + u * RP + r] = v[u][r];
  __builtin_amdgcn_wave_barrier();
#pragma unroll
  for (int u = 0; u < 2; ++u)
#pragma unroll
    for (int r = 0; r < 4; ++r) v[u][r] = lds[t.x3a + u * RP + 17 * r];
  __builtin_amdgcn_wave_barrier();
#pragma unroll
  for (int u = 0; u < 2; ++u) {
    cmul3<true>(v[u][1], v[u][2], v[u][3], t.c[0], t.c[1], t.c[2]);
    bfly4<true>(v[u][0], v[u][1], v[u][2], v[u][3]);
#pragma unroll
    for (int r = 0; r < 4; ++r) lds[t.x2b + u * RP + 4 * r] = v[u][r];
  }
  __builtin_amdgcn_wave_barrier();
#pragma unroll
  for (int u = 0; u < 2; ++u)
#pragma unroll
    for (int r = 0; r < 4; ++r) v[u][r] = lds[t.x2a + u * RP + 20 * r];
  __builtin_amdgcn_wave_barrier();
#pragma unroll
  for (int u = 0; u < 2; ++u) {
    cmul3<true>(v[u][1], v[u][2], v[u][3], t.b[0], t.b[1], t.b[2]);
    bfly4<true>(v[u][0], v[u][1], v[u][2], v[u][3]);
  }
  XL_PAD4(v[0]); XL_PAD4(v[1]);
#pragma unroll
  for (int u = 0; u < 2; ++u) {
    swap16(v[u][0], v[u][1]); swap16(v[u][2], v[u][3]);
    swap32(v[u][0], v[u][2]); swap32(v[u][1], v[u][3]);
  }
#pragma unroll
  for (int u = 0; u < 2; ++u) {
    cmul3<true>(v[u][1], v[u][2], v[u][3], t.a[0], t.a[1], t.a[2]);
    bfly4<true>(v[u][0], v[u][1], v[u][2], v[u][3]);
  }
}
__device__ __forceinline__ int zpos(int k) { return k + (k >> 4); }

__device__ __host__ __forceinline__ int k2_k1(int tt, int kb) {
  int seq = (tt >> 6) * 8 + (tt & 7);
  int q = seq >> 2, k0 = seq & 3, ka = (tt >> 3) & 7;
  return 8 * (k0 + 4 * ka + 32 * kb) + q;
}

__device__ __forceinline__ void transpose8x8(const u4 (&in)[8], u4 (&out)[8]) {
#pragma unroll
  for (int c2 = 0; c2 < 4; ++c2) {
#pragma unroll
    for (int r2 = 0; r2 < 4; ++r2) {
      unsigned a = in[2 * r2][c2], b = in[2 * r2 + 1][c2];
      out[2 * c2][r2] = __builtin_amdgcn_perm(b, a, 0x05040100u);
      out[2 * c2 + 1][r2] = __builtin_amdgcn_perm(b, a, 0x07060302u);
    }
  }
}

#define PREP_AUX 16
#define T_AUX 0
constexpr int AUX_SC1 = 16;
constexpr int OOB = 0x40000000;
__device__ __forceinline__ __amdgpu_buffer_rsrc_t ws_rsrc(unsigned char* ws) {
  return __builtin_amdgcn_make_buffer_rsrc((void*)ws, 0, (int)WS_BYTES, 0x00020000);
}

__device__ __forceinline__ int prep_w1_off(int job, int l, int r) {
  const int cg = job % 7, ks = job / 7, no = l & 31, h = l >> 5;
  return ((16 * ks + 8 * h + r) * FF + cg * 256 + no * 8) * 2;
}
__device__ __forceinline__ void prep_w1_fin(const u4 (&in)[8], __amdgpu_buffer_rsrc_t rs, u4* buf, int job, int l) {
  const int cg = job % 7, ks = job / 7, no = l & 31, h = l >> 5;
  u4 out[8];
  transpose8x8(in, out);
#pragma unroll
  for (int cc = 0; cc < 8; ++cc) buf[((no >> 2) * 64 + 8 * (no & 3) + 32 * h + cc) ^ (no & 7)] = out[cc];
  __builtin_amdgcn_wave_barrier();
#pragma unroll
  for (int f = 0; f < 8; ++f)
    __builtin_amdgcn_raw_buffer_store_b128(buf[(f * 64 + l) ^ ((4 * f + ((l >> 3) & 3)) & 7)], rs,
                                           (int)OFF_W1F + (((cg * 8 + f) * 32 + ks) * 64 + l) * 16, 0, PREP_AUX);
}
__device__ __forceinline__ int prep_w2_off(int job, int l, int e) {
  const int cg = job & 1, cs = job >> 1, no = l & 31, h = l >> 5;
  return ((16 * cs + 8 * (e >> 2) + 4 * h + (e & 3)) * D + cg * 256 + no * 8) * 2;
}
__device__ __forceinline__ void prep_w2_fin(const u4 (&in)[8], __amdgpu_buffer_rsrc_t rs, u4* buf, int job, int l) {
  const int cg = job & 1, cs = job >> 1, no = l & 31, h = l >> 5;
  u4 out[8];
  transpose8x8(in, out);
  const int base = (no >> 2) * 64 + 16 * (no & 1) + 4 * ((no >> 1) & 1) + 32 * h;
#pragma unroll
  for (int cc = 0; cc < 8; ++cc) buf[base + (cc & 3) + 8 * (cc >> 2)] = out[cc];
  __builtin_amdgcn_wave_barrier();
#pragma unroll
  for (int f = 0; f < 8; ++f)
    __builtin_amdgcn_raw_buffer_store_b128(buf[f * 64 + l], rs,
                                           (int)OFF_W2F + ((((cg * 4 + (f >> 1)) * 112 + cs) * 2 + (f & 1)) * 64 + l) * 16, 0, PREP_AUX);
}

constexpr int K0_MOD = 96, K0_WF = 128, K0_MISC = 32;
constexpr int K0_BLOCKS = K0_MOD + K0_WF + K0_MISC;
constexpr unsigned SENT = 0xFFFFFFFFu;
constexpr int FILL_PER_THREAD = (int)((OFF_X1 - OFF_H2F) / (256 * 256 * 16));
static_assert((size_t)FILL_PER_THREAD * 256 * 256 * 16 == OFF_X1 - OFF_H2F && K0_BLOCKS == 256, "fill covers the h2 hand-off buffer");
#define FILL_AUX 16

__global__ __launch_bounds__(256) void k0_prep(const unsigned short* __restrict__ cvec,
                                               const unsigned short* __restrict__ wmod,
                                               const unsigned short* __restrict__ wr,
                                               const unsigned short* __restrict__ wi,
                                               unsigned char* ws) {
  __shared__ float red[4][32];
  const int tid = threadIdx.x;
  int b = blockIdx.x;
  STAMP(0, 0);
  const __amdgpu_buffer_rsrc_t rs = ws_rsrc(ws);
  auto fill = [&]() {
#pragma unroll
    for (int i = 0; i < FILL_PER_THREAD; ++i)
      __builtin_amdgcn_raw_buffer_store_b128(u4{SENT, SENT, SENT, SENT}, rs,
                                             (int)OFF_H2F + ((blockIdx.x * FILL_PER_THREAD + i) * 256 + tid) * 16, 0, FILL_AUX);
  };
  if (b < K0_MOD) {
    const int co = tid & 3, rg = tid >> 2;
    u4 wv[8];
    unsigned short cs[8];
#pragma unroll
    for (int i = 0; i < 8; ++i) cs[i] = cvec[rg + 64 * i];
#pragma unroll
    for (int i = 0; i < 8; ++i)
      wv[i] = *(const u4*)(wmod + (size_t)(rg + 64 * i) * 3072 + b * 32 + co * 8);
    SCHED_FENCE();
    fill();
    SCHED_FENCE();
    f2 acc[4];
#pragma unroll
    for (int e = 0; e < 4; ++e) acc[e] = f2{0.f, 0.f};
#pragma unroll
    for (int i = 0; i < 8; ++i) {
      const float cv = bflo(cs[i]);
      const float sv = rb1(silu_f(cv));
#pragma unroll
      for (int e2 = 0; e2 < 4; ++e2) acc[e2] += sv * unpk(wv[i][e2]);
    }
    STAMP(0, 2);
#pragma unroll
    for (int e2 = 0; e2 < 4; ++e2) {
      float v0 = acc[e2].x, v1 = acc[e2].y;
      v0 += dppmov<0x124>(v0); v1 += dppmov<0x124>(v1);
      v0 += dppmov<0x128>(v0); v1 += dppmov<0x128>(v1);
      unsigned u0 = __builtin_bit_cast(unsigned, v0), u1 = __builtin_bit_cast(unsigned, v1);
      auto r = __builtin_amdgcn_permlane16_swap(u0, u1, false, false);
      float c = __builtin_bit_cast(float, (unsigned)r[0]) + __builtin_bit_cast(float, (unsigned)r[1]);
      unsigned uc = __builtin_bit_cast(unsigned, c);
      auto r2 = __builtin_amdgcn_permlane32_swap(uc, uc, false, false);
      c = __builtin_bit_cast(float, (unsigned)r2[0]) + __builtin_bit_cast(float, (unsigned)r2[1]);
      if ((tid & 0x2c) == 0) red[tid >> 6][co * 8 + e2 * 2 + ((tid >> 4) & 1)] = c;
    }
    __syncthreads();
    if (tid < 32) {
      float s = rb1((red[0][tid] + red[1][tid]) + (red[2][tid] + red[3][tid]));
      const int col = b * 32 + tid, p = col >> 9, e = col & 511;
      if (p == 1 || p == 4) s = rb1(s + 1.0f);
      const unsigned short sb = (unsigned short)(__builtin_bit_cast(unsigned, s) >> 16);
      const int n = e >> 1, l = n & 63, i = n >> 6, hf = e & 1;
      if (p < 2) ((unsigned short*)(ws + OFF_MODK1))[((l * 4 + i) * 2 + p) * 2 + hf] = sb;
      else if (p < 5) ((unsigned short*)(ws + OFF_MODK3))[((l * 4 + i) * 3 + (p == 2 ? 0 : (p == 4 ? 1 : 2))) * 2 + hf] = sb;
      else ((unsigned short*)(ws + OFF_GB))[e] = sb;
    }
    STAMP(0, 1);
    return;
  }
  b -= K0_MOD;
  if (b < K0_WF) {
    const int tp = (b & 7) * 16 + (b >> 3);
    const int k2 = tid;
    unsigned short vr[16], vi[16];
#pragma unroll
    for (int e = 0; e < 16; ++e) {
      const int k1 = k2_k1(2 * tp + (e >> 3), e & 7);
      vr[e] = wr[k1 * DF + k2];
      vi[e] = wi[k1 * DF + k2];
    }
    SCHED_FENCE();
    fill();
    SCHED_FENCE();
#pragma unroll
    for (int e4 = 0; e4 < 4; ++e4) {
      u4 o;
#pragma unroll
      for (int e = 0; e < 4; ++e) o[e] = (unsigned)vr[e4 * 4 + e] | ((unsigned)vi[e4 * 4 + e] << 16);
      __builtin_amdgcn_raw_buffer_store_b128(o, rs, (int)OFF_WF + (k2 * 512 + tp * 4 + e4) * 16, 0, 0);
    }
    STAMP(0, 3);
    STAMP_DRAIN();
    STAMP(0, 5);
    return;
  }
  b -= K0_WF;
  fill();
  if (tid < 64) {
    const int e = b * 64 + tid;
    const int tt = e & 255, kb = e >> 8;
    const int k1 = k2_k1(tt, kb), nk = (2048 - k1) & 2047;
    f2 a0 = f2{bflo(wr[k1 * DF]), bflo(wi[k1 * DF])};
    f2 a1 = f2{bflo(wr[nk * DF]), -bflo(wi[nk * DF])};
    f2 b0 = f2{bflo(wr[k1 * DF + 256]), bflo(wi[k1 * DF + 256])};
    f2 b1 = f2{bflo(wr[nk * DF + 256]), -bflo(wi[nk * DF + 256])};
    f2 w0h = (a0 + a1) * 0.5f, w256h = (b0 + b1) * 0.5f;
    f2 P = (w0h + w256h) * 0.5f, Q = (w0h - w256h) * 0.5f;
    ((f4*)(ws + OFF_PQ))[e] = f4{P.x, P.y, Q.x, Q.y};
    STAMP(0, 4);
  }
}

constexpr int ROWP = 322;

__global__ __launch_bounds__(512) void k1_rows(const unsigned* __restrict__ x32, const unsigned short* __restrict__ w1,
                                               const unsigned short* __restrict__ w2, unsigned char* ws) {
  __shared__ __attribute__((aligned(16))) f2 lds[8 * ROWP];
  __shared__ u4 pbuf[1024];
  const int j = blockIdx.x;
  const int tid = threadIdx.x, w = __builtin_amdgcn_readfirstlane(tid >> 6), l = tid & 63;
  const __amdgpu_buffer_rsrc_t rs = ws_rsrc(ws);
  STAMP(1, 0);
  const int s = j + 256 * w;
  unsigned xr[4], sc1[4], sh[4];
#pragma unroll
  for (int i = 0; i < 4; ++i) xr[i] = x32[(size_t)s * 256 + l + 64 * i];
  {
    const u4* mp = (const u4*)(ws + OFF_MODK1) + l * 2;
    const u4 m0 = mp[0], m1 = mp[1];
    sh[0] = m0[0]; sc1[0] = m0[1]; sh[1] = m0[2]; sc1[1] = m0[3];
    sh[2] = m1[0]; sc1[2] = m1[1]; sh[3] = m1[2]; sc1[3] = m1[3];
  }
  const bool side = (w == 4 || w == 5) && j < 224;
  u4 pin[8];
  {
    const __amdgpu_buffer_rsrc_t wrs =
        __builtin_amdgcn_make_buffer_rsrc((void*)(w == 4 ? w1 : w2), 0, side ? D * FF * 2 : 0, 0x00020000);
#pragma unroll
    for (int r = 0; r < 8; ++r)
      pin[r] = __builtin_amdgcn_raw_buffer_load_b128(wrs, w == 4 ? prep_w1_off(j, l, r) : prep_w2_off(j, l, r), 0, 0);
  }
#pragma unroll
  for (int i = 0; i < 2; ++i)
    __builtin_amdgcn_raw_buffer_store_b128(u4{SENT, SENT, SENT, SENT}, rs, w < 7 ? (int)OFF_AF + ((j * 14 + 2 * w + i) * 64 + l) * 16 : OOB, 0, FILL_AUX);
  SCHED_FENCE();
  Tw256 tw;
  make_tw256(tw, l);
  tw.x2a += w * ROWP; tw.x2b += w * ROWP; tw.x3a += w * ROWP; tw.x3b += w * ROWP;
  const int k2 = tid & 255;
  f2 t512 = tw2048(4 * k2 + 512);
  f2 twq[7];
#pragma unroll
  for (int q = 1; q < 8; ++q) twq[q - 1] = tw2048(j * q);
  int zw = w * ROWP + (l >> 4) + 4 * ((l >> 2) & 3) + 17 * (l & 3);
  int za = zpos(k2), zb = zpos((256 - k2) & 255);
  int to = (int)OFF_T + (j * 256 + k2) * 64;
  asm volatile("" : "+v"(twq[0]), "+v"(twq[1]), "+v"(twq[2]), "+v"(twq[3]), "+v"(twq[4]), "+v"(twq[5]), "+v"(twq[6]), "+v"(t512));
  asm volatile("" : "+v"(zw), "+v"(za), "+v"(zb), "+v"(to));
  PIN_TW(tw);
  SCHED_FENCE();
  f2 xv[4], v[4];
#pragma unroll
  for (int i = 0; i < 4; ++i) xv[i] = unpk(xr[i]);
  ln_mod(xv, sc1, sh, v);
  STAMP(1, 1);
  fft256_fwd(v, tw, lds);
  STAMP(1, 2);
#pragma unroll
  for (int r = 0; r < 4; ++r) lds[zw + 68 * r] = v[r];
  __syncthreads();
  if (w < 4) {
    f2 a[8], z[8], zr[8];
#pragma unroll
    for (int m = 0; m < 8; ++m) { z[m] = lds[m * ROWP + za]; zr[m] = lds[m * ROWP + zb]; }
#pragma unroll
    for (int m = 0; m < 8; m += 2) herm2(a[m], a[m + 1], z[m], zr[m], t512, z[m + 1], zr[m + 1], t512);
    if (w == 0) {
#pragma unroll
      for (int m = 0; m < 8; ++m) a[m].y = k2 == 0 ? 2.0f * (z[m].x - z[m].y) : a[m].y;
    }
    bfly8<false>(a);
    cmul7<false>(a, twq);
#pragma unroll
    for (int q = 0; q < 4; ++q)
      __builtin_amdgcn_raw_buffer_store_b128(__builtin_bit_cast(u4, f4{a[2 * q].x, a[2 * q].y, a[2 * q + 1].x, a[2 * q + 1].y}), rs,
                                             to + q * 16, 0, T_AUX);
    STAMP(1, 3);
    STAMP_DRAIN();
    STAMP(1, 4);
  } else if (side) {
    if (w == 4) prep_w1_fin(pin, rs, pbuf, j, l);
    else prep_w2_fin(pin, rs, pbuf + 512, j, l);
    STAMPT(1, 5, 256);
    STAMP_DRAIN();
    STAMPT(1, 6, 256);
  }
}

constexpr int SEQP = 64 + 1;
#define K2_TOUCH 0
__global__ __launch_bounds__(256 + 64 * K2_TOUCH) void k2_cols(unsigned char* ws) {
  __shared__ __attribute__((aligned(16))) f2 lds[2048 + 64];
  const int b = blockIdx.x;
  const int t = threadIdx.x, l = t & 63;
  const __amdgpu_buffer_rsrc_t rs = ws_rsrc(ws);
  STAMP(2, 0);
  const int k2 = (b & 7) * 32 + (b >> 3);
  const int jr = t >> 2, qp = t & 3;
  const int co = (int)OFF_T + (jr * 1024 + k2 * 4 + qp) * 16;
  u4 cin[4];
#pragma unroll
  for (int i = 0; i < 4; ++i) cin[i] = __builtin_amdgcn_raw_buffer_load_b128(rs, co + i * (1 << 20), 0, 0);
  u4 wq[8];
  {
    const int wo = k2 != 0 ? (int)OFF_WF + (k2 * 256 + t) * 32 : (int)OFF_PQ + t * 16;
    const int ws1 = k2 != 0 ? 16 : 4096;
    wq[0] = __builtin_amdgcn_raw_buffer_load_b128(rs, wo, 0, 0);
    wq[1] = __builtin_amdgcn_raw_buffer_load_b128(rs, wo + ws1, 0, 0);
    const int po = k2 != 0 ? OOB : wo;
#pragma unroll
    for (int kb = 2; kb < 8; ++kb) wq[kb] = __builtin_amdgcn_raw_buffer_load_b128(rs, po + kb * 4096, 0, 0);
  }
#pragma unroll
  for (int i = 0; i < 4; ++i)
    __builtin_amdgcn_raw_buffer_store_b128(u4{SENT, SENT, SENT, SENT}, rs,
                                           i * 256 + t < 896 ? (int)OFF_AF + ((256 + b) * 896 + i * 256 + t) * 16 : OOB, 0, FILL_AUX);
  SCHED_FENCE();
  f2 twA[3], twB[7];
#pragma unroll
  for (int r = 1; r < 4; ++r) twA[r - 1] = tw2048(jr * r * 8);
  const int bb = l >> 3;
#pragma unroll
  for (int r = 1; r < 8; ++r) twB[r - 1] = tw2048(bb * r * 32);
  int pa = (8 * qp) * SEQP + jr;
  int pb = ((t >> 6) * 8 + (t & 7)) * SEQP + bb;
  asm volatile("" : "+v"(twB[0]), "+v"(twB[1]), "+v"(twB[2]), "+v"(twB[3]), "+v"(twB[4]), "+v"(twB[5]), "+v"(twB[6]), "+v"(twA[0]), "+v"(twA[1]), "+v"(twA[2]));
  asm volatile("" : "+v"(pa), "+v"(pb));
  SCHED_FENCE();
  f2 a0[4], a1[4];
#pragma unroll
  for (int i = 0; i < 4; ++i) { const f4 c = __builtin_bit_cast(f4, cin[i]); a0[i] = c.xy; a1[i] = c.zw; }
  bfly4<false>(a0[0], a0[1], a0[2], a0[3]);
  bfly4<false>(a1[0], a1[1], a1[2], a1[3]);
  cmul3<false>(a0[1], a0[2], a0[3], twA[0], twA[1], twA[2]);
  cmul3<false>(a1[1], a1[2], a1[3], twA[0], twA[1], twA[2]);
#pragma unroll
  for (int r = 0; r < 4; ++r) {
    lds[pa + r * SEQP] = a0[r];
    lds[pa + (4 + r) * SEQP] = a1[r];
  }
  __syncthreads();
  STAMP(2, 1);
  f2 v[8];
#pragma unroll
  for (int a = 0; a < 8; ++a) v[a] = lds[pb + 8 * a];
  bfly8<false>(v);
  cmul7<false>(v, twB);
  XL_PAD8(v);
#pragma unroll
  for (int i = 0; i < 4; ++i) swap32(v[i], v[i + 4]);
  swap16(v[0], v[2]); swap16(v[1], v[3]); swap16(v[4], v[6]); swap16(v[5], v[7]);
#pragma unroll
  for (int i = 0; i < 8; i += 2) swap8(v[i], v[i + 1]);
  bfly8<false>(v);
  if (k2 != 0) {
    cmul4<false>(v[0], v[1], v[2], v[3], unpk(wq[0][0]), unpk(wq[0][1]), unpk(wq[0][2]), unpk(wq[0][3]));
    cmul4<false>(v[4], v[5], v[6], v[7], unpk(wq[1][0]), unpk(wq[1][1]), unpk(wq[1][2]), unpk(wq[1][3]));
  } else {
    __syncthreads();
#pragma unroll
    for (int kb = 0; kb < 8; ++kb) lds[k2_k1(t, kb)] = v[kb];
    __syncthreads();
#pragma unroll
    for (int kb = 0; kb < 8; ++kb) {
      f2 fn = lds[(2048 - k2_k1(t, kb)) & 2047];
      fn.y = -fn.y;
      const f4 c = __builtin_bit_cast(f4, wq[kb]);
      v[kb] = cfma(fn, c.zw, cmul(v[kb], c.xy));
    }
  }
  bfly8<true>(v);
  XL_PAD8(v);
#pragma unroll
  for (int i = 0; i < 8; i += 2) swap8(v[i], v[i + 1]);
  swap16(v[0], v[2]); swap16(v[1], v[3]); swap16(v[4], v[6]); swap16(v[5], v[7]);
#pragma unroll
  for (int i = 0; i < 4; ++i) swap32(v[i], v[i + 4]);
  cmul7<true>(v, twB);
  bfly8<true>(v);
  if (k2 == 0) __syncthreads();
#pragma unroll
  for (int a = 0; a < 8; ++a) lds[pb + 8 * a] = v[a];
  __syncthreads();
#pragma unroll
  for (int r = 0; r < 4; ++r) {
    a0[r] = lds[pa + r * SEQP];
    a1[r] = lds[pa + (4 + r) * SEQP];
  }
  cmul3<true>(a0[1], a0[2], a0[3], twA[0], twA[1], twA[2]);
  cmul3<true>(a1[1], a1[2], a1[3], twA[0], twA[1], twA[2]);
  bfly4<true>(a0[0], a0[1], a0[2], a0[3]);
  bfly4<true>(a1[0], a1[1], a1[2], a1[3]);
#pragma unroll
  for (int i = 0; i < 4; ++i)
    __builtin_amdgcn_raw_buffer_store_b128(__builtin_bit_cast(u4, f4{a0[i].x, a0[i].y, a1[i].x, a1[i].y}), rs, co + i * (1 << 20), 0, T_AUX);
  STAMP(2, 2);
  STAMP_DRAIN();
  STAMP(2, 3);
}

constexpr unsigned SPIN_CAP = 1u << 18;
constexpr int L_H2 = 0;
constexpr int L_AST = 65536;
constexpr int L_CNT = L_AST + 28672;
constexpr int L_R01 = 98304;
constexpr int L_STG = L_R01;
constexpr int LDS3_BYTES = 163840;
#define P_N1 28
#define P_W2A 12
#define W2B 20
#define FLAGSTEP 48
#define A0STEP 16
#define POLLSTEP 62
#define A2STEP 7
#define NPARK 8
#define F_W2A 8
#define F_L1NB 1
#define MIDBAR 1
#define S_DELAY 75
#define P_PRIO 0
#define F_DELAY 2
#define P_DELAY 22
#define BAR() do { SCHED_FENCE(); asm volatile("s_waitcnt lgkmcnt(0)" ::: "memory"); __builtin_amdgcn_s_barrier(); asm volatile("" ::: "memory"); SCHED_FENCE(); } while (0)

__device__ __forceinline__ unsigned nosent(unsigned v) { return v < SENT - 1u ? v : SENT - 1u; }
template <int N> __device__ __forceinline__ bool all_written(const u4* v) {
  unsigned m = 0u;
#pragma unroll
  for (int i = 0; i < N; ++i) {
    const unsigned a = v[i][0] > v[i][1] ? v[i][0] : v[i][1], c = v[i][2] > v[i][3] ? v[i][2] : v[i][3];
    m = m > a ? m : a;
    m = m > c ? m : c;
  }
  return __all(m != SENT);
}
template <int N> __device__ __forceinline__ void reload_until_written(u4* v, __amdgpu_buffer_rsrc_t rs, int off, int stride) {
  if (__builtin_expect(!all_written<N>(v), 0)) {
    unsigned s = 0;
    bool ok;
    do {
      __builtin_amdgcn_s_sleep(1);
      asm volatile("" : "+v"(off) :: "memory");
#pragma unroll
      for (int i = 0; i < N; ++i) v[i] = __builtin_amdgcn_raw_buffer_load_b128(rs, off + i * stride, 0, AUX_SC1);
      ok = all_written<N>(v);
    } while (!ok && ++s < SPIN_CAP);
  }
}
template <int NTB> __device__ __forceinline__ void wait_flags(unsigned v, gu32* word) {
  if (__builtin_expect(!__all(v >= (unsigned)NTB), 0)) {
    unsigned s = 0;
    do {
      __builtin_amdgcn_s_sleep(2);
      v = __hip_atomic_load(word, RLX_AGENT);
    } while (!__all(v >= (unsigned)NTB) && ++s < SPIN_CAP);
  }
  __builtin_amdgcn_fence(__ATOMIC_ACQUIRE, "wavefront");
  asm volatile("" ::: "memory");
}
template <int HAVE> constexpr int w2_at(int st) {
  if (st < 32) { const int f = HAVE + st; return f < W2B ? f : -1; }
  const int f = W2B + (st - 33);
  return (st >= 33 && f < 28) ? f : -1;
}
template <int HAVE> constexpr int vm_since(int stEnd) {
  int n = 0;
  for (int st = 32; st <= stEnd; ++st) n += (w2_at<HAVE>(st) >= 0 ? 1 : 0) + (st == POLLSTEP ? 1 : 0);
  return n;
}
static_assert(W2B <= 22 && FLAGSTEP >= 32 && FLAGSTEP < 64, "schedule");

constexpr int park_addr(int i, int fw) {
  const int slot = fw * NPARK + i;
  return slot < 28 ? L_AST + slot * 1024 : L_R01 + 21504 + (slot - 28) * 1024;
}
static_assert(4 * NPARK <= 64 && 8 * ROWP * 8 <= 21504, "parking space");
template <int F0, int F1> __device__ __forceinline__ void park(__amdgpu_buffer_rsrc_t rs, unsigned char* smem, int jj, int l) {
  u4 t[(F1 - F0) * NPARK];
#pragma unroll
  for (int f = F0; f < F1; ++f)
#pragma unroll
    for (int i = 0; i < NPARK; ++i)
      t[(f - F0) * NPARK + i] = __builtin_amdgcn_raw_buffer_load_b128(rs, (int)OFF_W1F + (((7 * jj + f) * 32 + i) * 64 + l) * 16, 0, 0);
  SCHED_FENCE();
#pragma unroll
  for (int f = F0; f < F1; ++f)
#pragma unroll
    for (int i = 0; i < NPARK; ++i) *(u4*)(smem + park_addr(i, f) + l * 16) = t[(f - F0) * NPARK + i];
}
#define LDW(arr, i, off) arr[i] = __builtin_amdgcn_raw_buffer_load_b128(rs, (off) + (i) * 1024, 0, 0)

template <int CLS>
__device__ __forceinline__ void k3_body(const unsigned* __restrict__ x32, unsigned char* ws,
                                        unsigned short* __restrict__ out, unsigned char* smem, int w) {
  const int b = blockIdx.x;
  const int g = (b & 7) + 8 * (b >> 6), jj = (b >> 3) & 7;
  const int j = 8 * g + jj;
  const int tid = threadIdx.x, l = tid & 63;
  const __amdgpu_buffer_rsrc_t rs = ws_rsrc(ws);
  f2* lds = (f2*)(smem + L_STG);
  u4 wv[32], uv[28];
  constexpr bool FW = CLS == 0;
  const int wo = (int)OFF_W1F + ((7 * jj + w) * 2048 + l) * 16;
  const int uo = (int)OFF_W2F + ((jj * 56 + 7 * w) * 256 + l) * 16;
  constexpr int HAVE = FW ? F_W2A : (CLS == 1 ? W2B : 28);
  if (FW) {
    u4 tin[4];
#pragma unroll
    for (int q = 0; q < 4; ++q) tin[q] = __builtin_amdgcn_raw_buffer_load_b128(rs, (int)OFF_T + (j * 256 + tid) * 64 + q * 16, 0, 0);
    unsigned xr[2][4];
#pragma unroll
    for (int u = 0; u < 2; ++u)
#pragma unroll
      for (int i = 0; i < 4; ++i) xr[u][i] = x32[(size_t)(j + 256 * (2 * w + u)) * 256 + l + 64 * i];
    u4 mk[3];
#pragma unroll
    for (int i = 0; i < 3; ++i) mk[i] = ((const u4*)(ws + OFF_MODK3))[l * 3 + i];
    SCHED_FENCE();
    const int kl = (l >> 4) + 4 * ((l >> 2) & 3) + 16 * (l & 3);
    Tw256 tw;
    make_tw256(tw, l);
    tw.x2a += 2 * w * ROWP; tw.x2b += 2 * w * ROWP; tw.x3a += 2 * w * ROWP; tw.x3b += 2 * w * ROWP;
    f2 t512[4];
    int zk[4], zn[4];
#pragma unroll
    for (int i = 0; i < 4; ++i) {
      t512[i] = tw2048(-4 * (kl + 64 * i) - 512);
      zk[i] = 2 * w * ROWP + zpos(kl + 64 * i);
      zn[i] = 2 * w * ROWP + zpos((256 - kl - 64 * i) & 255);
    }
    f2 twq[7];
#pragma unroll
    for (int q = 1; q < 8; ++q) twq[q - 1] = tw2048(j * q);
    int zc = zpos(tid);
    asm volatile("" : "+v"(twq[0]), "+v"(twq[1]), "+v"(twq[2]), "+v"(twq[3]), "+v"(twq[4]), "+v"(twq[5]), "+v"(twq[6]));
    asm volatile("" : "+v"(t512[0]), "+v"(t512[1]), "+v"(t512[2]), "+v"(t512[3]), "+v"(zc));
    asm volatile("" : "+v"(zk[0]), "+v"(zk[1]), "+v"(zk[2]), "+v"(zk[3]), "+v"(zn[0]), "+v"(zn[1]), "+v"(zn[2]), "+v"(zn[3]));
    PIN_TW(tw);
    SCHED_FENCE();
    {
      f2 a[8];
#pragma unroll
      for (int q = 0; q < 4; ++q) { const f4 c = __builtin_bit_cast(f4, tin[q]); a[2 * q] = c.xy; a[2 * q + 1] = c.zw; }
      cmul7<true>(a, twq);
      bfly8<true>(a);
#pragma unroll
      for (int m = 0; m < 8; ++m) lds[m * ROWP + zc] = a[m];
    }
    BAR();
    STAMP(3, 1);
    f2 v[2][4];
#pragma unroll
    for (int u = 0; u < 2; ++u) {
      f2 xk[4], xn[4];
#pragma unroll
      for (int r = 0; r < 4; ++r) { xk[r] = lds[zk[r] + u * ROWP]; xn[r] = lds[zn[r] + u * ROWP]; }
      herm2(v[u][0], v[u][1], xk[0], xn[0], t512[0], xk[1], xn[1], t512[1]);
      herm2(v[u][2], v[u][3], xk[2], xn[2], t512[2], xk[3], xn[3], t512[3]);
      if (l == 0) v[u][0] = f2{xk[0].x + xk[0].y, xk[0].x - xk[0].y};
    }
    __builtin_amdgcn_wave_barrier();
    fft256_inv2<ROWP>(v, tw, lds);
    unsigned sc1[4], sh[4];
    f2 ga[4];
#pragma unroll
    for (int i = 0; i < 4; ++i) {
      ga[i] = unpk(mk[(3 * i) >> 2][(3 * i) & 3]) * 4.76837158203125e-07f;
      sc1[i] = mk[(3 * i + 1) >> 2][(3 * i + 1) & 3];
      sh[i] = mk[(3 * i + 2) >> 2][(3 * i + 2) & 3];
    }
#pragma unroll
    for (int u = 0; u < 2; ++u) {
      f2 x1[4], h2[4];
      unsigned x1p[4];
#pragma unroll
      for (int i = 0; i < 4; ++i) {
        x1p[i] = pk(unpk(xr[u][i]) + ga[i] * v[u][i]);
        x1[i] = unpk(x1p[i]);
      }
      ln_mod(x1, sc1, sh, h2);
      unsigned* st = (unsigned*)(lds + (2 * w + u) * ROWP);
#pragma unroll
      for (int i = 0; i < 4; ++i) {
        st[l + 64 * i] = x1p[i];
        st[256 + l + 64 * i] = nosent(pk(h2[i]));
      }
    }
    BAR();
    STAMP(3, 2);
    __builtin_amdgcn_s_sleep(F_DELAY);
#pragma unroll
    for (int u = 0; u < 2; ++u)
      __builtin_amdgcn_raw_buffer_store_b128(*(const u4*)((const unsigned*)(lds + (2 * w + u) * ROWP) + 4 * l), rs,
                                             (int)OFF_X1 + ((64 * g + 8 * jj + 2 * w + u) * 64 + l) * 16, 0, AUX_SC1);
#pragma unroll
    for (int i = NPARK; i < 32; ++i) LDW(wv, i, wo);
#pragma unroll
    for (int i = 0; i < F_W2A; ++i) LDW(uv, i, uo);
#pragma unroll
    for (int i = 0; i < NPARK; ++i) wv[i] = *(const u4*)(smem + park_addr(i, w) + l * 16);
    STAMP(3, 3);
    BAR();
  } else {
    const int pw = w - 4;
    if (CLS == 1) {
#pragma unroll
      for (int i = 0; i < P_N1; ++i) LDW(wv, i, wo);
    } else {
      park<0, 2>(rs, smem, jj, l);
    }
    BAR();
    if (CLS == 1) {
#pragma unroll
      for (int i = P_N1; i < 32; ++i) LDW(wv, i, wo);
#pragma unroll
      for (int i = 0; i < P_W2A; ++i) LDW(uv, i, uo);
    } else {
      park<2, 4>(rs, smem, jj, l);
#pragma unroll
      for (int i = 0; i < 28; ++i) LDW(uv, i, uo);
    }
    BAR();
    __builtin_amdgcn_s_setprio(3);
    {
      u4 ho[2];
#pragma unroll
      for (int u = 0; u < 2; ++u) {
        const int ch = 8 * (2 * pw + u) + (l >> 3);
        ho[u] = *(const u4*)((const unsigned*)(lds + (l & 7) * ROWP) + 256 + 4 * ch);
      }
      const int ha = (int)OFF_H2F + (((g * 2 + (jj >> 2)) * 32 + 8 * pw + (l >> 4)) * 64 + 8 * (jj & 3) + (l & 7) + 32 * ((l >> 3) & 1)) * 16;
#pragma unroll
      for (int u = 0; u < 2; ++u) __builtin_amdgcn_raw_buffer_store_b128(ho[u], rs, ha + u * 4096, 0, AUX_SC1);
    }
    SCHED_FENCE();
    {
      const int hg = (int)OFF_H2F + g * 65536, hoff = (pw * 1024 + l) * 16;
      u4 hv[16];
      __builtin_amdgcn_s_sleep(P_DELAY);
      asm volatile("" ::: "memory");
#pragma unroll
      for (int i = 0; i < 16; ++i) hv[i] = __builtin_amdgcn_raw_buffer_load_b128(rs, hg + hoff + i * 1024, 0, AUX_SC1);
      STAMPT(3, 4, 256);
      reload_until_written<16>(hv, rs, hg + hoff, 1024);
#pragma unroll
      for (int i = 0; i < 16; ++i) *(u4*)(smem + L_H2 + hoff + i * 1024) = hv[i];
    }
    if (CLS == 1) {
      __builtin_amdgcn_s_setprio(P_PRIO);
      SCHED_FENCE();
#pragma unroll
      for (int i = P_W2A; i < W2B; ++i) LDW(uv, i, uo);
    }
    STAMPT(3, 8, 256);
    BAR();
  }
  STAMP(3, 5); STAMPT(4, 5, 256);
  const int aro = (int)OFF_AF + ((g * 8 + w) * 28 * 64 + l) * 16;
  u4 av[28];
#define LDA(i) av[i] = __builtin_amdgcn_raw_buffer_load_b128(rs, aro + (i) * 1024, 0, AUX_SC1)
  if (CLS != 2) {
    const u4* hs = (const u4*)(smem + L_H2);
    u4 hb[4];
    const int afo = (int)OFF_AF + (((g * 8 + jj) * 28 + 2 * w) * 64 + l) * 16;
#pragma unroll
    for (int st = 0; st < 3; ++st) hb[st] = hs[st * 64 + l];
#pragma unroll
    for (int th = 0; th < 2; ++th) {
      f16v acc;
#pragma unroll
      for (int i = 0; i < 16; ++i) acc[i] = 0.f;
      if (th == 0) __builtin_amdgcn_s_setprio(3); else __builtin_amdgcn_s_setprio(2);
#pragma unroll
      for (int ks = 0; ks < 32; ++ks) {
        const int st = th * 32 + ks;
        if (st + 3 < 64) hb[(st + 3) & 3] = hs[(st + 3) * 64 + l];
        if (w2_at<HAVE>(st) >= 0) LDW(uv, w2_at<HAVE>(st) < 0 ? 0 : w2_at<HAVE>(st), uo);
        SCHED_FENCE();
        acc = mfma32(wv[ks], hb[st & 3], acc);
        SCHED_FENCE();
      }
      if (FW && th == 0) asm volatile("s_waitcnt vmcnt(%0)" :: "i"(W2B - HAVE) : "memory");
#pragma unroll
      for (int sk = 0; sk < 2; ++sk) {
        u4 o;
#pragma unroll
        for (int e = 0; e < 4; ++e) o[e] = nosent(pk(f2{silu_f(acc[8 * sk + 2 * e]), silu_f(acc[8 * sk + 2 * e + 1])}));
        __builtin_amdgcn_raw_buffer_store_b128(o, rs, afo + (th * 14 + sk) * 1024, 0, AUX_SC1);
      }
      SCHED_FENCE();
      if (th == 0) { __builtin_amdgcn_s_barrier(); SCHED_FENCE(); }
    }
    __builtin_amdgcn_s_setprio(0);
    STAMP(3, 6); STAMPT(4, 6, 256);
  } else {
    __builtin_amdgcn_s_setprio(0);
    __builtin_amdgcn_s_barrier();
    __builtin_amdgcn_s_sleep(S_DELAY);
  }
  asm volatile("" ::: "memory");
  __builtin_amdgcn_s_setprio(3);
#pragma unroll
  for (int i = 0; i < (FW ? 14 : 28); ++i) LDA(i);
  __builtin_amdgcn_s_setprio(0);
  SCHED_FENCE();
  const int thf = w >> 2, nbf = (w >> 1) & 1, hff = w & 1;
  const int tok = 32 * thf + (l & 31);
  const int col = 64 * jj + 32 * nbf + 16 * (l >> 5) + 8 * hff;
  u4 x1v, gbv;
#pragma unroll
  for (int th = 0; th < 2; ++th) {
    if (th == 1) {
      BAR();
      STAMP(3, 7); STAMPT(4, 7, 256);
      x1v = __builtin_amdgcn_raw_buffer_load_b128(rs, (int)OFF_X1 + ((64 * g + tok) * 64 + (col >> 3)) * 16, 0, AUX_SC1);
      gbv = __builtin_amdgcn_raw_buffer_load_b128(rs, (int)OFF_GB + col * 2, 0, 0);
      SCHED_FENCE();
    }
    reload_until_written<14>(av + th * 14, rs, aro + th * 14 * 1024, 1024);
    SCHED_FENCE();
    if (th == 0) { STAMP(3, 12); STAMPT(4, 12, 256); }
    f4* red = (f4*)(smem + (th == 0 ? L_R01 : L_H2)) + w * 512 + l;
#pragma unroll
    for (int nb = 0; nb < 2; ++nb) {
      f16v acc;
#pragma unroll
      for (int i = 0; i < 16; ++i) acc[i] = 0.f;
#pragma unroll
      for (int k = 0; k < 14; ++k) acc = mfma32(uv[2 * k + nb], av[th * 14 + k], acc);
      if (FW && th == 0 && nb == F_L1NB) {
        SCHED_FENCE();
        __builtin_amdgcn_s_setprio(3);
#pragma unroll
        for (int i = 14; i < 28; ++i) LDA(i);
        __builtin_amdgcn_s_setprio(0);
        SCHED_FENCE();
      }
#pragma unroll
      for (int q4 = 0; q4 < 4; ++q4)
        red[(nb * 4 + q4) * 64] = f4{acc[4 * q4], acc[4 * q4 + 1], acc[4 * q4 + 2], acc[4 * q4 + 3]};
      SCHED_FENCE();
    }
  }
  STAMP(3, 9); STAMPT(4, 9, 256);
  BAR();
  f4 s0 = f4{0.f, 0.f, 0.f, 0.f}, s1 = s0;
  {
    const f4* red = (const f4*)(smem + (thf == 0 ? L_R01 : L_H2)) + (nbf * 4 + 2 * hff) * 64 + l;
#pragma unroll
    for (int ww = 0; ww < 8; ++ww) {
      s0 += red[ww * 512];
      s1 += red[ww * 512 + 64];
    }
  }
  u4 o;
  o[0] = pk(unpk(x1v[0]) + unpk(gbv[0]) * s0.xy);
  o[1] = pk(unpk(x1v[1]) + unpk(gbv[1]) * s0.zw);
  o[2] = pk(unpk(x1v[2]) + unpk(gbv[2]) * s1.xy);
  o[3] = pk(unpk(x1v[3]) + unpk(gbv[3]) * s1.zw);
  const int srow = 8 * g + (tok >> 3) + 256 * (tok & 7);
  *(u4*)(out + (size_t)srow * D + col) = o;
  STAMP(3, 10); STAMPT(4, 10, 256);
  STAMP_DRAIN();
  STAMP(3, 11); STAMPT(4, 11, 256);
}

__global__ __launch_bounds__(512, 2) void k3_fused(const unsigned* __restrict__ x32, unsigned char* ws,
                                                   unsigned short* __restrict__ out) {
  extern __shared__ __attribute__((aligned(16))) unsigned char smem[];
  const int w = __builtin_amdgcn_readfirstlane(threadIdx.x >> 6);
  STAMP(3, 0);
  if (w < 4) k3_body<0>(x32, ws, out, smem, w);
  else if (w < 7) k3_body<1>(x32, ws, out, smem, w);
  else k3_body<2>(x32, ws, out, smem, w);
}

}

extern "C" void kernel_launch(void* const* d_in, const int* in_sizes, int n_in, void* d_out, int out_size,
                              void* d_ws, size_t ws_size, hipStream_t stream) {
  const unsigned short* x = (const unsigned short*)d_in[0];
  const unsigned short* wr = (const unsigned short*)d_in[1];
  const unsigned short* wi = (const unsigned short*)d_in[2];
  const unsigned short* w1 = (const unsigned short*)d_in[4];
  const unsigned short* w2 = (const unsigned short*)d_in[5];
  const unsigned short* cv = (const unsigned short*)d_in[6];
  const unsigned short* wm = (const unsigned short*)d_in[7];
  unsigned char* ws = (unsigned char*)d_ws;
  (void)hipFuncSetAttribute((const void*)k3_fused, hipFuncAttributeMaxDynamicSharedMemorySize, LDS3_BYTES);
  k0_prep<<<K0_BLOCKS, 256, 0, stream>>>(cv, wm, wr, wi, ws);
  k1_rows<<<256, 512, 0, stream>>>((const unsigned*)x, w1, w2, ws);
  k2_cols<<<256, 256 + 64 * K2_TOUCH, 0, stream>>>(ws);
  k3_fused<<<256, 512, LDS3_BYTES, stream>>>((const unsigned*)x, ws, (unsigned short*)d_out);
}
```

```cpp
#include <hip/hip_runtime.h>
#include <hip/hip_bf16.h>
#include <stdint.h>
#include <type_traits>

namespace {

typedef float f2 __attribute__((ext_vector_type(2)));
typedef float f4 __attribute__((ext_vector_type(4)));
typedef unsigned u2 __attribute__((ext_vector_type(2)));
typedef unsigned u4 __attribute__((ext_vector_type(4)));
typedef short s8 __attribute__((ext_vector_type(8)));
typedef __bf16 bf2 __attribute__((ext_vector_type(2)));
#define GAS __attribute__((address_space(1)))
typedef GAS unsigned gu32;
#define RLX_AGENT __ATOMIC_RELAXED, __HIP_MEMORY_SCOPE_AGENT
#define SCHED_FENCE() __builtin_amdgcn_sched_barrier(0)

constexpr int S = 2048, D = 512, DF = 257, FF = 1792;

constexpr size_t OFF_F2    = 0;
constexpr size_t OFF_MODK1 = 262144;
constexpr size_t OFF_MODK3 = OFF_MODK1 + 2048;
constexpr size_t OFF_GB    = OFF_MODK3 + 3072;
constexpr size_t OFF_PQ    = OFF_MODK1 + 8192;
constexpr size_t OFF_WF    = OFF_PQ + 32768;
constexpr size_t OFF_T     = OFF_WF + (size_t)256 * 2048 * 4;
constexpr size_t OFF_H2F   = OFF_T + (size_t)256 * 2048 * 8;
constexpr size_t OFF_X1    = OFF_H2F + (size_t)S * D * 2;
constexpr size_t OFF_AF    = OFF_X1 + (size_t)S * D * 2;
constexpr size_t OFF_W1F   = OFF_AF + (size_t)S * FF * 2;
constexpr size_t OFF_W2F   = OFF_W1F + (size_t)D * FF * 2;
constexpr size_t WS_BYTES  = OFF_W2F + (size_t)D * FF * 2;

#define STAMP(kid, i) do {} while (0)
#define STAMPT(kid, i, thr) do {} while (0)
#define STAMP_DRAIN() do {} while (0)

__device__ __forceinline__ float bflo(unsigned u) { return __builtin_bit_cast(float, u << 16); }
__device__ __forceinline__ float bfhi(unsigned u) { return __builtin_bit_cast(float, u & 0xffff0000u); }
__device__ __forceinline__ f2 unpk(unsigned u) { return f2{bflo(u), bfhi(u)}; }
__device__ __forceinline__ unsigned pk(f2 v) {
  bf2 b = __builtin_convertvector(v, bf2);
  return __builtin_bit_cast(unsigned, b);
}
__device__ __forceinline__ f2 rb2(f2 v) { return unpk(pk(v)); }
__device__ __forceinline__ float rb1(float v) { return bflo(pk(f2{v, v})); }

__device__ __forceinline__ f2 tw2048(int t) {
  const float f = (float)t * (1.0f / 2048.0f);
  return f2{__builtin_amdgcn_cosf(f), -__builtin_amdgcn_sinf(f)};
}

__device__ __forceinline__ f2 cmul(f2 a, f2 w) {
  f2 t, d;
  asm("v_pk_mul_f32 %0, %1, %2 op_sel:[1,1] op_sel_hi:[1,0] neg_lo:[0,1]" : "=v"(t) : "v"(a), "v"(w));
  asm("v_pk_fma_f32 %0, %1, %2, %3 op_sel:[0,0,0] op_sel_hi:[0,1,1]" : "=v"(d) : "v"(a), "v"(w), "v"(t));
  return d;
}
__device__ __forceinline__ f2 cmulc(f2 a, f2 w) {
  f2 t, d;
  asm("v_pk_mul_f32 %0, %1, %2 op_sel:[0,0] op_sel_hi:[1,0]" : "=v"(t) : "v"(a), "v"(w));
  asm("v_pk_fma_f32 %0, %1, %2, %3 op_sel:[1,1,0] op_sel_hi:[0,1,1] neg_hi:[1,0,0]" : "=v"(d) : "v"(a), "v"(w), "v"(t));
  return d;
}
template <bool CONJ> __device__ __forceinline__ void cmul3(f2& a0, f2& a1, f2& a2, f2 w0, f2 w1, f2 w2) {
  f2 t0, t1, t2;
  if (!CONJ) {
    asm("v_pk_mul_f32 %3, %0, %6 op_sel:[1,1] op_sel_hi:[1,0] neg_lo:[0,1]\n\t"
        "v_pk_mul_f32 %4, %1, %7 op_sel:[1,1] op_sel_hi:[1,0] neg_lo:[0,1]\n\t"
        "v_pk_mul_f32 %5, %2, %8 op_sel:[1,1] op_sel_hi:[1,0] neg_lo:[0,1]\n\t"
        "v_pk_fma_f32 %0, %0, %6, %3 op_sel:[0,0,0] op_sel_hi:[0,1,1]\n\t"
        "v_pk_fma_f32 %1, %1, %7, %4 op_sel:[0,0,0] op_sel_hi:[0,1,1]\n\t"
        "v_pk_fma_f32 %2, %2, %8, %5 op_sel:[0,0,0] op_sel_hi:[0,1,1]"
        : "+v"(a0), "+v"(a1), "+v"(a2), "=&v"(t0), "=&v"(t1), "=&v"(t2) : "v"(w0), "v"(w1), "v"(w2));
  } else {
    asm("v_pk_mul_f32 %3, %0, %6 op_sel:[0,0] op_sel_hi:[1,0]\n\t"
        "v_pk_mul_f32 %4, %1, %7 op_sel:[0,0] op_sel_hi:[1,0]\n\t"
        "v_pk_mul_f32 %5, %2, %8 op_sel:[0,0] op_sel_hi:[1,0]\n\t"
        "v_pk_fma_f32 %0, %0, %6, %3 op_sel:[1,1,0] op_sel_hi:[0,1,1] neg_hi:[1,0,0]\n\t"
        "v_pk_fma_f32 %1, %1, %7, %4 op_sel:[1,1,0] op_sel_hi:[0,1,1] neg_hi:[1,0,0]\n\t"
        "v_pk_fma_f32 %2, %2, %8, %5 op_sel:[1,1,0] op_sel_hi:[0,1,1] neg_hi:[1,0,0]"
        : "+v"(a0), "+v"(a1), "+v"(a2), "=&v"(t0), "=&v"(t1), "=&v"(t2) : "v"(w0), "v"(w1), "v"(w2));
  }
}
template <bool CONJ> __device__ __forceinline__ void cmul4(f2& a0, f2& a1, f2& a2, f2& a3, f2 w0, f2 w1, f2 w2, f2 w3) {
  f2 t0, t1, t2, t3;
  if (!CONJ) {
    asm("v_pk_mul_f32 %4, %0, %8 op_sel:[1,1] op_sel_hi:[1,0] neg_lo:[0,1]\n\t"
        "v_pk_mul_f32 %5, %1, %9 op_sel:[1,1] op_sel_hi:[1,0] neg_lo:[0,1]\n\t"
        "v_pk_mul_f32 %6, %2, %10 op_sel:[1,1] op_sel_hi:[1,0] neg_lo:[0,1]\n\t"
        "v_pk_mul_f32 %7, %3, %11 op_sel:[1,1] op_sel_hi:[1,0] neg_lo:[0,1]\n\t"
        "v_pk_fma_f32 %0, %0, %8, %4 op_sel:[0,0,0] op_sel_hi:[0,1,1]\n\t"
        "v_pk_fma_f32 %1, %1, %9, %5 op_sel:[0,0,0] op_sel_hi:[0,1,1]\n\t"
        "v_pk_fma_f32 %2, %2, %10, %6 op_sel:[0,0,0] op_sel_hi:[0,1,1]\n\t"
        "v_pk_fma_f32 %3, %3, %11, %7 op_sel:[0,0,0] op_sel_hi:[0,1,1]"
        : "+v"(a0), "+v"(a1), "+v"(a2), "+v"(a3), "=&v"(t0), "=&v"(t1), "=&v"(t2), "=&v"(t3)
        : "v"(w0), "v"(w1), "v"(w2), "v"(w3));
  } else {
    asm("v_pk_mul_f32 %4, %0, %8 op_sel:[0,0] op_sel_hi:[1,0]\n\t"
        "v_pk_mul_f32 %5, %1, %9 op_sel:[0,0] op_sel_hi:[1,0]\n\t"
        "v_pk_mul_f32 %6, %2, %10 op_sel:[0,0] op_sel_hi:[1,0]\n\t"
        "v_pk_mul_f32 %7, %3, %11 op_sel:[0,0] op_sel_hi:[1,0]\n\t"
        "v_pk_fma_f32 %0, %0, %8, %4 op_sel:[1,1,0] op_sel_hi:[0,1,1] neg_hi:[1,0,0]\n\t"
        "v_pk_fma_f32 %1, %1, %9, %5 op_sel:[1,1,0] op_sel_hi:[0,1,1] neg_hi:[1,0,0]\n\t"
        "v_pk_fma_f32 %2, %2, %10, %6 op_sel:[1,1,0] op_sel_hi:[0,1,1] neg_hi:[1,0,0]\n\t"
        "v_pk_fma_f32 %3, %3, %11, %7 op_sel:[1,1,0] op_sel_hi:[0,1,1] neg_hi:[1,0,0]"
        : "+v"(a0), "+v"(a1), "+v"(a2), "+v"(a3), "=&v"(t0), "=&v"(t1), "=&v"(t2), "=&v"(t3)
        : "v"(w0), "v"(w1), "v"(w2), "v"(w3));
  }
}
template <bool CONJ> __device__ __forceinline__ void cmul7(f2 (&a)[8], const f2 (&w)[7]) {
  cmul3<CONJ>(a[1], a[2], a[3], w[0], w[1], w[2]);
  cmul4<CONJ>(a[4], a[5], a[6], a[7], w[3], w[4], w[5], w[6]);
}
__device__ __forceinline__ void herm2(f2& r0, f2& r1, f2 z0, f2 q0, f2 w0, f2 z1, f2 q1, f2 w1) {
  f2 d0, d1;
  asm("v_pk_add_f32 %0, %4, %5 neg_hi:[0,1]\n\t"
      "v_pk_add_f32 %2, %4, %5 neg_lo:[0,1]\n\t"
      "v_pk_add_f32 %1, %7, %8 neg_hi:[0,1]\n\t"
      "v_pk_add_f32 %3, %7, %8 neg_lo:[0,1]\n\t"
      "v_pk_fma_f32 %0, %2, %6, %0 op_sel:[1,1,0] op_sel_hi:[1,0,1] neg_lo:[0,1,0]\n\t"
      "v_pk_fma_f32 %1, %3, %9, %1 op_sel:[1,1,0] op_sel_hi:[1,0,1] neg_lo:[0,1,0]\n\t"
      "v_pk_fma_f32 %0, %2, %6, %0 op_sel:[0,0,0] op_sel_hi:[0,1,1]\n\t"
      "v_pk_fma_f32 %1, %3, %9, %1 op_sel:[0,0,0] op_sel_hi:[0,1,1]"
      : "=&v"(r0), "=&v"(r1), "=&v"(d0), "=&v"(d1) : "v"(z0), "v"(q0), "v"(w0), "v"(z1), "v"(q1), "v"(w1));
}
__device__ __forceinline__ f2 cfma(f2 a, f2 w, f2 c) {
  f2 t, d;
  asm("v_pk_fma_f32 %0, %1, %2, %3 op_sel:[1,1,0] op_sel_hi:[1,0,1] neg_lo:[0,1,0]" : "=v"(t) : "v"(a), "v"(w), "v"(c));
  asm("v_pk_fma_f32 %0, %1, %2, %3 op_sel:[0,0,0] op_sel_hi:[0,1,1]" : "=v"(d) : "v"(a), "v"(w), "v"(t));
  return d;
}
__device__ __forceinline__ f2 add_mi(f2 a, f2 b) {
  f2 d;
  asm("v_pk_add_f32 %0, %1, %2 op_sel:[0,1] op_sel_hi:[1,0] neg_hi:[0,1]" : "=v"(d) : "v"(a), "v"(b));
  return d;
}
__device__ __forceinline__ f2 add_pi(f2 a, f2 b) {
  f2 d;
  asm("v_pk_add_f32 %0, %1, %2 op_sel:[0,1] op_sel_hi:[1,0] neg_lo:[0,1]" : "=v"(d) : "v"(a), "v"(b));
  return d;
}
__device__ __forceinline__ f2 add_cj(f2 a, f2 b) {
  f2 d;
  asm("v_pk_add_f32 %0, %1, %2 neg_hi:[0,1]" : "=v"(d) : "v"(a), "v"(b));
  return d;
}
__device__ __forceinline__ f2 sub_cj(f2 a, f2 b) {
  f2 d;
  asm("v_pk_add_f32 %0, %1, %2 neg_lo:[0,1]" : "=v"(d) : "v"(a), "v"(b));
  return d;
}
template <bool INV> __device__ __forceinline__ f2 addr(f2 a, f2 b) { return INV ? add_pi(a, b) : add_mi(a, b); }
template <bool INV> __device__ __forceinline__ f2 subr(f2 a, f2 b) { return INV ? add_mi(a, b) : add_pi(a, b); }

template <bool INV> __device__ __forceinline__ void bfly4(f2& a0, f2& a1, f2& a2, f2& a3) {
  const f2 t0 = a0 + a2, t1 = a0 - a2, t2 = a1 + a3, u = a1 - a3;
  a0 = t0 + t2; a2 = t0 - t2; a1 = addr<INV>(t1, u); a3 = subr<INV>(t1, u);
}
template <bool INV> __device__ __forceinline__ void bfly8(f2 (&a)[8]) {
  constexpr float R = 0.70710678118654752440f;
  bfly4<INV>(a[0], a[2], a[4], a[6]);
  bfly4<INV>(a[1], a[3], a[5], a[7]);
  const f2 e0 = a[0], e1 = a[2], e2 = a[4], e3 = a[6];
  const f2 o0 = a[1], o2 = a[5];
  const f2 p1 = addr<INV>(a[3], a[3]);
  const f2 p3 = subr<INV>(a[7], a[7]);
  a[0] = e0 + o0; a[4] = e0 - o0;
  a[1] = e1 + R * p1; a[5] = e1 - R * p1;
  a[2] = addr<INV>(e2, o2); a[6] = subr<INV>(e2, o2);
  a[3] = e3 - R * p3; a[7] = e3 + R * p3;
}
#define XL_PAD4(v) asm volatile("s_nop 1" : "+v"(v[0]), "+v"(v[1]), "+v"(v[2]), "+v"(v[3]))
#define XL_PAD8(v) asm volatile("s_nop 1" : "+v"(v[0]), "+v"(v[1]), "+v"(v[2]), "+v"(v[3]), "+v"(v[4]), "+v"(v[5]), "+v"(v[6]), "+v"(v[7]))

__device__ __forceinline__ void swap32(f2& a, f2& b) {
  const u2 au = __builtin_bit_cast(u2, a), bu = __builtin_bit_cast(u2, b);
  const unsigned ax = au[0], ay = au[1], bx = bu[0], by = bu[1];
  auto rx = __builtin_amdgcn_permlane32_swap(ax, bx, false, false);
  auto ry = __builtin_amdgcn_permlane32_swap(ay, by, false, false);
  a = __builtin_bit_cast(f2, u2{(unsigned)rx[0], (unsigned)ry[0]});
  b = __builtin_bit_cast(f2, u2{(unsigned)rx[1], (unsigned)ry[1]});
}
__device__ __forceinline__ void swap16(f2& a, f2& b) {
  const u2 au = __builtin_bit_cast(u2, a), bu = __builtin_bit_cast(u2, b);
  const unsigned ax = au[0], ay = au[1], bx = bu[0], by = bu[1];
  auto rx = __builtin_amdgcn_permlane16_swap(ax, bx, false, false);
  auto ry = __builtin_amdgcn_permlane16_swap(ay, by, false, false);
  a = __builtin_bit_cast(f2, u2{(unsigned)rx[0], (unsigned)ry[0]});
  b = __builtin_bit_cast(f2, u2{(unsigned)rx[1], (unsigned)ry[1]});
}
__device__ __forceinline__ void swap8(f2& a, f2& b) {
  const u2 au = __builtin_bit_cast(u2, a), bu = __builtin_bit_cast(u2, b);
  const int ax = au[0], ay = au[1], bx = bu[0], by = bu[1];
  const int nax = __builtin_amdgcn_update_dpp(ax, bx, 0x128, 0xf, 0xc, false);
  const int nbx = __builtin_amdgcn_update_dpp(bx, ax, 0x128, 0xf, 0x3, false);
  const int nay = __builtin_amdgcn_update_dpp(ay, by, 0x128, 0xf, 0xc, false);
  const int nby = __builtin_amdgcn_update_dpp(by, ay, 0x128, 0xf, 0x3, false);
  a = __builtin_bit_cast(f2, u2{(unsigned)nax, (unsigned)nay});
  b = __builtin_bit_cast(f2, u2{(unsigned)nbx, (unsigned)nby});
}

template <int CTRL> __device__ __forceinline__ float dppmov(float v) {
  return __builtin_bit_cast(float, __builtin_amdgcn_update_dpp(0, __builtin_bit_cast(int, v), CTRL, 0xf, 0xf, true));
}
__device__ __forceinline__ float wave_sum(float v) {
  v += dppmov<0xB1>(v);
  v += dppmov<0x4E>(v);
  v += dppmov<0x141>(v);
  v += dppmov<0x140>(v);
  {
    unsigned u = __builtin_bit_cast(unsigned, v);
    auto r = __builtin_amdgcn_permlane16_swap(u, u, false, false);
    v = __builtin_bit_cast(float, (unsigned)r[0]) + __builtin_bit_cast(float, (unsigned)r[1]);
  }
  {
    unsigned u = __builtin_bit_cast(unsigned, v);
    auto r = __builtin_amdgcn_permlane32_swap(u, u, false, false);
    v = __builtin_bit_cast(float, (unsigned)r[0]) + __builtin_bit_cast(float, (unsigned)r[1]);
  }
  return v;
}

__device__ __forceinline__ f4 mfma(u4 a, u4 b, f4 c) {
  return __builtin_amdgcn_mfma_f32_16x16x32_bf16(__builtin_bit_cast(s8, a), __builtin_bit_cast(s8, b), c, 0, 0, 0);
}
typedef float f16v __attribute__((ext_vector_type(16)));
__device__ __forceinline__ f16v mfma32(u4 a, u4 b, f16v c) {
  return __builtin_amdgcn_mfma_f32_32x32x16_bf16(__builtin_bit_cast(s8, a), __builtin_bit_cast(s8, b), c, 0, 0, 0);
}
__device__ __forceinline__ float silu_f(float v) {
  return v * __builtin_amdgcn_rcpf(1.0f + __expf(-v));
}

__device__ __forceinline__ void wave_sum2(float& a, float& b) {
  a += dppmov<0xB1>(a);  b += dppmov<0xB1>(b);
  a += dppmov<0x4E>(a);  b += dppmov<0x4E>(b);
  a += dppmov<0x141>(a); b += dppmov<0x141>(b);
  a += dppmov<0x140>(a); b += dppmov<0x140>(b);
  unsigned ua = __builtin_bit_cast(unsigned, a), ub = __builtin_bit_cast(unsigned, b);
  auto r = __builtin_amdgcn_permlane16_swap(ua, ub, false, false);
  float c = __builtin_bit_cast(float, (unsigned)r[0]) + __builtin_bit_cast(float, (unsigned)r[1]);
  unsigned uc = __builtin_bit_cast(unsigned, c);
  auto r2 = __builtin_amdgcn_permlane32_swap(uc, uc, false, false);
  c = __builtin_bit_cast(float, (unsigned)r2[0]) + __builtin_bit_cast(float, (unsigned)r2[1]);
  a = __builtin_bit_cast(float, __builtin_amdgcn_readlane(__builtin_bit_cast(int, c), 0));
  b = __builtin_bit_cast(float, __builtin_amdgcn_readlane(__builtin_bit_cast(int, c), 16));
}
__device__ __forceinline__ void ln_mod(const f2 (&xv)[4], const unsigned (&sc1)[4], const unsigned (&sh)[4], f2 (&h)[4]) {
  const float x00 = xv[0].x;
  const float piv = __builtin_bit_cast(float, __builtin_amdgcn_readfirstlane(__builtin_bit_cast(int, x00)));
  f2 d[4];
#pragma unroll
  for (int i = 0; i < 4; ++i) d[i] = xv[i] - piv;
  const f2 s2 = (d[0] + d[1]) + (d[2] + d[3]);
  const f2 q2 = (d[0] * d[0] + d[1] * d[1]) + (d[2] * d[2] + d[3] * d[3]);
  float s1 = s2.x + s2.y, sq = q2.x + q2.y;
  wave_sum2(s1, sq);
  const float dm = s1 * (1.0f / 512.0f);
  const float var = __builtin_fmaxf(sq * (1.0f / 512.0f) - dm * dm, 0.0f);
  const float mbp = rb1(piv + dm) - piv;
  const float sd = rb1(__builtin_amdgcn_sqrtf(rb1(rb1(var) + 1e-5f)));
  const float rs = __builtin_amdgcn_rcpf(sd);
#pragma unroll
  for (int i = 0; i < 4; ++i) h[i] = ((d[i] - mbp) * rs) * unpk(sc1[i]) + unpk(sh[i]);
}

struct Tw256 { f2 a[3], b[3], c[3]; int x2a, x2b, x3a, x3b; };
__device__ __forceinline__ void make_tw256(Tw256& t, int l) {
#pragma unroll
  for (int r = 1; r < 4; ++r) {
    t.a[r - 1] = tw2048(l * r * 8);
    t.b[r - 1] = tw2048((l & 15) * r * 32);
    t.c[r - 1] = tw2048((l & 3) * r * 128);
  }
  const int d2 = l >> 4, d1 = (l >> 2) & 3, d0 = l & 3;
  t.x2a = 80 * d2 + 4 * d1 + d0;
  t.x2b = 80 * d2 + 20 * d1 + d0;
  t.x3a = 80 * d2 + 4 * d1 + d0;
  t.x3b = 80 * d2 + 4 * d1 + 17 * d0;
}
#define PIN_TW(t) do { \
  asm volatile("" : "+v"(t.a[0]), "+v"(t.a[1]), "+v"(t.a[2]), "+v"(t.b[0]), "+v"(t.b[1]), "+v"(t.b[2]), "+v"(t.c[0]), "+v"(t.c[1]), "+v"(t.c[2])); \
  asm volatile("" : "+v"(t.x2a), "+v"(t.x2b), "+v"(t.x3a), "+v"(t.x3b)); } while (0)

__device__ __forceinline__ void fft256_fwd(f2 (&v)[4], const Tw256& t, f2* lds) {
  bfly4<false>(v[0], v[1], v[2], v[3]);
  cmul3<false>(v[1], v[2], v[3], t.a[0], t.a[1], t.a[2]);
  XL_PAD4(v);
  swap32(v[0], v[2]); swap32(v[1], v[3]);
  swap16(v[0], v[1]); swap16(v[2], v[3]);
  bfly4<false>(v[0], v[1], v[2], v[3]);
  cmul3<false>(v[1], v[2], v[3], t.b[0], t.b[1], t.b[2]);
#pragma unroll
  for (int r = 0; r < 4; ++r) lds[t.x2a + 20 * r] = v[r];
  __builtin_amdgcn_wave_barrier();
#pragma unroll
  for (int r = 0; r < 4; ++r) v[r] = lds[t.x2b + 4 * r];
  __builtin_amdgcn_wave_barrier();
  bfly4<false>(v[0], v[1], v[2], v[3]);
  cmul3<false>(v[1], v[2], v[3], t.c[0], t.c[1], t.c[2]);
#pragma unroll
  for (int r = 0; r < 4; ++r) lds[t.x3a + 17 * r] = v[r];
  __builtin_amdgcn_wave_barrier();
#pragma unroll
  for (int r = 0; r < 4; ++r) v[r] = lds[t.x3b + r];
  __builtin_amdgcn_wave_barrier();
  bfly4<false>(v[0], v[1], v[2], v[3]);
}
template <class H> __device__ __forceinline__ void fft256_inv(f2 (&v)[4], const Tw256& t, f2* lds, H hook) {
  bfly4<true>(v[0], v[1], v[2], v[3]);
#pragma unroll
  for (int r = 0; r < 4; ++r) lds[t.x3b + r] = v[r];
  __builtin_amdgcn_wave_barrier();
#pragma unroll
  for (int r = 0; r < 4; ++r) v[r] = lds[t.x3a + 17 * r];
  __builtin_amdgcn_wave_barrier();
  hook(std::integral_constant<int, 0>{});
  cmul3<true>(v[1], v[2], v[3], t.c[0], t.c[1], t.c[2]);
  bfly4<true>(v[0], v[1], v[2], v[3]);
#pragma unroll
  for (int r = 0; r < 4; ++r) lds[t.x2b + 4 * r] = v[r];
  __builtin_amdgcn_wave_barrier();
#pragma unroll
  for (int r = 0; r < 4; ++r) v[r] = lds[t.x2a + 20 * r];
  __builtin_amdgcn_wave_barrier();
  hook(std::integral_constant<int, 1>{});
  cmul3<true>(v[1], v[2], v[3], t.b[0], t.b[1], t.b[2]);
  bfly4<true>(v[0], v[1], v[2], v[3]);
  XL_PAD4(v);
  swap16(v[0], v[1]); swap16(v[2], v[3]);
  swap32(v[0], v[2]); swap32(v[1], v[3]);
  hook(std::integral_constant<int, 2>{});
  cmul3<true>(v[1], v[2], v[3], t.a[0], t.a[1], t.a[2]);
  bfly4<true>(v[0], v[1], v[2], v[3]);
}
template <int RP> __device__ __forceinline__ void fft256_inv2(f2 (&v)[2][4], const Tw256& t, f2* lds) {
#pragma unroll
  for (int u = 0; u < 2; ++u) bfly4<true>(v[u][0], v[u][1], v[u][2], v[u][3]);
#pragma unroll
  for (int u = 0; u < 2; ++u)
#pragma unroll
    for (int r = 0; r < 4; ++r) lds[t.x3b + u * RP + r] = v[u][r];
  __builtin_amdgcn_wave_barrier();
#pragma unroll
  for (int u = 0; u < 2; ++u)
#pragma unroll
    for (int r = 0; r < 4; ++r) v[u][r] = lds[t.x3a + u * RP + 17 * r];
  __builtin_amdgcn_wave_barrier();
#pragma unroll
  for (int u = 0; u < 2; ++u) {
    cmul3<true>(v[u][1], v[u][2], v[u][3], t.c[0], t.c[1], t.c[2]);
    bfly4<true>(v[u][0], v[u][1], v[u][2], v[u][3]);
#pragma unroll
    for (int r = 0; r < 4; ++r) lds[t.x2b + u * RP + 4 * r] = v[u][r];
  }
  __builtin_amdgcn_wave_barrier();
#pragma unroll
  for (int u = 0; u < 2; ++u)
#pragma unroll
    for (int r = 0; r < 4; ++r) v[u][r] = lds[t.x2a + u * RP + 20 * r];
  __builtin_amdgcn_wave_barrier();
#pragma unroll
  for (int u = 0; u < 2; ++u) {
    cmul3<true>(v[u][1], v[u][2], v[u][3], t.b[0], t.b[1], t.b[2]);
    bfly4<true>(v[u][0], v[u][1], v[u][2], v[u][3]);
  }
  XL_PAD4(v[0]); XL_PAD4(v[1]);
#pragma unroll
  for (int u = 0; u < 2; ++u) {
    swap16(v[u][0], v[u][1]); swap16(v[u][2], v[u][3]);
    swap32(v[u][0], v[u][2]); swap32(v[u][1], v[u][3]);
  }
#pragma unroll
  for (int u = 0; u < 2; ++u) {
    cmul3<true>(v[u][1], v[u][2], v[u][3], t.a[0], t.a[1], t.a[2]);
    bfly4<true>(v[u][0], v[u][1], v[u][2], v[u][3]);
  }
}
__device__ __forceinline__ int zpos(int k) { return k + (k >> 4); }

__device__ __host__ __forceinline__ int k2_k1(int tt, int kb) {
  int seq = (tt >> 6) * 8 + (tt & 7);
  int q = seq >> 2, k0 = seq & 3, ka = (tt >> 3) & 7;
  return 8 * (k0 + 4 * ka + 32 * kb) + q;
}

__device__ __forceinline__ void transpose8x8(const u4 (&in)[8], u4 (&out)[8]) {
#pragma unroll
  for (int c2 = 0; c2 < 4; ++c2) {
#pragma unroll
    for (int r2 = 0; r2 < 4; ++r2) {
      unsigned a = in[2 * r2][c2], b = in[2 * r2 + 1][c2];
      out[2 * c2][r2] = __builtin_amdgcn_perm(b, a, 0x05040100u);
      out[2 * c2 + 1][r2] = __builtin_amdgcn_perm(b, a, 0x07060302u);
    }
  }
}

#define PREP_AUX 16
#define T_AUX 0
constexpr int AUX_SC1 = 16;
constexpr int OOB = 0x40000000;
__device__ __forceinline__ __amdgpu_buffer_rsrc_t ws_rsrc(unsigned char* ws) {
  return __builtin_amdgcn_make_buffer_rsrc((void*)ws, 0, (int)WS_BYTES, 0x00020000);
}

__device__ __forceinline__ int prep_w1_off(int job, int l, int r) {
  const int cg = job % 7, ks = job / 7, no = l & 31, h = l >> 5;
  return ((16 * ks + 8 * h + r) * FF + cg * 256 + no * 8) * 2;
}
__device__ __forceinline__ void prep_w1_fin(const u4 (&in)[8], __amdgpu_buffer_rsrc_t rs, u4* buf, int job, int l) {
  const int cg = job % 7, ks = job / 7, no = l & 31, h = l >> 5;
  u4 out[8];
  transpose8x8(in, out);
#pragma unroll
  for (int cc = 0; cc < 8; ++cc) buf[((no >> 2) * 64 + 8 * (no & 3) + 32 * h + cc) ^ (no & 7)] = out[cc];
  __builtin_amdgcn_wave_barrier();
#pragma unroll
  for (int f = 0; f < 8; ++f)
    __builtin_amdgcn_raw_buffer_store_b128(buf[(f * 64 + l) ^ ((4 * f + ((l >> 3) & 3)) & 7)], rs,
                                           (int)OFF_W1F + (((cg * 8 + f) * 32 + ks) * 64 + l) * 16, 0, PREP_AUX);
}
__device__ __forceinline__ int prep_w2_off(int job, int l, int e) {
  const int cg = job & 1, cs = job >> 1, no = l & 31, h = l >> 5;
  return ((16 * cs + 8 * (e >> 2) + 4 * h + (e & 3)) * D + cg * 256 + no * 8) * 2;
}
__device__ __forceinline__ void prep_w2_fin(const u4 (&in)[8], __amdgpu_buffer_rsrc_t rs, u4* buf, int job, int l) {
  const int cg = job & 1, cs = job >> 1, no = l & 31, h = l >> 5;
  u4 out[8];
  transpose8x8(in, out);
  const int base = (no >> 2) * 64 + 16 * (no & 1) + 4 * ((no >> 1) & 1) + 32 * h;
#pragma unroll
  for (int cc = 0; cc < 8; ++cc) buf[base + (cc & 3) + 8 * (cc >> 2)] = out[cc];
  __builtin_amdgcn_wave_barrier();
#pragma unroll
  for (int f = 0; f < 8; ++f)
    __builtin_amdgcn_raw_buffer_store_b128(buf[f * 64 + l], rs,
                                           (int)OFF_W2F + ((((cg * 4 + (f >> 1)) * 112 + cs) * 2 + (f & 1)) * 64 + l) * 16, 0, PREP_AUX);
}

constexpr int K0_MOD = 96, K0_WF = 128, K0_MISC = 32;
constexpr int K0_BLOCKS = K0_MOD + K0_WF + K0_MISC;
constexpr unsigned SENT = 0xFFFFFFFFu;
constexpr int FILL_PER_THREAD = (int)((OFF_X1 - OFF_H2F) / (256 * 256 * 16));
static_assert((size_t)FILL_PER_THREAD * 256 * 256 * 16 == OFF_X1 - OFF_H2F && K0_BLOCKS == 256, "fill covers the h2 hand-off buffer");
#define FILL_AUX 16

__global__ __launch_bounds__(256) void k0_prep(const unsigned short* __restrict__ cvec,
                                               const unsigned short* __restrict__ wmod,
                                               const unsigned short* __restrict__ wr,
                                               const unsigned short* __restrict__ wi,
                                               unsigned char* ws) {
  __shared__ float red[4][32];
  const int tid = threadIdx.x;
  int b = blockIdx.x;
  STAMP(0, 0);
  const __amdgpu_buffer_rsrc_t rs = ws_rsrc(ws);
  auto fill = [&]() {
#pragma unroll
    for (int i = 0; i < FILL_PER_THREAD; ++i)
      __builtin_amdgcn_raw_buffer_store_b128(u4{SENT, SENT, SENT, SENT}, rs,
                                             (int)OFF_H2F + ((blockIdx.x * FILL_PER_THREAD + i) * 256 + tid) * 16, 0, FILL_AUX);
  };
  if (b < K0_MOD) {
    const int co = tid & 3, rg = tid >> 2;
    u4 wv[8];
    unsigned short cs[8];
#pragma unroll
    for (int i = 0; i < 8; ++i) cs[i] = cvec[rg + 64 * i];
#pragma unroll
    for (int i = 0; i < 8; ++i)
      wv[i] = *(const u4*)(wmod + (size_t)(rg + 64 * i) * 3072 + b * 32 + co * 8);
    SCHED_FENCE();
    fill();
    SCHED_FENCE();
    f2 acc[4];
#pragma unroll
    for (int e = 0; e < 4; ++e) acc[e] = f2{0.f, 0.f};
#pragma unroll
    for (int i = 0; i < 8; ++i) {
      const float cv = bflo(cs[i]);
      const float sv = rb1(silu_f(cv));
#pragma unroll
      for (int e2 = 0; e2 < 4; ++e2) acc[e2] += sv * unpk(wv[i][e2]);
    }
    STAMP(0, 2);
#pragma unroll
    for (int e2 = 0; e2 < 4; ++e2) {
      float v0 = acc[e2].x, v1 = acc[e2].y;
      v0 += dppmov<0x124>(v0); v1 += dppmov<0x124>(v1);
      v0 += dppmov<0x128>(v0); v1 += dppmov<0x128>(v1);
      unsigned u0 = __builtin_bit_cast(unsigned, v0), u1 = __builtin_bit_cast(unsigned, v1);
      auto r = __builtin_amdgcn_permlane16_swap(u0, u1, false, false);
      float c = __builtin_bit_cast(float, (unsigned)r[0]) + __builtin_bit_cast(float, (unsigned)r[1]);
      unsigned uc = __builtin_bit_cast(unsigned, c);
      auto r2 = __builtin_amdgcn_permlane32_swap(uc, uc, false, false);
      c = __builtin_bit_cast(float, (unsigned)r2[0]) + __builtin_bit_cast(float, (unsigned)r2[1]);
      if ((tid & 0x2c) == 0) red[tid >> 6][co * 8 + e2 * 2 + ((tid >> 4) & 1)] = c;
    }
    __syncthreads();
    if (tid < 32) {
      float s = rb1((red[0][tid] + red[1][tid]) + (red[2][tid] + red[3][tid]));
      const int col = b * 32 + tid, p = col >> 9, e = col & 511;
      if (p == 1 || p == 4) s = rb1(s + 1.0f);
      const unsigned short sb = (unsigned short)(__builtin_bit_cast(unsigned, s) >> 16);
      const int n = e >> 1, l = n & 63, i = n >> 6, hf = e & 1;
      if (p < 2) ((unsigned short*)(ws + OFF_MODK1))[((l * 4 + i) * 2 + p) * 2 + hf] = sb;
      else if (p < 5) ((unsigned short*)(ws + OFF_MODK3))[((l * 4 + i) * 3 + (p == 2 ? 0 : (p == 4 ? 1 : 2))) * 2 + hf] = sb;
      else ((unsigned short*)(ws + OFF_GB))[e] = sb;
    }
    STAMP(0, 1);
    return;
  }
  b -= K0_MOD;
  if (b < K0_WF) {
    const int tp = (b & 7) * 16 + (b >> 3);
    const int k2 = tid;
    unsigned short vr[16], vi[16];
#pragma unroll
    for (int e = 0; e < 16; ++e) {
      const int k1 = k2_k1(2 * tp + (e >> 3), e & 7);
      vr[e] = wr[k1 * DF + k2];
      vi[e] = wi[k1 * DF + k2];
    }
    SCHED_FENCE();
    fill();
    SCHED_FENCE();
#pragma unroll
    for (int e4 = 0; e4 < 4; ++e4) {
      u4 o;
#pragma unroll
      for (int e = 0; e < 4; ++e) o[e] = (unsigned)vr[e4 * 4 + e] | ((unsigned)vi[e4 * 4 + e] << 16);
      __builtin_amdgcn_raw_buffer_store_b128(o, rs, (int)OFF_WF + (k2 * 512 + tp * 4 + e4) * 16, 0, 0);
    }
    STAMP(0, 3);
    STAMP_DRAIN();
    STAMP(0, 5);
    return;
  }
  b -= K0_WF;
  fill();
  if (tid < 64) {
    const int e = b * 64 + tid;
    const int tt = e & 255, kb = e >> 8;
    const int k1 = k2_k1(tt, kb), nk = (2048 - k1) & 2047;
    f2 a0 = f2{bflo(wr[k1 * DF]), bflo(wi[k1 * DF])};
    f2 a1 = f2{bflo(wr[nk * DF]), -bflo(wi[nk * DF])};
    f2 b0 = f2{bflo(wr[k1 * DF + 256]), bflo(wi[k1 * DF + 256])};
    f2 b1 = f2{bflo(wr[nk * DF + 256]), -bflo(wi[nk * DF + 256])};
    f2 w0h = (a0 + a1) * 0.5f, w256h = (b0 + b1) * 0.5f;
    f2 P = (w0h + w256h) * 0.5f, Q = (w0h - w256h) * 0.5f;
    ((f4*)(ws + OFF_PQ))[e] = f4{P.x, P.y, Q.x, Q.y};
    STAMP(0, 4);
  }
}

constexpr int ROWP = 322;

__global__ __launch_bounds__(640) void k1_rows(const unsigned* __restrict__ x32, const unsigned short* __restrict__ w1,
                                               const unsigned short* __restrict__ w2, unsigned char* ws) {
  __shared__ __attribute__((aligned(16))) f2 lds[8 * ROWP];
  __shared__ u4 pbuf[1024];
  const int j = blockIdx.x;
  const int tid = threadIdx.x, w = __builtin_amdgcn_readfirstlane(tid >> 6), l = tid & 63;
  const __amdgpu_buffer_rsrc_t rs = ws_rsrc(ws);
  STAMP(1, 0);
  if (w >= 8) {
    if (j < 224) {
      u4 pin[8];
      const unsigned char* src = (const unsigned char*)(w == 8 ? w1 : w2);
#pragma unroll
      for (int r = 0; r < 8; ++r) pin[r] = *(const u4*)(src + (w == 8 ? prep_w1_off(j, l, r) : prep_w2_off(j, l, r)));
      if (w == 8) prep_w1_fin(pin, rs, pbuf, j, l);
      else prep_w2_fin(pin, rs, pbuf + 512, j, l);
    }
    STAMPT(1, 5, 512);
    __builtin_amdgcn_s_barrier();
    STAMP_DRAIN();
    STAMPT(1, 6, 512);
    return;
  }
  const int s = j + 256 * w;
  unsigned xr[4], sc1[4], sh[4];
#pragma unroll
  for (int i = 0; i < 4; ++i) xr[i] = x32[(size_t)s * 256 + l + 64 * i];
  {
    const u4* mp = (const u4*)(ws + OFF_MODK1) + l * 2;
    const u4 m0 = mp[0], m1 = mp[1];
    sh[0] = m0[0]; sc1[0] = m0[1]; sh[1] = m0[2]; sc1[1] = m0[3];
    sh[2] = m1[0]; sc1[2] = m1[1]; sh[3] = m1[2]; sc1[3] = m1[3];
  }
#pragma unroll
  for (int i = 0; i < 2; ++i)
    __builtin_amdgcn_raw_buffer_store_b128(u4{SENT, SENT, SENT, SENT}, rs, w < 7 ? (int)OFF_AF + ((j * 14 + 2 * w + i) * 64 + l) * 16 : OOB, 0, FILL_AUX);
  SCHED_FENCE();
  Tw256 tw;
  make_tw256(tw, l);
  tw.x2a += w * ROWP; tw.x2b += w * ROWP; tw.x3a += w * ROWP; tw.x3b += w * ROWP;
  const int k2 = tid & 255;
  f2 t512 = tw2048(4 * k2 + 512);
  f2 twq[7];
#pragma unroll
  for (int q = 1; q < 8; ++q) twq[q - 1] = tw2048(j * q);
  int zw = w * ROWP + (l >> 4) + 4 * ((l >> 2) & 3) + 17 * (l & 3);
  int za = zpos(k2), zb = zpos((256 - k2) & 255);
  int to = (int)OFF_T + (j * 256 + k2) * 64;
  asm volatile("" : "+v"(twq[0]), "+v"(twq[1]), "+v"(twq[2]), "+v"(twq[3]), "+v"(twq[4]), "+v"(twq[5]), "+v"(twq[6]), "+v"(t512));
  asm volatile("" : "+v"(zw), "+v"(za), "+v"(zb), "+v"(to));
  PIN_TW(tw);
  SCHED_FENCE();
  f2 xv[4], v[4];
#pragma unroll
  for (int i = 0; i < 4; ++i) xv[i] = unpk(xr[i]);
  ln_mod(xv, sc1, sh, v);
  STAMP(1, 1);
  fft256_fwd(v, tw, lds);
  STAMP(1, 2);
#pragma unroll
  for (int r = 0; r < 4; ++r) lds[zw + 68 * r] = v[r];
  __syncthreads();
  if (w < 4) {
    f2 a[8], z[8], zr[8];
#pragma unroll
    for (int m = 0; m < 8; ++m) { z[m] = lds[m * ROWP + za]; zr[m] = lds[m * ROWP + zb]; }
#pragma unroll
    for (int m = 0; m < 8; m += 2) herm2(a[m], a[m + 1], z[m], zr[m], t512, z[m + 1], zr[m + 1], t512);
    if (w == 0) {
#pragma unroll
      for (int m = 0; m < 8; ++m) a[m].y = k2 == 0 ? 2.0f * (z[m].x - z[m].y) : a[m].y;
    }
    bfly8<false>(a);
    cmul7<false>(a, twq);
#pragma unroll
    for (int q = 0; q < 4; ++q)
      __builtin_amdgcn_raw_buffer_store_b128(__builtin_bit_cast(u4, f4{a[2 * q].x, a[2 * q].y, a[2 * q + 1].x, a[2 * q + 1].y}), rs,
                                             to + q * 16, 0, T_AUX);
    STAMP(1, 3);
    STAMP_DRAIN();
    STAMP(1, 4);
  }
}

constexpr int SEQP = 64 + 1;
#define K2_TOUCH 0
__global__ __launch_bounds__(256 + 64 * K2_TOUCH) void k2_cols(unsigned char* ws) {
  __shared__ __attribute__((aligned(16))) f2 lds[2048 + 64];
  const int b = blockIdx.x;
  const int t = threadIdx.x, l = t & 63;
  const __amdgpu_buffer_rsrc_t rs = ws_rsrc(ws);
  STAMP(2, 0);
  const int k2 = (b & 7) * 32 + (b >> 3);
  const int jr = t >> 2, qp = t & 3;
  const int co = (int)OFF_T + (jr * 1024 + k2 * 4 + qp) * 16;
  u4 cin[4];
#pragma unroll
  for (int i = 0; i < 4; ++i) cin[i] = __builtin_amdgcn_raw_buffer_load_b128(rs, co + i * (1 << 20), 0, 0);
  u4 wq[8];
  {
    const int wo = k2 != 0 ? (int)OFF_WF + (k2 * 256 + t) * 32 : (int)OFF_PQ + t * 16;
    const int ws1 = k2 != 0 ? 16 : 4096;
    wq[0] = __builtin_amdgcn_raw_buffer_load_b128(rs, wo, 0, 0);
    wq[1] = __builtin_amdgcn_raw_buffer_load_b128(rs, wo + ws1, 0, 0);
    const int po = k2 != 0 ? OOB : wo;
#pragma unroll
    for (int kb = 2; kb < 8; ++kb) wq[kb] = __builtin_amdgcn_raw_buffer_load_b128(rs, po + kb * 4096, 0, 0);
  }
#pragma unroll
  for (int i = 0; i < 4; ++i)
    __builtin_amdgcn_raw_buffer_store_b128(u4{SENT, SENT, SENT, SENT}, rs,
                                           i * 256 + t < 896 ? (int)OFF_AF + ((256 + b) * 896 + i * 256 + t) * 16 : OOB, 0, FILL_AUX);
  SCHED_FENCE();
  f2 twA[3], twB[7];
#pragma unroll
  for (int r = 1; r < 4; ++r) twA[r - 1] = tw2048(jr * r * 8);
  const int bb = l >> 3;
#pragma unroll
  for (int r = 1; r < 8; ++r) twB[r - 1] = tw2048(bb * r * 32);
  int pa = (8 * qp) * SEQP + jr;
  int pb = ((t >> 6) * 8 + (t & 7)) * SEQP + bb;
  asm volatile("" : "+v"(twB[0]), "+v"(twB[1]), "+v"(twB[2]), "+v"(twB[3]), "+v"(twB[4]), "+v"(twB[5]), "+v"(twB[6]), "+v"(twA[0]), "+v"(twA[1]), "+v"(twA[2]));
  asm volatile("" : "+v"(pa), "+v"(pb));
  SCHED_FENCE();
  f2 a0[4], a1[4];
#pragma unroll
  for (int i = 0; i < 4; ++i) { const f4 c = __builtin_bit_cast(f4, cin[i]); a0[i] = c.xy; a1[i] = c.zw; }
  bfly4<false>(a0[0], a0[1], a0[2], a0[3]);
  bfly4<false>(a1[0], a1[1], a1[2], a1[3]);
  cmul3<false>(a0[1], a0[2], a0[3], twA[0], twA[1], twA[2]);
  cmul3<false>(a1[1], a1[2], a1[3], twA[0], twA[1], twA[2]);
#pragma unroll
  for (int r = 0; r < 4; ++r) {
    lds[pa + r * SEQP] = a0[r];
    lds[pa + (4 + r) * SEQP] = a1[r];
  }
  __syncthreads();
  STAMP(2, 1);
  f2 v[8];
#pragma unroll
  for (int a = 0; a < 8; ++a) v[a] = lds[pb + 8 * a];
  bfly8<false>(v);
  cmul7<false>(v, twB);
  XL_PAD8(v);
#pragma unroll
  for (int i = 0; i < 4; ++i) swap32(v[i], v[i + 4]);
  swap16(v[0], v[2]); swap16(v[1], v[3]); swap16(v[4], v[6]); swap16(v[5], v[7]);
#pragma unroll
  for (int i = 0; i < 8; i += 2) swap8(v[i], v[i + 1]);
  bfly8<false>(v);
  if (k2 != 0) {
    cmul4<false>(v[0], v[1], v[2], v[3], unpk(wq[0][0]), unpk(wq[0][1]), unpk(wq[0][2]), unpk(wq[0][3]));
    cmul4<false>(v[4], v[5], v[6], v[7], unpk(wq[1][0]), unpk(wq[1][1]), unpk(wq[1][2]), unpk(wq[1][3]));
  } else {
    __syncthreads();
#pragma unroll
    for (int kb = 0; kb < 8; ++kb) lds[k2_k1(t, kb)] = v[kb];
    __syncthreads();
#pragma unroll
    for (int kb = 0; kb < 8; ++kb) {
      f2 fn = lds[(2048 - k2_k1(t, kb)) & 2047];
      fn.y = -fn.y;
      const f4 c = __builtin_bit_cast(f4, wq[kb]);
      v[kb] = cfma(fn, c.zw, cmul(v[kb], c.xy));
    }
  }
  bfly8<true>(v);
  XL_PAD8(v);
#pragma unroll
  for (int i = 0; i < 8; i += 2) swap8(v[i], v[i + 1]);
  swap16(v[0], v[2]); swap16(v[1], v[3]); swap16(v[4], v[6]); swap16(v[5], v[7]);
#pragma unroll
  for (int i = 0; i < 4; ++i) swap32(v[i], v[i + 4]);
  cmul7<true>(v, twB);
  bfly8<true>(v);
  if (k2 == 0) __syncthreads();
#pragma unroll
  for (int a = 0; a < 8; ++a) lds[pb + 8 * a] = v[a];
  __syncthreads();
#pragma unroll
  for (int r = 0; r < 4; ++r) {
    a0[r] = lds[pa + r * SEQP];
    a1[r] = lds[pa + (4 + r) * SEQP];
  }
  cmul3<true>(a0[1], a0[2], a0[3], twA[0], twA[1], twA[2]);
  cmul3<true>(a1[1], a1[2], a1[3], twA[0], twA[1], twA[2]);
  bfly4<true>(a0[0], a0[1], a0[2], a0[3]);
  bfly4<true>(a1[0], a1[1], a1[2], a1[3]);
#pragma unroll
  for (int i = 0; i < 4; ++i)
    __builtin_amdgcn_raw_buffer_store_b128(__builtin_bit_cast(u4, f4{a0[i].x, a0[i].y, a1[i].x, a1[i].y}), rs, co + i * (1 << 20), 0, T_AUX);
  STAMP(2, 2);
  STAMP_DRAIN();
  STAMP(2, 3);
}

constexpr unsigned SPIN_CAP = 1u << 18;
constexpr int L_H2 = 0;
constexpr int L_R01 = 65536;
constexpr int L_STG = 131072;
constexpr int LDS3_BYTES = 131072 + 8 * ROWP * 8;
#define P_N1 12
#define P_W2A 12
#define W2B 20
#define FLAGSTEP 48
#define A0STEP 16
#define POLLSTEP 62
#define A2STEP 7
#define NPARK 8
#define F_W2A 8
#define F_L1NB 1
#define P_N2 16
#define P_N3 24
#define F_DELAY2 12
#define MIDBAR 1
#define S_DELAY 75
#define P_PRIO 0
#define F_DELAY 2
#define P_DELAY 4
#define BAR() do { SCHED_FENCE(); asm volatile("s_waitcnt lgkmcnt(0)" ::: "memory"); __builtin_amdgcn_s_barrier(); asm volatile("" ::: "memory"); SCHED_FENCE(); } while (0)

__device__ __forceinline__ unsigned nosent(unsigned v) { return v < SENT - 1u ? v : SENT - 1u; }
template <int N> __device__ __forceinline__ bool all_written(const u4* v) {
  unsigned m = 0u;
#pragma unroll
  for (int i = 0; i < N; ++i) {
    const unsigned a = v[i][0] > v[i][1] ? v[i][0] : v[i][1], c = v[i][2] > v[i][3] ? v[i][2] : v[i][3];
    m = m > a ? m : a;
    m = m > c ? m : c;
  }
  return __all(m != SENT);
}
template <int N> __device__ __forceinline__ void reload_until_written(u4* v, __amdgpu_buffer_rsrc_t rs, int off, int stride) {
  if (__builtin_expect(!all_written<N>(v), 0)) {
    unsigned s = 0;
    bool ok;
    do {
      __builtin_amdgcn_s_sleep(1);
      asm volatile("" : "+v"(off) :: "memory");
#pragma unroll
      for (int i = 0; i < N; ++i) v[i] = __builtin_amdgcn_raw_buffer_load_b128(rs, off + i * stride, 0, AUX_SC1);
      ok = all_written<N>(v);
    } while (!ok && ++s < SPIN_CAP);
  }
}
template <int NTB> __device__ __forceinline__ void wait_flags(unsigned v, gu32* word) {
  if (__builtin_expect(!__all(v >= (unsigned)NTB), 0)) {
    unsigned s = 0;
    do {
      __builtin_amdgcn_s_sleep(2);
      v = __hip_atomic_load(word, RLX_AGENT);
    } while (!__all(v >= (unsigned)NTB) && ++s < SPIN_CAP);
  }
  __builtin_amdgcn_fence(__ATOMIC_ACQUIRE, "wavefront");
  asm volatile("" ::: "memory");
}
template <int HAVE> constexpr int w2_at(int st) {
  if (st < 32) { const int f = HAVE + st; return f < W2B ? f : -1; }
  const int f = W2B + (st - 33);
  return (st >= 33 && f < 28) ? f : -1;
}
template <int HAVE> constexpr int vm_since(int stEnd) {
  int n = 0;
  for (int st = 32; st <= stEnd; ++st) n += (w2_at<HAVE>(st) >= 0 ? 1 : 0) + (st == POLLSTEP ? 1 : 0);
  return n;
}
static_assert(W2B <= 22 && FLAGSTEP >= 32 && FLAGSTEP < 64, "schedule");

__device__ __forceinline__ void dma16(u4 srd, int voff, int soff, unsigned dst) {
  unsigned keep;
  asm volatile("s_mov_b32 %0, m0\n\ts_mov_b32 m0, %1\n\ts_nop 0\n\tbuffer_load_dwordx4 %2, %3, %4 offen lds\n\ts_mov_b32 m0, %0"
               : "=&s"(keep) : "s"(dst), "v"(voff), "s"(srd), "s"(soff) : "memory");
}
template <int I0, int I1> __device__ __forceinline__ void dma_run(u4 srd, int voff, unsigned dst) {
#pragma unroll
  for (int i = I0; i < I1; ++i) dma16(srd, voff, i * 1024, dst + i * 1024);
}
#define VM_WAIT(n) do { SCHED_FENCE(); asm volatile("s_waitcnt vmcnt(%0)" :: "i"(n) : "memory"); SCHED_FENCE(); } while (0)
__device__ __forceinline__ u4 make_srd(const void* p, unsigned bytes) {
  const unsigned long long a = (unsigned long long)p;
  return u4{(unsigned)a, (unsigned)(a >> 32) & 0xffffu, bytes, 0x00020000u};
}
#define LDW(arr, i, off) arr[i] = __builtin_amdgcn_raw_buffer_load_b128(rs, (off) + (i) * 1024, 0, 0)

template <int CLS>
__device__ __forceinline__ void k3_body(const unsigned* __restrict__ x32, unsigned char* ws,
                                        unsigned short* __restrict__ out, unsigned char* smem, int w) {
  const int b = blockIdx.x;
  const int g = (b & 7) + 8 * (b >> 6), jj = (b >> 3) & 7;
  const int j = 8 * g + jj;
  const int tid = threadIdx.x, l = tid & 63;
  const __amdgpu_buffer_rsrc_t rs = ws_rsrc(ws);
  f2* lds = (f2*)(smem + L_STG);
  u4 wv[32], uv[28];
  constexpr bool FW = CLS == 0;
  const int wo = (int)OFF_W1F + ((7 * jj + w) * 2048 + l) * 16;
  const int uo = (int)OFF_W2F + ((jj * 56 + 7 * w) * 256 + l) * 16;
  constexpr int HAVE = FW ? F_W2A : (CLS == 1 ? 0 : 28);
  if (FW) {
    u4 tin[4];
#pragma unroll
    for (int q = 0; q < 4; ++q) tin[q] = __builtin_amdgcn_raw_buffer_load_b128(rs, (int)OFF_T + (j * 256 + tid) * 64 + q * 16, 0, 0);
    unsigned xr[2][4];
#pragma unroll
    for (int u = 0; u < 2; ++u)
#pragma unroll
      for (int i = 0; i < 4; ++i) xr[u][i] = x32[(size_t)(j + 256 * (2 * w + u)) * 256 + l + 64 * i];
    u4 mk[3];
#pragma unroll
    for (int i = 0; i < 3; ++i) mk[i] = ((const u4*)(ws + OFF_MODK3))[l * 3 + i];
    SCHED_FENCE();
    const int kl = (l >> 4) + 4 * ((l >> 2) & 3) + 16 * (l & 3);
    Tw256 tw;
    make_tw256(tw, l);
    tw.x2a += 2 * w * ROWP; tw.x2b += 2 * w * ROWP; tw.x3a += 2 * w * ROWP; tw.x3b += 2 * w * ROWP;
    f2 t512[4];
    int zk[4], zn[4];
#pragma unroll
    for (int i = 0; i < 4; ++i) {
      t512[i] = tw2048(-4 * (kl + 64 * i) - 512);
      zk[i] = 2 * w * ROWP + zpos(kl + 64 * i);
      zn[i] = 2 * w * ROWP + zpos((256 - kl - 64 * i) & 255);
    }
    f2 twq[7];
#pragma unroll
    for (int q = 1; q < 8; ++q) twq[q - 1] = tw2048(j * q);
    int zc = zpos(tid);
    asm volatile("" : "+v"(twq[0]), "+v"(twq[1]), "+v"(twq[2]), "+v"(twq[3]), "+v"(twq[4]), "+v"(twq[5]), "+v"(twq[6]));
    asm volatile("" : "+v"(t512[0]), "+v"(t512[1]), "+v"(t512[2]), "+v"(t512[3]), "+v"(zc));
    asm volatile("" : "+v"(zk[0]), "+v"(zk[1]), "+v"(zk[2]), "+v"(zk[3]), "+v"(zn[0]), "+v"(zn[1]), "+v"(zn[2]), "+v"(zn[3]));
    PIN_TW(tw);
    SCHED_FENCE();
    {
      f2 a[8];
#pragma unroll
      for (int q = 0; q < 4; ++q) { const f4 c = __builtin_bit_cast(f4, tin[q]); a[2 * q] = c.xy; a[2 * q + 1] = c.zw; }
      cmul7<true>(a, twq);
      bfly8<true>(a);
#pragma unroll
      for (int m = 0; m < 8; ++m) lds[m * ROWP + zc] = a[m];
    }
    BAR();
    STAMP(3, 1);
    f2 v[2][4];
#pragma unroll
    for (int u = 0; u < 2; ++u) {
      f2 xk[4], xn[4];
#pragma unroll
      for (int r = 0; r < 4; ++r) { xk[r] = lds[zk[r] + u * ROWP]; xn[r] = lds[zn[r] + u * ROWP]; }
      herm2(v[u][0], v[u][1], xk[0], xn[0], t512[0], xk[1], xn[1], t512[1]);
      herm2(v[u][2], v[u][3], xk[2], xn[2], t512[2], xk[3], xn[3], t512[3]);
      if (l == 0) v[u][0] = f2{xk[0].x + xk[0].y, xk[0].x - xk[0].y};
    }
    __builtin_amdgcn_wave_barrier();
    fft256_inv2<ROWP>(v, tw, lds);
    STAMP(3, 14);
    unsigned sc1[4], sh[4];
    f2 ga[4];
#pragma unroll
    for (int i = 0; i < 4; ++i) {
      ga[i] = unpk(mk[(3 * i) >> 2][(3 * i) & 3]) * 4.76837158203125e-07f;
      sc1[i] = mk[(3 * i + 1) >> 2][(3 * i + 1) & 3];
      sh[i] = mk[(3 * i + 2) >> 2][(3 * i + 2) & 3];
    }
#pragma unroll
    for (int u = 0; u < 2; ++u) {
      f2 x1[4], h2[4];
      unsigned x1p[4];
#pragma unroll
      for (int i = 0; i < 4; ++i) {
        x1p[i] = pk(unpk(xr[u][i]) + ga[i] * v[u][i]);
        x1[i] = unpk(x1p[i]);
      }
      ln_mod(x1, sc1, sh, h2);
      unsigned* st = (unsigned*)(lds + (2 * w + u) * ROWP);
#pragma unroll
      for (int i = 0; i < 4; ++i) {
        st[l + 64 * i] = x1p[i];
        st[256 + l + 64 * i] = nosent(pk(h2[i]));
      }
    }
    STAMP(3, 15);
    BAR();
    STAMP(3, 2);
    __builtin_amdgcn_s_sleep(F_DELAY);
#pragma unroll
    for (int u = 0; u < 2; ++u)
      __builtin_amdgcn_raw_buffer_store_b128(*(const u4*)((const unsigned*)(lds + (2 * w + u) * ROWP) + 4 * l), rs,
                                             (int)OFF_X1 + ((64 * g + 8 * jj + 2 * w + u) * 64 + l) * 16, 0, AUX_SC1);
    BAR();
#pragma unroll
    for (int i = 0; i < 32; ++i) wv[i] = *(const u4*)(smem + (w * 32 + i) * 1024 + l * 16);
    BAR();
    __builtin_amdgcn_s_sleep(F_DELAY2);
#pragma unroll
    for (int i = 0; i < F_W2A; ++i) LDW(uv, i, uo);
    STAMP(3, 3);
    BAR();
  } else {
    const int pw = w - 4;
    const u4 srd = make_srd(ws, (unsigned)WS_BYTES);
    const int po = (int)OFF_W1F + ((7 * jj + pw) * 2048 + l) * 16;
    dma_run<0, P_N1>(srd, po, pw * 32768);
    BAR();
    dma_run<P_N1, 32>(srd, po, pw * 32768);
    if (CLS == 1) {
#pragma unroll
      for (int i = 0; i < P_N2; ++i) LDW(wv, i, wo);
    } else {
#pragma unroll
      for (int i = 0; i < P_N2; ++i) LDW(uv, i, uo);
    }
    BAR();
    __builtin_amdgcn_s_setprio(3);
    {
      u4 ho[2];
#pragma unroll
      for (int u = 0; u < 2; ++u) {
        const int ch = 8 * (2 * pw + u) + (l >> 3);
        ho[u] = *(const u4*)((const unsigned*)(lds + (l & 7) * ROWP) + 256 + 4 * ch);
      }
      const int ha = (int)OFF_H2F + (((g * 2 + (jj >> 2)) * 32 + 8 * pw + (l >> 4)) * 64 + 8 * (jj & 3) + (l & 7) + 32 * ((l >> 3) & 1)) * 16;
#pragma unroll
      for (int u = 0; u < 2; ++u) __builtin_amdgcn_raw_buffer_store_b128(ho[u], rs, ha + u * 4096, 0, AUX_SC1);
    }
    VM_WAIT(P_N2 + 2);
    BAR();
    BAR();
    if (CLS == 1) {
#pragma unroll
      for (int i = P_N2; i < P_N3; ++i) LDW(wv, i, wo);
    } else {
#pragma unroll
      for (int i = P_N2; i < (P_N3 < 28 ? P_N3 : 28); ++i) LDW(uv, i, uo);
    }
    SCHED_FENCE();
    {
      const int hg = (int)OFF_H2F + g * 65536, hoff = (pw * 1024 + l) * 16;
      u4 hv[16];
      __builtin_amdgcn_s_sleep(P_DELAY);
      asm volatile("" ::: "memory");
#pragma unroll
      for (int i = 0; i < 16; ++i) hv[i] = __builtin_amdgcn_raw_buffer_load_b128(rs, hg + hoff + i * 1024, 0, AUX_SC1);
      STAMPT(3, 4, 256);
      SCHED_FENCE();
      if (CLS == 1) {
#pragma unroll
        for (int i = P_N3; i < 32; ++i) LDW(wv, i, wo);
      } else {
#pragma unroll
        for (int i = P_N3; i < 28; ++i) LDW(uv, i, uo);
      }
      SCHED_FENCE();
      reload_until_written<16>(hv, rs, hg + hoff, 1024);
#pragma unroll
      for (int i = 0; i < 16; ++i) *(u4*)(smem + L_H2 + hoff + i * 1024) = hv[i];
    }
    STAMPT(3, 8, 256);
    BAR();
  }
  STAMP(3, 5); STAMPT(4, 5, 256);
  const int aro = (int)OFF_AF + ((g * 8 + w) * 28 * 64 + l) * 16;
  u4 av[28];
#define LDA(i) av[i] = __builtin_amdgcn_raw_buffer_load_b128(rs, aro + (i) * 1024, 0, AUX_SC1)
  if (CLS != 2) {
    const u4* hs = (const u4*)(smem + L_H2);
    u4 hb[4];
    const int afo = (int)OFF_AF + (((g * 8 + jj) * 28 + 2 * w) * 64 + l) * 16;
#pragma unroll
    for (int st = 0; st < 3; ++st) hb[st] = hs[st * 64 + l];
#pragma unroll
    for (int th = 0; th < 2; ++th) {
      f16v acc;
#pragma unroll
      for (int i = 0; i < 16; ++i) acc[i] = 0.f;
      if (th == 0) __builtin_amdgcn_s_setprio(3); else __builtin_amdgcn_s_setprio(2);
#pragma unroll
      for (int ks = 0; ks < 32; ++ks) {
        const int st = th * 32 + ks;
        if (st + 3 < 64) hb[(st + 3) & 3] = hs[(st + 3) * 64 + l];
        if (w2_at<HAVE>(st) >= 0) LDW(uv, w2_at<HAVE>(st) < 0 ? 0 : w2_at<HAVE>(st), uo);
        SCHED_FENCE();
        acc = mfma32(wv[ks], hb[st & 3], acc);
        SCHED_FENCE();
      }
      if (FW && th == 0) asm volatile("s_waitcnt vmcnt(%0)" :: "i"(W2B - HAVE) : "memory");
#pragma unroll
      for (int sk = 0; sk < 2; ++sk) {
        u4 o;
#pragma unroll
        for (int e = 0; e < 4; ++e) o[e] = nosent(pk(f2{silu_f(acc[8 * sk + 2 * e]), silu_f(acc[8 * sk + 2 * e + 1])}));
        __builtin_amdgcn_raw_buffer_store_b128(o, rs, afo + (th * 14 + sk) * 1024, 0, AUX_SC1);
      }
      SCHED_FENCE();
      if (th == 0) { __builtin_amdgcn_s_barrier(); SCHED_FENCE(); }
    }
    __builtin_amdgcn_s_setprio(0);
    STAMP(3, 6); STAMPT(4, 6, 256);
  } else {
    __builtin_amdgcn_s_setprio(0);
    __builtin_amdgcn_s_barrier();
    __builtin_amdgcn_s_sleep(S_DELAY);
  }
  asm volatile("" ::: "memory");
  __builtin_amdgcn_s_setprio(3);
#pragma unroll
  for (int i = 0; i < (FW ? 14 : 28); ++i) LDA(i);
  __builtin_amdgcn_s_setprio(0);
  SCHED_FENCE();
  const int thf = w >> 2, nbf = (w >> 1) & 1, hff = w & 1;
  const int tok = 32 * thf + (l & 31);
  const int col = 64 * jj + 32 * nbf + 16 * (l >> 5) + 8 * hff;
  u4 x1v, gbv;
#pragma unroll
  for (int th = 0; th < 2; ++th) {
    if (th == 1) {
      BAR();
      STAMP(3, 7); STAMPT(4, 7, 256);
      x1v = __builtin_amdgcn_raw_buffer_load_b128(rs, (int)OFF_X1 + ((64 * g + tok) * 64 + (col >> 3)) * 16, 0, AUX_SC1);
      gbv = __builtin_amdgcn_raw_buffer_load_b128(rs, (int)OFF_GB + col * 2, 0, 0);
      SCHED_FENCE();
    }
    reload_until_written<14>(av + th * 14, rs, aro + th * 14 * 1024, 1024);
    SCHED_FENCE();
    if (th == 0) { STAMP(3, 12); STAMPT(4, 12, 256); }
    f4* red = (f4*)(smem + (th == 0 ? L_R01 : L_H2)) + w * 512 + l;
#pragma unroll
    for (int nb = 0; nb < 2; ++nb) {
      f16v acc;
#pragma unroll
      for (int i = 0; i < 16; ++i) acc[i] = 0.f;
#pragma unroll
      for (int k = 0; k < 14; ++k) acc = mfma32(uv[2 * k + nb], av[th * 14 + k], acc);
      if (FW && th == 0 && nb == F_L1NB) {
        SCHED_FENCE();
        __builtin_amdgcn_s_setprio(3);
#pragma unroll
        for (int i = 14; i < 28; ++i) LDA(i);
        __builtin_amdgcn_s_setprio(0);
        SCHED_FENCE();
      }
#pragma unroll
      for (int q4 = 0; q4 < 4; ++q4)
        red[(nb * 4 + q4) * 64] = f4{acc[4 * q4], acc[4 * q4 + 1], acc[4 * q4 + 2], acc[4 * q4 + 3]};
      SCHED_FENCE();
    }
  }
  STAMP(3, 9); STAMPT(4, 9, 256);
  BAR();
  f4 s0 = f4{0.f, 0.f, 0.f, 0.f}, s1 = s0;
  {
    const f4* red = (const f4*)(smem + (thf == 0 ? L_R01 : L_H2)) + (nbf * 4 + 2 * hff) * 64 + l;
#pragma unroll
    for (int ww = 0; ww < 8; ++ww) {
      s0 += red[ww * 512];
      s1 += red[ww * 512 + 64];
    }
  }
  u4 o;
  o[0] = pk(unpk(x1v[0]) + unpk(gbv[0]) * s0.xy);
  o[1] = pk(unpk(x1v[1]) + unpk(gbv[1]) * s0.zw);
  o[2] = pk(unpk(x1v[2]) + unpk(gbv[2]) * s1.xy);
  o[3] = pk(unpk(x1v[3]) + unpk(gbv[3]) * s1.zw);
  const int srow = 8 * g + (tok >> 3) + 256 * (tok & 7);
  *(u4*)(out + (size_t)srow * D + col) = o;
  STAMP(3, 10); STAMPT(4, 10, 256);
  STAMP_DRAIN();
  STAMP(3, 11); STAMPT(4, 11, 256);
}

__global__ __launch_bounds__(512, 2) void k3_fused(const unsigned* __restrict__ x32, unsigned char* ws,
                                                   unsigned short* __restrict__ out) {
  extern __shared__ __attribute__((aligned(16))) unsigned char smem[];
  const int w = __builtin_amdgcn_readfirstlane(threadIdx.x >> 6);
  STAMP(3, 0);
  if (w < 4) k3_body<0>(x32, ws, out, smem, w);
  else if (w < 7) k3_body<1>(x32, ws, out, smem, w);
  else k3_body<2>(x32, ws, out, smem, w);
}

}

extern "C" void kernel_launch(void* const* d_in, const int* in_sizes, int n_in, void* d_out, int out_size,
                              void* d_ws, size_t ws_size, hipStream_t stream) {
  const unsigned short* x = (const unsigned short*)d_in[0];
  const unsigned short* wr = (const unsigned short*)d_in[1];
  const unsigned short* wi = (const unsigned short*)d_in[2];
  const unsigned short* w1 = (const unsigned short*)d_in[4];
  const unsigned short* w2 = (const unsigned short*)d_in[5];
  const unsigned short* cv = (const unsigned short*)d_in[6];
  const unsigned short* wm = (const unsigned short*)d_in[7];
  unsigned char* ws = (unsigned char*)d_ws;
  (void)hipFuncSetAttribute((const void*)k3_fused, hipFuncAttributeMaxDynamicSharedMemorySize, LDS3_BYTES);
  k0_prep<<<K0_BLOCKS, 256, 0, stream>>>(cv, wm, wr, wi, ws);
  k1_rows<<<256, 640, 0, stream>>>((const unsigned*)x, w1, w2, ws);
  k2_cols<<<256, 256 + 64 * K2_TOUCH, 0, stream>>>(ws);
  k3_fused<<<256, 512, LDS3_BYTES, stream>>>((const unsigned*)x, ws, (unsigned short*)d_out);
}
```

```cpp
#include <hip/hip_runtime.h>
#include <hip/hip_bf16.h>
#include <stdint.h>
#include <type_traits>

namespace {

typedef float f2 __attribute__((ext_vector_type(2)));
typedef float f4 __attribute__((ext_vector_type(4)));
typedef unsigned u2 __attribute__((ext_vector_type(2)));
typedef unsigned u4 __attribute__((ext_vector_type(4)));
typedef short s8 __attribute__((ext_vector_type(8)));
typedef __bf16 bf2 __attribute__((ext_vector_type(2)));
#define GAS __attribute__((address_space(1)))
typedef GAS unsigned gu32;
#define RLX_AGENT __ATOMIC_RELAXED, __HIP_MEMORY_SCOPE_AGENT
#define SCHED_FENCE() __builtin_amdgcn_sched_barrier(0)

constexpr int S = 2048, D = 512, DF = 257, FF = 1792;

constexpr size_t OFF_F2    = 0;
constexpr size_t OFF_MODK1 = 262144;
constexpr size_t OFF_MODK3 = OFF_MODK1 + 2048;
constexpr size_t OFF_GB    = OFF_MODK3 + 3072;
constexpr size_t OFF_PQ    = OFF_MODK1 + 8192;
constexpr size_t OFF_WF    = OFF_PQ + 32768;
constexpr size_t OFF_T     = OFF_WF + (size_t)256 * 2048 * 4;
constexpr size_t OFF_H2F   = OFF_T + (size_t)256 * 2048 * 8;
constexpr size_t OFF_X1    = OFF_H2F + (size_t)S * D * 2;
constexpr size_t OFF_AF    = OFF_X1 + (size_t)S * D * 2;
constexpr size_t OFF_W1F   = OFF_AF + (size_t)S * FF * 2;
constexpr size_t OFF_W2F   = OFF_W1F + (size_t)D * FF * 2;
constexpr size_t WS_BYTES  = OFF_W2F + (size_t)D * FF * 2;

#define STAMP(kid, i) do {} while (0)
#define STAMPT(kid, i, thr) do {} while (0)
#define STAMP_DRAIN() do {} while (0)

__device__ __forceinline__ float bflo(unsigned u) { return __builtin_bit_cast(float, u << 16); }
__device__ __forceinline__ float bfhi(unsigned u) { return __builtin_bit_cast(float, u & 0xffff0000u); }
__device__ __forceinline__ f2 unpk(unsigned u) { return f2{bflo(u), bfhi(u)}; }
__device__ __forceinline__ unsigned pk(f2 v) {
  bf2 b = __builtin_convertvector(v, bf2);
  return __builtin_bit_cast(unsigned, b);
}
__device__ __forceinline__ f2 rb2(f2 v) { return unpk(pk(v)); }
__device__ __forceinline__ float rb1(float v) { return bflo(pk(f2{v, v})); }

__device__ __forceinline__ f2 tw2048(int t) {
  const float f = (float)t * (1.0f / 2048.0f);
  return f2{__builtin_amdgcn_cosf(f), -__builtin_amdgcn_sinf(f)};
}

__device__ __forceinline__ f2 cmul(f2 a, f2 w) {
  f2 t, d;
  asm("v_pk_mul_f32 %0, %1, %2 op_sel:[1,1] op_sel_hi:[1,0] neg_lo:[0,1]" : "=v"(t) : "v"(a), "v"(w));
  asm("v_pk_fma_f32 %0, %1, %2, %3 op_sel:[0,0,0] op_sel_hi:[0,1,1]" : "=v"(d) : "v"(a), "v"(w), "v"(t));
  return d;
}
__device__ __forceinline__ f2 cmulc(f2 a, f2 w) {
  f2 t, d;
  asm("v_pk_mul_f32 %0, %1, %2 op_sel:[0,0] op_sel_hi:[1,0]" : "=v"(t) : "v"(a), "v"(w));
  asm("v_pk_fma_f32 %0, %1, %2, %3 op_sel:[1,1,0] op_sel_hi:[0,1,1] neg_hi:[1,0,0]" : "=v"(d) : "v"(a), "v"(w), "v"(t));
  return d;
}
template <bool CONJ> __device__ __forceinline__ void cmul3(f2& a0, f2& a1, f2& a2, f2 w0, f2 w1, f2 w2) {
  f2 t0, t1, t2;
  if (!CONJ) {
    asm("v_pk_mul_f32 %3, %0, %6 op_sel:[1,1] op_sel_hi:[1,0] neg_lo:[0,1]\n\t"
        "v_pk_mul_f32 %4, %1, %7 op_sel:[1,1] op_sel_hi:[1,0] neg_lo:[0,1]\n\t"
        "v_pk_mul_f32 %5, %2, %8 op_sel:[1,1] op_sel_hi:[1,0] neg_lo:[0,1]\n\t"
        "v_pk_fma_f32 %0, %0, %6, %3 op_sel:[0,0,0] op_sel_hi:[0,1,1]\n\t"
        "v_pk_fma_f32 %1, %1, %7, %4 op_sel:[0,0,0] op_sel_hi:[0,1,1]\n\t"
        "v_pk_fma_f32 %2, %2, %8, %5 op_sel:[0,0,0] op_sel_hi:[0,1,1]"
        : "+v"(a0), "+v"(a1), "+v"(a2), "=&v"(t0), "=&v"(t1), "=&v"(t2) : "v"(w0), "v"(w1), "v"(w2));
  } else {
    asm("v_pk_mul_f32 %3, %0, %6 op_sel:[0,0] op_sel_hi:[1,0]\n\t"
        "v_pk_mul_f32 %4, %1, %7 op_sel:[0,0] op_sel_hi:[1,0]\n\t"
        "v_pk_mul_f32 %5, %2, %8 op_sel:[0,0] op_sel_hi:[1,0]\n\t"
        "v_pk_fma_f32 %0, %0, %6, %3 op_sel:[1,1,0] op_sel_hi:[0,1,1] neg_hi:[1,0,0]\n\t"
        "v_pk_fma_f32 %1, %1, %7, %4 op_sel:[1,1,0] op_sel_hi:[0,1,1] neg_hi:[1,0,0]\n\t"
        "v_pk_fma_f32 %2, %2, %8, %5 op_sel:[1,1,0] op_sel_hi:[0,1,1] neg_hi:[1,0,0]"
        : "+v"(a0), "+v"(a1), "+v"(a2), "=&v"(t0), "=&v"(t1), "=&v"(t2) : "v"(w0), "v"(w1), "v"(w2));
  }
}
template <bool CONJ> __device__ __forceinline__ void cmul4(f2& a0, f2& a1, f2& a2, f2& a3, f2 w0, f2 w1, f2 w2, f2 w3) {
  f2 t0, t1, t2, t3;
  if (!CONJ) {
    asm("v_pk_mul_f32 %4, %0, %8 op_sel:[1,1] op_sel_hi:[1,0] neg_lo:[0,1]\n\t"
        "v_pk_mul_f32 %5, %1, %9 op_sel:[1,1] op_sel_hi:[1,0] neg_lo:[0,1]\n\t"
        "v_pk_mul_f32 %6, %2, %10 op_sel:[1,1] op_sel_hi:[1,0] neg_lo:[0,1]\n\t"
        "v_pk_mul_f32 %7, %3, %11 op_sel:[1,1] op_sel_hi:[1,0] neg_lo:[0,1]\n\t"
        "v_pk_fma_f32 %0, %0, %8, %4 op_sel:[0,0,0] op_sel_hi:[0,1,1]\n\t"
        "v_pk_fma_f32 %1, %1, %9, %5 op_sel:[0,0,0] op_sel_hi:[0,1,1]\n\t"
        "v_pk_fma_f32 %2, %2, %10, %6 op_sel:[0,0,0] op_sel_hi:[0,1,1]\n\t"
        "v_pk_fma_f32 %3, %3, %11, %7 op_sel:[0,0,0] op_sel_hi:[0,1,1]"
        : "+v"(a0), "+v"(a1), "+v"(a2), "+v"(a3), "=&v"(t0), "=&v"(t1), "=&v"(t2), "=&v"(t3)
        : "v"(w0), "v"(w1), "v"(w2), "v"(w3));
  } else {
    asm("v_pk_mul_f32 %4, %0, %8 op_sel:[0,0] op_sel_hi:[1,0]\n\t"
        "v_pk_mul_f32 %5, %1, %9 op_sel:[0,0] op_sel_hi:[1,0]\n\t"
        "v_pk_mul_f32 %6, %2, %10 op_sel:[0,0] op_sel_hi:[1,0]\n\t"
        "v_pk_mul_f32 %7, %3, %11 op_sel:[0,0] op_sel_hi:[1,0]\n\t"
        "v_pk_fma_f32 %0, %0, %8, %4 op_sel:[1,1,0] op_sel_hi:[0,1,1] neg_hi:[1,0,0]\n\t"
        "v_pk_fma_f32 %1, %1, %9, %5 op_sel:[1,1,0] op_sel_hi:[0,1,1] neg_hi:[1,0,0]\n\t"
        "v_pk_fma_f32 %2, %2, %10, %6 op_sel:[1,1,0] op_sel_hi:[0,1,1] neg_hi:[1,0,0]\n\t"
        "v_pk_fma_f32 %3, %3, %11, %7 op_sel:[1,1,0] op_sel_hi:[0,1,1] neg_hi:[1,0,0]"
        : "+v"(a0), "+v"(a1), "+v"(a2), "+v"(a3), "=&v"(t0), "=&v"(t1), "=&v"(t2), "=&v"(t3)
        : "v"(w0), "v"(w1), "v"(w2), "v"(w3));
  }
}
template <bool CONJ> __device__ __forceinline__ void cmul7(f2 (&a)[8], const f2 (&w)[7]) {
  cmul3<CONJ>(a[1], a[2], a[3], w[0], w[1], w[2]);
  cmul4<CONJ>(a[4], a[5], a[6], a[7], w[3], w[4], w[5], w[6]);
}
__device__ __forceinline__ void herm2(f2& r0, f2& r1, f2 z0, f2 q0, f2 w0, f2 z1, f2 q1, f2 w1) {
  f2 d0, d1;
  asm("v_pk_add_f32 %0, %4, %5 neg_hi:[0,1]\n\t"
      "v_pk_add_f32 %2, %4, %5 neg_lo:[0,1]\n\t"
      "v_pk_add_f32 %1, %7, %8 neg_hi:[0,1]\n\t"
      "v_pk_add_f32 %3, %7, %8 neg_lo:[0,1]\n\t"
      "v_pk_fma_f32 %0, %2, %6, %0 op_sel:[1,1,0] op_sel_hi:[1,0,1] neg_lo:[0,1,0]\n\t"
      "v_pk_fma_f32 %1, %3, %9, %1 op_sel:[1,1,0] op_sel_hi:[1,0,1] neg_lo:[0,1,0]\n\t"
      "v_pk_fma_f32 %0, %2, %6, %0 op_sel:[0,0,0] op_sel_hi:[0,1,1]\n\t"
      "v_pk_fma_f32 %1, %3, %9, %1 op_sel:[0,0,0] op_sel_hi:[0,1,1]"
      : "=&v"(r0), "=&v"(r1), "=&v"(d0), "=&v"(d1) : "v"(z0), "v"(q0), "v"(w0), "v"(z1), "v"(q1), "v"(w1));
}
__device__ __forceinline__ f2 cfma(f2 a, f2 w, f2 c) {
  f2 t, d;
  asm("v_pk_fma_f32 %0, %1, %2, %3 op_sel:[1,1,0] op_sel_hi:[1,0,1] neg_lo:[0,1,0]" : "=v"(t) : "v"(a), "v"(w), "v"(c));
  asm("v_pk_fma_f32 %0, %1, %2, %3 op_sel:[0,0,0] op_sel_hi:[0,1,1]" : "=v"(d) : "v"(a), "v"(w), "v"(t));
  return d;
}
__device__ __forceinline__ f2 add_mi(f2 a, f2 b) {
  f2 d;
  asm("v_pk_add_f32 %0, %1, %2 op_sel:[0,1] op_sel_hi:[1,0] neg_hi:[0,1]" : "=v"(d) : "v"(a), "v"(b));
  return d;
}
__device__ __forceinline__ f2 add_pi(f2 a, f2 b) {
  f2 d;
  asm("v_pk_add_f32 %0, %1, %2 op_sel:[0,1] op_sel_hi:[1,0] neg_lo:[0,1]" : "=v"(d) : "v"(a), "v"(b));
  return d;
}
__device__ __forceinline__ f2 add_cj(f2 a, f2 b) {
  f2 d;
  asm("v_pk_add_f32 %0, %1, %2 neg_hi:[0,1]" : "=v"(d) : "v"(a), "v"(b));
  return d;
}
__device__ __forceinline__ f2 sub_cj(f2 a, f2 b) {
  f2 d;
  asm("v_pk_add_f32 %0, %1, %2 neg_lo:[0,1]" : "=v"(d) : "v"(a), "v"(b));
  return d;
}
template <bool INV> __device__ __forceinline__ f2 addr(f2 a, f2 b) { return INV ? add_pi(a, b) : add_mi(a, b); }
template <bool INV> __device__ __forceinline__ f2 subr(f2 a, f2 b) { return INV ? add_mi(a, b) : add_pi(a, b); }

template <bool INV> __device__ __forceinline__ void bfly4(f2& a0, f2& a1, f2& a2, f2& a3) {
  const f2 t0 = a0 + a2, t1 = a0 - a2, t2 = a1 + a3, u = a1 - a3;
  a0 = t0 + t2; a2 = t0 - t2; a1 = addr<INV>(t1, u); a3 = subr<INV>(t1, u);
}
template <bool INV> __device__ __forceinline__ void bfly8(f2 (&a)[8]) {
  constexpr float R = 0.70710678118654752440f;
  bfly4<INV>(a[0], a[2], a[4], a[6]);
  bfly4<INV>(a[1], a[3], a[5], a[7]);
  const f2 e0 = a[0], e1 = a[2], e2 = a[4], e3 = a[6];
  const f2 o0 = a[1], o2 = a[5];
  const f2 p1 = addr<INV>(a[3], a[3]);
  const f2 p3 = subr<INV>(a[7], a[7]);
  a[0] = e0 + o0; a[4] = e0 - o0;
  a[1] = e1 + R * p1; a[5] = e1 - R * p1;
  a[2] = addr<INV>(e2, o2); a[6] = subr<INV>(e2, o2);
  a[3] = e3 - R * p3; a[7] = e3 + R * p3;
}
#define XL_PAD4(v) asm volatile("s_nop 1" : "+v"(v[0]), "+v"(v[1]), "+v"(v[2]), "+v"(v[3]))
#define XL_PAD8(v) asm volatile("s_nop 1" : "+v"(v[0]), "+v"(v[1]), "+v"(v[2]), "+v"(v[3]), "+v"(v[4]), "+v"(v[5]), "+v"(v[6]), "+v"(v[7]))

__device__ __forceinline__ void swap32(f2& a, f2& b) {
  const u2 au = __builtin_bit_cast(u2, a), bu = __builtin_bit_cast(u2, b);
  const unsigned ax = au[0], ay = au[1], bx = bu[0], by = bu[1];
  auto rx = __builtin_amdgcn_permlane32_swap(ax, bx, false, false);
  auto ry = __builtin_amdgcn_permlane32_swap(ay, by, false, false);
  a = __builtin_bit_cast(f2, u2{(unsigned)rx[0], (unsigned)ry[0]});
  b = __builtin_bit_cast(f2, u2{(unsigned)rx[1], (unsigned)ry[1]});
}
__device__ __forceinline__ void swap16(f2& a, f2& b) {
  const u2 au = __builtin_bit_cast(u2, a), bu = __builtin_bit_cast(u2, b);
  const unsigned ax = au[0], ay = au[1], bx = bu[0], by = bu[1];
  auto rx = __builtin_amdgcn_permlane16_swap(ax, bx, false, false);
  auto ry = __builtin_amdgcn_permlane16_swap(ay, by, false, false);
  a = __builtin_bit_cast(f2, u2{(unsigned)rx[0], (unsigned)ry[0]});
  b = __builtin_bit_cast(f2, u2{(unsigned)rx[1], (unsigned)ry[1]});
}
__device__ __forceinline__ void swap8(f2& a, f2& b) {
  const u2 au = __builtin_bit_cast(u2, a), bu = __builtin_bit_cast(u2, b);
  const int ax = au[0], ay = au[1], bx = bu[0], by = bu[1];
  const int nax = __builtin_amdgcn_update_dpp(ax, bx, 0x128, 0xf, 0xc, false);
  const int nbx = __builtin_amdgcn_update_dpp(bx, ax, 0x128, 0xf, 0x3, false);
  const int nay = __builtin_amdgcn_update_dpp(ay, by, 0x128, 0xf, 0xc, false);
  const int nby = __builtin_amdgcn_update_dpp(by, ay, 0x128, 0xf, 0x3, false);
  a = __builtin_bit_cast(f2, u2{(unsigned)nax, (unsigned)nay});
  b = __builtin_bit_cast(f2, u2{(unsigned)nbx, (unsigned)nby});
}

template <int CTRL> __device__ __forceinline__ float dppmov(float v) {
  return __builtin_bit_cast(float, __builtin_amdgcn_update_dpp(0, __builtin_bit_cast(int, v), CTRL, 0xf, 0xf, true));
}
__device__ __forceinline__ float wave_sum(float v) {
  v += dppmov<0xB1>(v);
  v += dppmov<0x4E>(v);
  v += dppmov<0x141>(v);
  v += dppmov<0x140>(v);
  {
    unsigned u = __builtin_bit_cast(unsigned, v);
    auto r = __builtin_amdgcn_permlane16_swap(u, u, false, false);
    v = __builtin_bit_cast(float, (unsigned)r[0]) + __builtin_bit_cast(float, (unsigned)r[1]);
  }
  {
    unsigned u = __builtin_bit_cast(unsigned, v);
    auto r = __builtin_amdgcn_permlane32_swap(u, u, false, false);
    v = __builtin_bit_cast(float, (unsigned)r[0]) + __builtin_bit_cast(float, (unsigned)r[1]);
  }
  return v;
}

__device__ __forceinline__ f4 mfma(u4 a, u4 b, f4 c) {
  return __builtin_amdgcn_mfma_f32_16x16x32_bf16(__builtin_bit_cast(s8, a), __builtin_bit_cast(s8, b), c, 0, 0, 0);
}
typedef float f16v __attribute__((ext_vector_type(16)));
__device__ __forceinline__ f16v mfma32(u4 a, u4 b, f16v c) {
  return __builtin_amdgcn_mfma_f32_32x32x16_bf16(__builtin_bit_cast(s8, a), __builtin_bit_cast(s8, b), c, 0, 0, 0);
}
__device__ __forceinline__ float silu_f(float v) {
  return v * __builtin_amdgcn_rcpf(1.0f + __expf(-v));
}

__device__ __forceinline__ void wave_sum2(float& a, float& b) {
  a += dppmov<0xB1>(a);  b += dppmov<0xB1>(b);
  a += dppmov<0x4E>(a);  b += dppmov<0x4E>(b);
  a += dppmov<0x141>(a); b += dppmov<0x141>(b);
  a += dppmov<0x140>(a); b += dppmov<0x140>(b);
  unsigned ua = __builtin_bit_cast(unsigned, a), ub = __builtin_bit_cast(unsigned, b);
  auto r = __builtin_amdgcn_permlane16_swap(ua, ub, false, false);
  float c = __builtin_bit_cast(float, (unsigned)r[0]) + __builtin_bit_cast(float, (unsigned)r[1]);
  unsigned uc = __builtin_bit_cast(unsigned, c);
  auto r2 = __builtin_amdgcn_permlane32_swap(uc, uc, false, false);
  c = __builtin_bit_cast(float, (unsigned)r2[0]) + __builtin_bit_cast(float, (unsigned)r2[1]);
  a = __builtin_bit_cast(float, __builtin_amdgcn_readlane(__builtin_bit_cast(int, c), 0));
  b = __builtin_bit_cast(float, __builtin_amdgcn_readlane(__builtin_bit_cast(int, c), 16));
}
__device__ __forceinline__ void ln_mod(const f2 (&xv)[4], const unsigned (&sc1)[4], const unsigned (&sh)[4], f2 (&h)[4]) {
  const float x00 = xv[0].x;
  const float piv = __builtin_bit_cast(float, __builtin_amdgcn_readfirstlane(__builtin_bit_cast(int, x00)));
  f2 d[4];
#pragma unroll
  for (int i = 0; i < 4; ++i) d[i] = xv[i] - piv;
  const f2 s2 = (d[0] + d[1]) + (d[2] + d[3]);
  const f2 q2 = (d[0] * d[0] + d[1] * d[1]) + (d[2] * d[2] + d[3] * d[3]);
  float s1 = s2.x + s2.y, sq = q2.x + q2.y;
  wave_sum2(s1, sq);
  const float dm = s1 * (1.0f / 512.0f);
  const float var = __builtin_fmaxf(sq * (1.0f / 512.0f) - dm * dm, 0.0f);
  const float mbp = rb1(piv + dm) - piv;
  const float sd = rb1(__builtin_amdgcn_sqrtf(rb1(rb1(var) + 1e-5f)));
  const float rs = __builtin_amdgcn_rcpf(sd);
#pragma unroll
  for (int i = 0; i < 4; ++i) h[i] = ((d[i] - mbp) * rs) * unpk(sc1[i]) + unpk(sh[i]);
}

struct Tw256 { f2 a[3], b[3], c[3]; int x2a, x2b, x3a, x3b; };
__device__ __forceinline__ void make_tw256(Tw256& t, int l) {
#pragma unroll
  for (int r = 1; r < 4; ++r) {
    t.a[r - 1] = tw2048(l * r * 8);
    t.b[r - 1] = tw2048((l & 15) * r * 32);
    t.c[r - 1] = tw2048((l & 3) * r * 128);
  }
  const int d2 = l >> 4, d1 = (l >> 2) & 3, d0 = l & 3;
  t.x2a = 80 * d2 + 4 * d1 + d0;
  t.x2b = 80 * d2 + 20 * d1 + d0;
  t.x3a = 80 * d2 + 4 * d1 + d0;
  t.x3b = 80 * d2 + 4 * d1 + 17 * d0;
}
#define PIN_TW(t) do { \
  asm volatile("" : "+v"(t.a[0]), "+v"(t.a[1]), "+v"(t.a[2]), "+v"(t.b[0]), "+v"(t.b[1]), "+v"(t.b[2]), "+v"(t.c[0]), "+v"(t.c[1]), "+v"(t.c[2])); \
  asm volatile("" : "+v"(t.x2a), "+v"(t.x2b), "+v"(t.x3a), "+v"(t.x3b)); } while (0)

__device__ __forceinline__ void fft256_fwd(f2 (&v)[4], const Tw256& t, f2* lds) {
  bfly4<false>(v[0], v[1], v[2], v[3]);
  cmul3<false>(v[1], v[2], v[3], t.a[0], t.a[1], t.a[2]);
  XL_PAD4(v);
  swap32(v[0], v[2]); swap32(v[1], v[3]);
  swap16(v[0], v[1]); swap16(v[2], v[3]);
  bfly4<false>(v[0], v[1], v[2], v[3]);
  cmul3<false>(v[1], v[2], v[3], t.b[0], t.b[1], t.b[2]);
#pragma unroll
  for (int r = 0; r < 4; ++r) lds[t.x2a + 20 * r] = v[r];
  __builtin_amdgcn_wave_barrier();
#pragma unroll
  for (int r = 0; r < 4; ++r) v[r] = lds[t.x2b + 4 * r];
  __builtin_amdgcn_wave_barrier();
  bfly4<false>(v[0], v[1], v[2], v[3]);
  cmul3<false>(v[1], v[2], v[3], t.c[0], t.c[1], t.c[2]);
#pragma unroll
  for (int r = 0; r < 4; ++r) lds[t.x3a + 17 * r] = v[r];
  __builtin_amdgcn_wave_barrier();
#pragma unroll
  for (int r = 0; r < 4; ++r) v[r] = lds[t.x3b + r];
  __builtin_amdgcn_wave_barrier();
  bfly4<false>(v[0], v[1], v[2], v[3]);
}
template <class H> __device__ __forceinline__ void fft256_inv(f2 (&v)[4], const Tw256& t, f2* lds, H hook) {
  bfly4<true>(v[0], v[1], v[2], v[3]);
#pragma unroll
  for (int r = 0; r < 4; ++r) lds[t.x3b + r] = v[r];
  __builtin_amdgcn_wave_barrier();
#pragma unroll
  for (int r = 0; r < 4; ++r) v[r] = lds[t.x3a + 17 * r];
  __builtin_amdgcn_wave_barrier();
  hook(std::integral_constant<int, 0>{});
  cmul3<true>(v[1], v[2], v[3], t.c[0], t.c[1], t.c[2]);
  bfly4<true>(v[0], v[1], v[2], v[3]);
#pragma unroll
  for (int r = 0; r < 4; ++r) lds[t.x2b + 4 * r] = v[r];
  __builtin_amdgcn_wave_barrier();
#pragma unroll
  for (int r = 0; r < 4; ++r) v[r] = lds[t.x2a + 20 * r];
  __builtin_amdgcn_wave_barrier();
  hook(std::integral_constant<int, 1>{});
  cmul3<true>(v[1], v[2], v[3], t.b[0], t.b[1], t.b[2]);
  bfly4<true>(v[0], v[1], v[2], v[3]);
  XL_PAD4(v);
  swap16(v[0], v[1]); swap16(v[2], v[3]);
  swap32(v[0], v[2]); swap32(v[1], v[3]);
  hook(std::integral_constant<int, 2>{});
  cmul3<true>(v[1], v[2], v[3], t.a[0], t.a[1], t.a[2]);
  bfly4<true>(v[0], v[1], v[2], v[3]);
}
template <int RP> __device__ __forceinline__ void fft256_inv2(f2 (&v)[2][4], const Tw256& t, f2* lds) {
#pragma unroll
  for (int u = 0; u < 2; ++u) bfly4<true>(v[u][0], v[u][1], v[u][2], v[u][3]);
#pragma unroll
  for (int u = 0; u < 2; ++u)
#pragma unroll
    for (int r = 0; r < 4; ++r) lds[t.x3b + u * RP + r] = v[u][r];
  __builtin_amdgcn_wave_barrier();
#pragma unroll
  for (int u = 0; u < 2; ++u)
#pragma unroll
    for (int r = 0; r < 4; ++r) v[u][r] = lds[t.x3a + u * RP + 17 * r];
  __builtin_amdgcn_wave_barrier();
#pragma unroll
  for (int u = 0; u < 2; ++u) {
    cmul3<true>(v[u][1], v[u][2], v[u][3], t.c[0], t.c[1], t.c[2]);
    bfly4<true>(v[u][0], v[u][1], v[u][2], v[u][3]);
#pragma unroll
    for (int r = 0; r < 4; ++r) lds[t.x2b + u * RP + 4 * r] = v[u][r];
  }
  __builtin_amdgcn_wave_barrier();
#pragma unroll
  for (int u = 0; u < 2; ++u)
#pragma unroll
    for (int r = 0; r < 4; ++r) v[u][r] = lds[t.x2a + u * RP + 20 * r];
  __builtin_amdgcn_wave_barrier();
#pragma unroll
  for (int u = 0; u < 2; ++u) {
    cmul3<true>(v[u][1], v[u][2], v[u][3], t.b[0], t.b[1], t.b[2]);
    bfly4<true>(v[u][0], v[u][1], v[u][2], v[u][3]);
  }
  XL_PAD4(v[0]); XL_PAD4(v[1]);
#pragma unroll
  for (int u = 0; u < 2; ++u) {
    swap16(v[u][0], v[u][1]); swap16(v[u][2], v[u][3]);
    swap32(v[u][0], v[u][2]); swap32(v[u][1], v[u][3]);
  }
#pragma unroll
  for (int u = 0; u < 2; ++u) {
    cmul3<true>(v[u][1], v[u][2], v[u][3], t.a[0], t.a[1], t.a[2]);
    bfly4<true>(v[u][0], v[u][1], v[u][2], v[u][3]);
  }
}
__device__ __forceinline__ int zpos(int k) { return k + (k >> 4); }

__device__ __host__ __forceinline__ int k2_k1(int tt, int kb) {
  int seq = (tt >> 6) * 8 + (tt & 7);
  int q = seq >> 2, k0 = seq & 3, ka = (tt >> 3) & 7;
  return 8 * (k0 + 4 * ka + 32 * kb) + q;
}

__device__ __forceinline__ void transpose8x8(const u4 (&in)[8], u4 (&out)[8]) {
#pragma unroll
  for (int c2 = 0; c2 < 4; ++c2) {
#pragma unroll
    for (int r2 = 0; r2 < 4; ++r2) {
      unsigned a = in[2 * r2][c2], b = in[2 * r2 + 1][c2];
      out[2 * c2][r2] = __builtin_amdgcn_perm(b, a, 0x05040100u);
      out[2 * c2 + 1][r2] = __builtin_amdgcn_perm(b, a, 0x07060302u);
    }
  }
}

#define K1_COAL 1
#define MOD_FILL_LATE 0
#define PREP_AUX 16
#define T_AUX 16
#define T2_AUX 16
#define WF_AUX 16
#define OUT_NT 0
constexpr int AUX_SC1 = 16;
constexpr int OOB = 0x40000000;
__device__ __forceinline__ __amdgpu_buffer_rsrc_t ws_rsrc(unsigned char* ws) {
  return __builtin_amdgcn_make_buffer_rsrc((void*)ws, 0, (int)WS_BYTES, 0x00020000);
}

__device__ __forceinline__ int prep_w1_off(int job, int l, int r) {
  const int cg = job % 7, ks = job / 7, no = l & 31, h = l >> 5;
  return ((16 * ks + 8 * h + r) * FF + cg * 256 + no * 8) * 2;
}
__device__ __forceinline__ void prep_w1_fin(const u4 (&in)[8], __amdgpu_buffer_rsrc_t rs, u4* buf, int job, int l) {
  const int cg = job % 7, ks = job / 7, no = l & 31, h = l >> 5;
  u4 out[8];
  transpose8x8(in, out);
#pragma unroll
  for (int cc = 0; cc < 8; ++cc) buf[((no >> 2) * 64 + 8 * (no & 3) + 32 * h + cc) ^ (no & 7)] = out[cc];
  __builtin_amdgcn_wave_barrier();
#pragma unroll
  for (int f = 0; f < 8; ++f)
    __builtin_amdgcn_raw_buffer_store_b128(buf[(f * 64 + l) ^ ((4 * f + ((l >> 3) & 3)) & 7)], rs,
                                           (int)OFF_W1F + (((cg * 8 + f) * 32 + ks) * 64 + l) * 16, 0, PREP_AUX);
}
__device__ __forceinline__ int prep_w2_off(int job, int l, int e) {
  const int cg = job & 1, cs = job >> 1, no = l & 31, h = l >> 5;
  return ((16 * cs + 8 * (e >> 2) + 4 * h + (e & 3)) * D + cg * 256 + no * 8) * 2;
}
__device__ __forceinline__ void prep_w2_fin(const u4 (&in)[8], __amdgpu_buffer_rsrc_t rs, u4* buf, int job, int l) {
  const int cg = job & 1, cs = job >> 1, no = l & 31, h = l >> 5;
  u4 out[8];
  transpose8x8(in, out);
  const int base = (no >> 2) * 64 + 16 * (no & 1) + 4 * ((no >> 1) & 1) + 32 * h;
#pragma unroll
  for (int cc = 0; cc < 8; ++cc) buf[base + (cc & 3) + 8 * (cc >> 2)] = out[cc];
  __builtin_amdgcn_wave_barrier();
#pragma unroll
  for (int f = 0; f < 8; ++f)
    __builtin_amdgcn_raw_buffer_store_b128(buf[f * 64 + l], rs,
                                           (int)OFF_W2F + ((((cg * 4 + (f >> 1)) * 112 + cs) * 2 + (f & 1)) * 64 + l) * 16, 0, PREP_AUX);
}

constexpr int K0_MOD = 96, K0_WF = 128, K0_MISC = 32;
constexpr int K0_BLOCKS = K0_MOD + K0_WF + K0_MISC;
constexpr unsigned SENT = 0xFFFFFFFFu;
constexpr int FILL_PER_THREAD = (int)((OFF_H2F - OFF_T) / (256 * 256 * 16));
static_assert((size_t)FILL_PER_THREAD * 256 * 256 * 16 == OFF_H2F - OFF_T && K0_BLOCKS == 256, "fill covers the spectrum hand-off buffer");
#define FILL_AUX 16

__global__ __launch_bounds__(256) void k0_prep(const unsigned short* __restrict__ cvec,
                                               const unsigned short* __restrict__ wmod,
                                               const unsigned short* __restrict__ wr,
                                               const unsigned short* __restrict__ wi,
                                               unsigned char* ws) {
  __shared__ float red[4][32];
  __shared__ u4 wbuf[1024];
  const int tid = threadIdx.x;
  int b = blockIdx.x;
  STAMP(0, 0);
  const __amdgpu_buffer_rsrc_t rs = ws_rsrc(ws);
  auto fill = [&]() {
#pragma unroll
    for (int i = 0; i < FILL_PER_THREAD; ++i)
      __builtin_amdgcn_raw_buffer_store_b128(u4{SENT, SENT, SENT, SENT}, rs,
                                             (int)OFF_T + ((blockIdx.x * FILL_PER_THREAD + i) * 256 + tid) * 16, 0, FILL_AUX);
  };
  if (b < K0_MOD) {
    const int co = tid & 3, rg = tid >> 2;
    u4 wv[8];
    unsigned short cs[8];
#pragma unroll
    for (int i = 0; i < 8; ++i) cs[i] = cvec[rg + 64 * i];
#pragma unroll
    for (int i = 0; i < 8; ++i)
      wv[i] = *(const u4*)(wmod + (size_t)(rg + 64 * i) * 3072 + b * 32 + co * 8);
    SCHED_FENCE();
    fill();
    SCHED_FENCE();
    f2 acc[4];
#pragma unroll
    for (int e = 0; e < 4; ++e) acc[e] = f2{0.f, 0.f};
#pragma unroll
    for (int i = 0; i < 8; ++i) {
      const float cv = bflo(cs[i]);
      const float sv = rb1(silu_f(cv));
#pragma unroll
      for (int e2 = 0; e2 < 4; ++e2) acc[e2] += sv * unpk(wv[i][e2]);
    }
    STAMP(0, 2);
#pragma unroll
    for (int e2 = 0; e2 < 4; ++e2) {
      float v0 = acc[e2].x, v1 = acc[e2].y;
      v0 += dppmov<0x124>(v0); v1 += dppmov<0x124>(v1);
      v0 += dppmov<0x128>(v0); v1 += dppmov<0x128>(v1);
      unsigned u0 = __builtin_bit_cast(unsigned, v0), u1 = __builtin_bit_cast(unsigned, v1);
      auto r = __builtin_amdgcn_permlane16_swap(u0, u1, false, false);
      float c = __builtin_bit_cast(float, (unsigned)r[0]) + __builtin_bit_cast(float, (unsigned)r[1]);
      unsigned uc = __builtin_bit_cast(unsigned, c);
      auto r2 = __builtin_amdgcn_permlane32_swap(uc, uc, false, false);
      c = __builtin_bit_cast(float, (unsigned)r2[0]) + __builtin_bit_cast(float, (unsigned)r2[1]);
      if ((tid & 0x2c) == 0) red[tid >> 6][co * 8 + e2 * 2 + ((tid >> 4) & 1)] = c;
    }
    __syncthreads();
    if (tid < 32) {
      float s = rb1((red[0][tid] + red[1][tid]) + (red[2][tid] + red[3][tid]));
      const int col = b * 32 + tid, p = col >> 9, e = col & 511;
      if (p == 1 || p == 4) s = rb1(s + 1.0f);
      const unsigned short sb = (unsigned short)(__builtin_bit_cast(unsigned, s) >> 16);
      const int n = e >> 1, l = n & 63, i = n >> 6, hf = e & 1;
      if (p < 2) ((unsigned short*)(ws + OFF_MODK1))[((l * 4 + i) * 2 + p) * 2 + hf] = sb;
      else if (p < 5) ((unsigned short*)(ws + OFF_MODK3))[((l * 4 + i) * 3 + (p == 2 ? 0 : (p == 4 ? 1 : 2))) * 2 + hf] = sb;
      else ((unsigned short*)(ws + OFF_GB))[e] = sb;
    }
    STAMP(0, 1);
    return;
  }
  b -= K0_MOD;
  if (b < K0_WF) {
    const int tq = b >> 1, k2 = (b & 1) * 128 + (tid & 127), tp2 = tid >> 7;
    unsigned short vr[16], vi[16];
#pragma unroll
    for (int e = 0; e < 16; ++e) {
      const int k1 = k2_k1(4 * tq + 2 * tp2 + (e >> 3), e & 7);
      vr[e] = wr[k1 * DF + k2];
      vi[e] = wi[k1 * DF + k2];
    }
    SCHED_FENCE();
    fill();
    SCHED_FENCE();
#pragma unroll
    for (int e4 = 0; e4 < 4; ++e4) {
      u4 o;
#pragma unroll
      for (int e = 0; e < 4; ++e) o[e] = (unsigned)vr[e4 * 4 + e] | ((unsigned)vi[e4 * 4 + e] << 16);
      wbuf[(tid & 127) * 8 + ((tp2 * 4 + e4) ^ (tid & 7))] = o;
    }
    __syncthreads();
#pragma unroll
    for (int i = 0; i < 4; ++i) {
      const int kl = i * 32 + (tid >> 3);
      __builtin_amdgcn_raw_buffer_store_b128(wbuf[kl * 8 + ((tid & 7) ^ (kl & 7))], rs,
                                             (int)OFF_WF + (((b & 1) * 128 + kl) * 512 + tq * 8 + (tid & 7)) * 16, 0, WF_AUX);
    }
    STAMP(0, 3);
    STAMP_DRAIN();
    STAMP(0, 5);
    return;
  }
  b -= K0_WF;
  fill();
  if (tid < 64) {
    const int e = b * 64 + tid;
    const int tt = e & 255, kb = e >> 8;
    const int k1 = k2_k1(tt, kb), nk = (2048 - k1) & 2047;
    f2 a0 = f2{bflo(wr[k1 * DF]), bflo(wi[k1 * DF])};
    f2 a1 = f2{bflo(wr[nk * DF]), -bflo(wi[nk * DF])};
    f2 b0 = f2{bflo(wr[k1 * DF + 256]), bflo(wi[k1 * DF + 256])};
    f2 b1 = f2{bflo(wr[nk * DF + 256]), -bflo(wi[nk * DF + 256])};
    f2 w0h = (a0 + a1) * 0.5f, w256h = (b0 + b1) * 0.5f;
    f2 P = (w0h + w256h) * 0.5f, Q = (w0h - w256h) * 0.5f;
    ((f4*)(ws + OFF_PQ))[e] = f4{P.x, P.y, Q.x, Q.y};
    STAMP(0, 4);
  }
}

constexpr unsigned SPIN_CAP = 1u << 18;
__device__ __forceinline__ unsigned nosent(unsigned v) { return v < SENT - 1u ? v : SENT - 1u; }
template <int N> __device__ __forceinline__ bool all_written(const u4* v) {
  unsigned m = 0u;
#pragma unroll
  for (int i = 0; i < N; ++i) {
    const unsigned a = v[i][0] > v[i][1] ? v[i][0] : v[i][1], c = v[i][2] > v[i][3] ? v[i][2] : v[i][3];
    m = m > a ? m : a;
    m = m > c ? m : c;
  }
  return __all(m != SENT);
}
template <int N> __device__ __forceinline__ void reload_until_written(u4* v, __amdgpu_buffer_rsrc_t rs, int off, int stride) {
  if (__builtin_expect(!all_written<N>(v), 0)) {
    unsigned s = 0;
    bool ok;
    do {
      __builtin_amdgcn_s_sleep(1);
      asm volatile("" : "+v"(off) :: "memory");
#pragma unroll
      for (int i = 0; i < N; ++i) v[i] = __builtin_amdgcn_raw_buffer_load_b128(rs, off + i * stride, 0, AUX_SC1);
      ok = all_written<N>(v);
    } while (!ok && ++s < SPIN_CAP);
  }
}

constexpr int SEQP = 64 + 1;
#define PREP_DELAY 60
#define C_DELAY 40
__device__ __forceinline__ void sync4(unsigned* cnt, unsigned& target, int l) {
  target += 4u;
  SCHED_FENCE();
  asm volatile("s_waitcnt lgkmcnt(0)" ::: "memory");
  if (l == 0) __hip_atomic_fetch_add(cnt, 1u, __ATOMIC_RELAXED, __HIP_MEMORY_SCOPE_WORKGROUP);
  for (unsigned sp = 0; sp < (SPIN_CAP << 4); ++sp)
    if (__hip_atomic_load(cnt, __ATOMIC_RELAXED, __HIP_MEMORY_SCOPE_WORKGROUP) >= target) break;
  asm volatile("" ::: "memory");
  SCHED_FENCE();
}
__device__ __forceinline__ void cols_body(unsigned char* ws, f2* lds, unsigned* cnt, int t) {
  unsigned tgt = 0u;
  const int b = blockIdx.x;
  const int l = t & 63;
  const __amdgpu_buffer_rsrc_t rs = ws_rsrc(ws);
  const int k2 = (b & 7) * 32 + (b >> 3);
  const int jr = t >> 2, qp = t & 3;
  const int co = (int)OFF_T + (jr * 1024 + k2 * 4 + qp) * 16;
  u4 wq[8];
  {
    const int wo = k2 != 0 ? (int)OFF_WF + (k2 * 256 + t) * 32 : (int)OFF_PQ + t * 16;
    const int ws1 = k2 != 0 ? 16 : 4096;
    wq[0] = __builtin_amdgcn_raw_buffer_load_b128(rs, wo, 0, 0);
    wq[1] = __builtin_amdgcn_raw_buffer_load_b128(rs, wo + ws1, 0, 0);
    const int po = k2 != 0 ? OOB : wo;
#pragma unroll
    for (int kb = 2; kb < 8; ++kb) wq[kb] = __builtin_amdgcn_raw_buffer_load_b128(rs, po + kb * 4096, 0, 0);
  }
  SCHED_FENCE();
  f2 twA[3], twB[7];
#pragma unroll
  for (int r = 1; r < 4; ++r) twA[r - 1] = tw2048(jr * r * 8);
  const int bb = l >> 3;
#pragma unroll
  for (int r = 1; r < 8; ++r) twB[r - 1] = tw2048(bb * r * 32);
  int pa = (8 * qp) * SEQP + jr;
  int pb = ((t >> 6) * 8 + (t & 7)) * SEQP + bb;
  asm volatile("" : "+v"(twB[0]), "+v"(twB[1]), "+v"(twB[2]), "+v"(twB[3]), "+v"(twB[4]), "+v"(twB[5]), "+v"(twB[6]), "+v"(twA[0]), "+v"(twA[1]), "+v"(twA[2]));
  asm volatile("" : "+v"(pa), "+v"(pb));
  SCHED_FENCE();
  __builtin_amdgcn_s_sleep(C_DELAY);
  asm volatile("" ::: "memory");
  u4 cin[4];
#pragma unroll
  for (int i = 0; i < 4; ++i) cin[i] = __builtin_amdgcn_raw_buffer_load_b128(rs, co + i * (1 << 20), 0, AUX_SC1);
  reload_until_written<4>(cin, rs, co, 1 << 20);
  SCHED_FENCE();
#pragma unroll
  for (int i = 0; i < 2; ++i)
    __builtin_amdgcn_raw_buffer_store_b128(u4{SENT, SENT, SENT, SENT}, rs, (int)OFF_H2F + (b * 512 + i * 256 + t) * 16, 0, FILL_AUX);
#pragma unroll
  for (int i = 0; i < 4; ++i)
    __builtin_amdgcn_raw_buffer_store_b128(u4{SENT, SENT, SENT, SENT}, rs,
                                           i * 256 + t < 896 ? (int)OFF_AF + ((256 + b) * 896 + i * 256 + t) * 16 : OOB, 0, FILL_AUX);
  SCHED_FENCE();
  STAMPT(2, 0, 256);
  f2 a0[4], a1[4];
#pragma unroll
  for (int i = 0; i < 4; ++i) { const f4 c = __builtin_bit_cast(f4, cin[i]); a0[i] = c.xy; a1[i] = c.zw; }
  bfly4<false>(a0[0], a0[1], a0[2], a0[3]);
  bfly4<false>(a1[0], a1[1], a1[2], a1[3]);
  cmul3<false>(a0[1], a0[2], a0[3], twA[0], twA[1], twA[2]);
  cmul3<false>(a1[1], a1[2], a1[3], twA[0], twA[1], twA[2]);
#pragma unroll
  for (int r = 0; r < 4; ++r) {
    lds[pa + r * SEQP] = a0[r];
    lds[pa + (4 + r) * SEQP] = a1[r];
  }
  sync4(cnt, tgt, l);
  STAMPT(2, 1, 256);
  f2 v[8];
#pragma unroll
  for (int a = 0; a < 8; ++a) v[a] = lds[pb + 8 * a];
  bfly8<false>(v);
  cmul7<false>(v, twB);
  XL_PAD8(v);
#pragma unroll
  for (int i = 0; i < 4; ++i) swap32(v[i], v[i + 4]);
  swap16(v[0], v[2]); swap16(v[1], v[3]); swap16(v[4], v[6]); swap16(v[5], v[7]);
#pragma unroll
  for (int i = 0; i < 8; i += 2) swap8(v[i], v[i + 1]);
  bfly8<false>(v);
  if (k2 != 0) {
    cmul4<false>(v[0], v[1], v[2], v[3], unpk(wq[0][0]), unpk(wq[0][1]), unpk(wq[0][2]), unpk(wq[0][3]));
    cmul4<false>(v[4], v[5], v[6], v[7], unpk(wq[1][0]), unpk(wq[1][1]), unpk(wq[1][2]), unpk(wq[1][3]));
  } else {
    sync4(cnt, tgt, l);
#pragma unroll
    for (int kb = 0; kb < 8; ++kb) lds[k2_k1(t, kb)] = v[kb];
    sync4(cnt, tgt, l);
#pragma unroll
    for (int kb = 0; kb < 8; ++kb) {
      f2 fn = lds[(2048 - k2_k1(t, kb)) & 2047];
      fn.y = -fn.y;
      const f4 c = __builtin_bit_cast(f4, wq[kb]);
      v[kb] = cfma(fn, c.zw, cmul(v[kb], c.xy));
    }
  }
  bfly8<true>(v);
  XL_PAD8(v);
#pragma unroll
  for (int i = 0; i < 8; i += 2) swap8(v[i], v[i + 1]);
  swap16(v[0], v[2]); swap16(v[1], v[3]); swap16(v[4], v[6]); swap16(v[5], v[7]);
#pragma unroll
  for (int i = 0; i < 4; ++i) swap32(v[i], v[i + 4]);
  cmul7<true>(v, twB);
  bfly8<true>(v);
  if (k2 == 0) sync4(cnt, tgt, l);
#pragma unroll
  for (int a = 0; a < 8; ++a) lds[pb + 8 * a] = v[a];
  sync4(cnt, tgt, l);
#pragma unroll
  for (int r = 0; r < 4; ++r) {
    a0[r] = lds[pa + r * SEQP];
    a1[r] = lds[pa + (4 + r) * SEQP];
  }
  cmul3<true>(a0[1], a0[2], a0[3], twA[0], twA[1], twA[2]);
  cmul3<true>(a1[1], a1[2], a1[3], twA[0], twA[1], twA[2]);
  bfly4<true>(a0[0], a0[1], a0[2], a0[3]);
  bfly4<true>(a1[0], a1[1], a1[2], a1[3]);
#pragma unroll
  for (int i = 0; i < 4; ++i)
    __builtin_amdgcn_raw_buffer_store_b128(__builtin_bit_cast(u4, f4{a0[i].x, a0[i].y, a1[i].x, a1[i].y}), rs, co + i * (1 << 20), 0, T2_AUX);
  STAMPT(2, 2, 256);
  STAMP_DRAIN();
  STAMPT(2, 3, 256);
}

constexpr int ROWP = 322;

__global__ __launch_bounds__(640) void k1_rows(const unsigned* __restrict__ x32, const unsigned short* __restrict__ w1,
                                               const unsigned short* __restrict__ w2, unsigned char* ws) {
  __shared__ __attribute__((aligned(16))) f2 lds[8 * ROWP];
  __shared__ u4 pbuf[1024], tbuf[1024];
  __shared__ __attribute__((aligned(16))) f2 ldc[2048 + 64];
  __shared__ unsigned ccnt;
  const int j = blockIdx.x;
  const int tid = threadIdx.x, w = __builtin_amdgcn_readfirstlane(tid >> 6), l = tid & 63;
  const __amdgpu_buffer_rsrc_t rs = ws_rsrc(ws);
  STAMP(1, 0);
  if (w >= 8) {
    if (j >= 224) return;
    u4 pin[8];
    const int jc = j;
    const unsigned char* src = (const unsigned char*)(w == 8 ? w1 : w2);
#pragma unroll
    for (int r = 0; r < 8; ++r) pin[r] = *(const u4*)(src + (w == 8 ? prep_w1_off(jc, l, r) : prep_w2_off(jc, l, r)));
    asm volatile("" ::: "memory");
    SCHED_FENCE();
    __builtin_amdgcn_s_barrier();
    __builtin_amdgcn_s_sleep(PREP_DELAY);
    SCHED_FENCE();
    if (w == 8) prep_w1_fin(pin, rs, pbuf, j, l);
    else prep_w2_fin(pin, rs, pbuf + 512, j, l);
    STAMPT(1, 5, 512);
    STAMP_DRAIN();
    STAMPT(1, 6, 512);
    return;
  }
  if (tid == 0) ccnt = 0u;
  const int s = j + 256 * w;
  unsigned xr[4], sc1[4], sh[4];
#pragma unroll
  for (int i = 0; i < 4; ++i) xr[i] = x32[(size_t)s * 256 + l + 64 * i];
  {
    const u4* mp = (const u4*)(ws + OFF_MODK1) + l * 2;
    const u4 m0 = mp[0], m1 = mp[1];
    sh[0] = m0[0]; sc1[0] = m0[1]; sh[1] = m0[2]; sc1[1] = m0[3];
    sh[2] = m1[0]; sc1[2] = m1[1]; sh[3] = m1[2]; sc1[3] = m1[3];
  }
#pragma unroll
  for (int i = 0; i < 2; ++i)
    __builtin_amdgcn_raw_buffer_store_b128(u4{SENT, SENT, SENT, SENT}, rs, w < 7 ? (int)OFF_AF + ((j * 14 + 2 * w + i) * 64 + l) * 16 : OOB, 0, FILL_AUX);
  SCHED_FENCE();
  Tw256 tw;
  make_tw256(tw, l);
  tw.x2a += w * ROWP; tw.x2b += w * ROWP; tw.x3a += w * ROWP; tw.x3b += w * ROWP;
  const int k2 = tid & 255;
  f2 t512 = tw2048(4 * k2 + 512);
  f2 twq[7];
#pragma unroll
  for (int q = 1; q < 8; ++q) twq[q - 1] = tw2048(j * q);
  int zw = w * ROWP + (l >> 4) + 4 * ((l >> 2) & 3) + 17 * (l & 3);
  int za = zpos(k2), zb = zpos((256 - k2) & 255);
  int to = (int)OFF_T + (j * 256 + k2) * 64;
  asm volatile("" : "+v"(twq[0]), "+v"(twq[1]), "+v"(twq[2]), "+v"(twq[3]), "+v"(twq[4]), "+v"(twq[5]), "+v"(twq[6]), "+v"(t512));
  asm volatile("" : "+v"(zw), "+v"(za), "+v"(zb), "+v"(to));
  PIN_TW(tw);
  SCHED_FENCE();
  f2 xv[4], v[4];
#pragma unroll
  for (int i = 0; i < 4; ++i) xv[i] = unpk(xr[i]);
  ln_mod(xv, sc1, sh, v);
  STAMP(1, 1);
  fft256_fwd(v, tw, lds);
  STAMP(1, 2);
#pragma unroll
  for (int r = 0; r < 4; ++r) lds[zw + 68 * r] = v[r];
  __syncthreads();
  if (w < 4) {
    f2 a[8], z[8], zr[8];
#pragma unroll
    for (int m = 0; m < 8; ++m) { z[m] = lds[m * ROWP + za]; zr[m] = lds[m * ROWP + zb]; }
#pragma unroll
    for (int m = 0; m < 8; m += 2) herm2(a[m], a[m + 1], z[m], zr[m], t512, z[m + 1], zr[m + 1], t512);
    if (w == 0) {
#pragma unroll
      for (int m = 0; m < 8; ++m) a[m].y = k2 == 0 ? 2.0f * (z[m].x - z[m].y) : a[m].y;
    }
    bfly8<false>(a);
    cmul7<false>(a, twq);
    {
      u4* tb = tbuf + w * 256;
#pragma unroll
      for (int q = 0; q < 4; ++q)
      {
        const u4 o = __builtin_bit_cast(u4, f4{a[2 * q].x, a[2 * q].y, a[2 * q + 1].x, a[2 * q + 1].y});
        tb[l * 4 + (q ^ ((l >> 1) & 3))] = u4{nosent(o[0]), nosent(o[1]), nosent(o[2]), nosent(o[3])};
      }
      __builtin_amdgcn_wave_barrier();
#pragma unroll
      for (int i = 0; i < 4; ++i) {
        const int src = 16 * i + (l >> 2);
        __builtin_amdgcn_raw_buffer_store_b128(tb[src * 4 + ((l & 3) ^ ((src >> 1) & 3))], rs,
                                               (int)OFF_T + (j * 1024 + w * 256 + i * 64 + l) * 16, 0, AUX_SC1);
      }
    }
    STAMP(1, 3);
    STAMP_DRAIN();
    STAMP(1, 4);
  } else {
    cols_body(ws, ldc, &ccnt, tid - 256);
  }
}

constexpr int L_H2 = 0;
constexpr int L_R01 = 65536;
constexpr int L_STG = 131072;
constexpr int LDS3_BYTES = 131072 + 8 * ROWP * 8;
#define P_N1 12
#define P_W2A 12
#define W2B 20
#define FLAGSTEP 48
#define A0STEP 16
#define POLLSTEP 62
#define A2STEP 7
#define NPARK 8
#define F_W2A 8
#define F_L1NB 1
#define P_N2 16
#define P_N3 24
#define F_DELAY2 12
#define ENDBAR 1
#define S_DELAY2 24
#define A_ST 49
#define L1_EARLY(c) (!ENDBAR && (c) == 2)
#define P_L1NB 1
#define MIDBAR 1
#define S_DELAY 30
#define P_PRIO 0
#define F_DELAY 2
#define P_DELAY 4
#define BAR() do { SCHED_FENCE(); asm volatile("s_waitcnt lgkmcnt(0)" ::: "memory"); __builtin_amdgcn_s_barrier(); asm volatile("" ::: "memory"); SCHED_FENCE(); } while (0)

template <int NTB> __device__ __forceinline__ void wait_flags(unsigned v, gu32* word) {
  if (__builtin_expect(!__all(v >= (unsigned)NTB), 0)) {
    unsigned s = 0;
    do {
      __builtin_amdgcn_s_sleep(2);
      v = __hip_atomic_load(word, RLX_AGENT);
    } while (!__all(v >= (unsigned)NTB) && ++s < SPIN_CAP);
  }
  __builtin_amdgcn_fence(__ATOMIC_ACQUIRE, "wavefront");
  asm volatile("" ::: "memory");
}
template <int HAVE> constexpr int w2_at(int st) {
  if (st < 32) { const int f = HAVE + st; return f < W2B ? f : -1; }
  const int f = W2B + (st - 33);
  return (st >= 33 && f < 28) ? f : -1;
}
template <int HAVE> constexpr int vm_since(int stEnd) {
  int n = 0;
  for (int st = 32; st <= stEnd; ++st) n += (w2_at<HAVE>(st) >= 0 ? 1 : 0) + (st == POLLSTEP ? 1 : 0);
  return n;
}
static_assert(W2B <= 22 && FLAGSTEP >= 32 && FLAGSTEP < 64, "schedule");

__device__ __forceinline__ void dma16(u4 srd, int voff, int soff, unsigned dst) {
  unsigned keep;
  asm volatile("s_mov_b32 %0, m0\n\ts_mov_b32 m0, %1\n\ts_nop 0\n\tbuffer_load_dwordx4 %2, %3, %4 offen lds\n\ts_mov_b32 m0, %0"
               : "=&s"(keep) : "s"(dst), "v"(voff), "s"(srd), "s"(soff) : "memory");
}
template <int I0, int I1> __device__ __forceinline__ void dma_run(u4 srd, int voff, unsigned dst) {
#pragma unroll
  for (int i = I0; i < I1; ++i) dma16(srd, voff, i * 1024, dst + i * 1024);
}
#define VM_WAIT(n) do { SCHED_FENCE(); asm volatile("s_waitcnt vmcnt(%0)" :: "i"(n) : "memory"); SCHED_FENCE(); } while (0)
__device__ __forceinline__ u4 make_srd(const void* p, unsigned bytes) {
  const unsigned long long a = (unsigned long long)p;
  return u4{(unsigned)a, (unsigned)(a >> 32) & 0xffffu, bytes, 0x00020000u};
}
#define LDW(arr, i, off) arr[i] = __builtin_amdgcn_raw_buffer_load_b128(rs, (off) + (i) * 1024, 0, 0)

template <int CLS>
__device__ __forceinline__ void k3_body(const unsigned* __restrict__ x32, unsigned char* ws,
                                        unsigned short* __restrict__ out, unsigned char* smem, int w) {
  const int b = blockIdx.x;
  const int g = (b & 7) + 8 * (b >> 6), jj = (b >> 3) & 7;
  const int j = 8 * g + jj;
  const int tid = threadIdx.x, l = tid & 63;
  const __amdgpu_buffer_rsrc_t rs = ws_rsrc(ws);
  f2* lds = (f2*)(smem + L_STG);
  u4 wv[32], uv[28];
  constexpr bool FW = CLS == 0;
  const int wo = (int)OFF_W1F + ((7 * jj + w) * 2048 + l) * 16;
  const int uo = (int)OFF_W2F + ((jj * 56 + 7 * w) * 256 + l) * 16;
  constexpr int HAVE = FW ? F_W2A : (CLS == 1 ? 0 : 28);
  if (FW) {
    u4 tin[4];
#pragma unroll
    for (int q = 0; q < 4; ++q) tin[q] = __builtin_amdgcn_raw_buffer_load_b128(rs, (int)OFF_T + (j * 256 + tid) * 64 + q * 16, 0, 0);
    unsigned xr[2][4];
#pragma unroll
    for (int u = 0; u < 2; ++u)
#pragma unroll
      for (int i = 0; i < 4; ++i) xr[u][i] = x32[(size_t)(j + 256 * (2 * w + u)) * 256 + l + 64 * i];
    u4 mk[3];
#pragma unroll
    for (int i = 0; i < 3; ++i) mk[i] = ((const u4*)(ws + OFF_MODK3))[l * 3 + i];
    SCHED_FENCE();
    const int kl = (l >> 4) + 4 * ((l >> 2) & 3) + 16 * (l & 3);
    Tw256 tw;
    make_tw256(tw, l);
    tw.x2a += 2 * w * ROWP; tw.x2b += 2 * w * ROWP; tw.x3a += 2 * w * ROWP; tw.x3b += 2 * w * ROWP;
    f2 t512[4];
    int zk[4], zn[4];
#pragma unroll
    for (int i = 0; i < 4; ++i) {
      t512[i] = tw2048(-4 * (kl + 64 * i) - 512);
      zk[i] = 2 * w * ROWP + zpos(kl + 64 * i);
      zn[i] = 2 * w * ROWP + zpos((256 - kl - 64 * i) & 255);
    }
    f2 twq[7];
#pragma unroll
    for (int q = 1; q < 8; ++q) twq[q - 1] = tw2048(j * q);
    int zc = zpos(tid);
    asm volatile("" : "+v"(twq[0]), "+v"(twq[1]), "+v"(twq[2]), "+v"(twq[3]), "+v"(twq[4]), "+v"(twq[5]), "+v"(twq[6]));
    asm volatile("" : "+v"(t512[0]), "+v"(t512[1]), "+v"(t512[2]), "+v"(t512[3]), "+v"(zc));
    asm volatile("" : "+v"(zk[0]), "+v"(zk[1]), "+v"(zk[2]), "+v"(zk[3]), "+v"(zn[0]), "+v"(zn[1]), "+v"(zn[2]), "+v"(zn[3]));
    PIN_TW(tw);
    SCHED_FENCE();
    {
      f2 a[8];
#pragma unroll
      for (int q = 0; q < 4; ++q) { const f4 c = __builtin_bit_cast(f4, tin[q]); a[2 * q] = c.xy; a[2 * q + 1] = c.zw; }
      cmul7<true>(a, twq);
      bfly8<true>(a);
#pragma unroll
      for (int m = 0; m < 8; ++m) lds[m * ROWP + zc] = a[m];
    }
    BAR();
    STAMP(3, 1);
    f2 v[2][4];
#pragma unroll
    for (int u = 0; u < 2; ++u) {
      f2 xk[4], xn[4];
#pragma unroll
      for (int r = 0; r < 4; ++r) { xk[r] = lds[zk[r] + u * ROWP]; xn[r] = lds[zn[r] + u * ROWP]; }
      herm2(v[u][0], v[u][1], xk[0], xn[0], t512[0], xk[1], xn[1], t512[1]);
      herm2(v[u][2], v[u][3], xk[2], xn[2], t512[2], xk[3], xn[3], t512[3]);
      if (l == 0) v[u][0] = f2{xk[0].x + xk[0].y, xk[0].x - xk[0].y};
    }
    __builtin_amdgcn_wave_barrier();
    fft256_inv2<ROWP>(v, tw, lds);
    unsigned sc1[4], sh[4];
    f2 ga[4];
#pragma unroll
    for (int i = 0; i < 4; ++i) {
      ga[i] = unpk(mk[(3 * i) >> 2][(3 * i) & 3]) * 4.76837158203125e-07f;
      sc1[i] = mk[(3 * i + 1) >> 2][(3 * i + 1) & 3];
      sh[i] = mk[(3 * i + 2) >> 2][(3 * i + 2) & 3];
    }
#pragma unroll
    for (int u = 0; u < 2; ++u) {
      f2 x1[4], h2[4];
      unsigned x1p[4];
#pragma unroll
      for (int i = 0; i < 4; ++i) {
        x1p[i] = pk(unpk(xr[u][i]) + ga[i] * v[u][i]);
        x1[i] = unpk(x1p[i]);
      }
      ln_mod(x1, sc1, sh, h2);
      unsigned* st = (unsigned*)(lds + (2 * w + u) * ROWP);
#pragma unroll
      for (int i = 0; i < 4; ++i) {
        st[l + 64 * i] = x1p[i];
        st[256 + l + 64 * i] = nosent(pk(h2[i]));
      }
    }
    BAR();
    STAMP(3, 2);
    __builtin_amdgcn_s_sleep(F_DELAY);
#pragma unroll
    for (int u = 0; u < 2; ++u)
      __builtin_amdgcn_raw_buffer_store_b128(*(const u4*)((const unsigned*)(lds + (2 * w + u) * ROWP) + 4 * l), rs,
                                             (int)OFF_X1 + ((64 * g + 8 * jj + 2 * w + u) * 64 + l) * 16, 0, AUX_SC1);
    BAR();
#pragma unroll
    for (int i = 0; i < 32; ++i) wv[i] = *(const u4*)(smem + (w * 32 + i) * 1024 + l * 16);
    BAR();
    __builtin_amdgcn_s_sleep(F_DELAY2);
#pragma unroll
    for (int i = 0; i < F_W2A; ++i) LDW(uv, i, uo);
    STAMP(3, 3);
    BAR();
  } else {
    const int pw = w - 4;
    const u4 srd = make_srd(ws, (unsigned)WS_BYTES);
    const int po = (int)OFF_W1F + ((7 * jj + pw) * 2048 + l) * 16;
    dma_run<0, P_N1>(srd, po, pw * 32768);
    BAR();
    dma_run<P_N1, 32>(srd, po, pw * 32768);
    if (CLS == 1) {
#pragma unroll
      for (int i = 0; i < P_N2; ++i) LDW(wv, i, wo);
    } else {
#pragma unroll
      for (int i = 0; i < P_N2; ++i) LDW(uv, i, uo);
    }
    BAR();
    __builtin_amdgcn_s_setprio(3);
    {
      u4 ho[2];
#pragma unroll
      for (int u = 0; u < 2; ++u) {
        const int ch = 8 * (2 * pw + u) + (l >> 3);
        ho[u] = *(const u4*)((const unsigned*)(lds + (l & 7) * ROWP) + 256 + 4 * ch);
      }
      const int ha = (int)OFF_H2F + (((g * 2 + (jj >> 2)) * 32 + 8 * pw + (l >> 4)) * 64 + 8 * (jj & 3) + (l & 7) + 32 * ((l >> 3) & 1)) * 16;
#pragma unroll
      for (int u = 0; u < 2; ++u) __builtin_amdgcn_raw_buffer_store_b128(ho[u], rs, ha + u * 4096, 0, AUX_SC1);
    }
    VM_WAIT(P_N2 + 2);
    BAR();
    BAR();
    if (CLS == 1) {
#pragma unroll
      for (int i = P_N2; i < P_N3; ++i) LDW(wv, i, wo);
    } else {
#pragma unroll
      for (int i = P_N2; i < (P_N3 < 28 ? P_N3 : 28); ++i) LDW(uv, i, uo);
    }
    SCHED_FENCE();
    {
      const int hg = (int)OFF_H2F + g * 65536, hoff = (pw * 1024 + l) * 16;
      u4 hv[16];
      __builtin_amdgcn_s_sleep(P_DELAY);
      asm volatile("" ::: "memory");
#pragma unroll
      for (int i = 0; i < 16; ++i) hv[i] = __builtin_amdgcn_raw_buffer_load_b128(rs, hg + hoff + i * 1024, 0, AUX_SC1);
      STAMPT(3, 4, 256);
      SCHED_FENCE();
      if (CLS == 1) {
#pragma unroll
        for (int i = P_N3; i < 32; ++i) LDW(wv, i, wo);
      } else {
#pragma unroll
        for (int i = P_N3; i < 28; ++i) LDW(uv, i, uo);
      }
      SCHED_FENCE();
      reload_until_written<16>(hv, rs, hg + hoff, 1024);
#pragma unroll
      for (int i = 0; i < 16; ++i) *(u4*)(smem + L_H2 + hoff + i * 1024) = hv[i];
    }
    STAMPT(3, 8, 256);
    BAR();
  }
  STAMP(3, 5); STAMPT(4, 5, 256);
  const int aro = (int)OFF_AF + ((g * 8 + w) * 28 * 64 + l) * 16;
  u4 av[28];
#define LDA(i) av[i] = __builtin_amdgcn_raw_buffer_load_b128(rs, aro + (i) * 1024, 0, AUX_SC1)
  if (CLS != 2) {
    const u4* hs = (const u4*)(smem + L_H2);
    u4 hb[4];
    const int afo = (int)OFF_AF + (((g * 8 + jj) * 28 + 2 * w) * 64 + l) * 16;
#pragma unroll
    for (int st = 0; st < 3; ++st) hb[st] = hs[st * 64 + l];
#pragma unroll
    for (int th = 0; th < 2; ++th) {
      f16v acc;
#pragma unroll
      for (int i = 0; i < 16; ++i) acc[i] = 0.f;
      if (th == 0) __builtin_amdgcn_s_setprio(3); else __builtin_amdgcn_s_setprio(2);
#pragma unroll
      for (int ks = 0; ks < 32; ++ks) {
        const int st = th * 32 + ks;
        if (st + 3 < 64) hb[(st + 3) & 3] = hs[(st + 3) * 64 + l];
        if (w2_at<HAVE>(st) >= 0) LDW(uv, w2_at<HAVE>(st) < 0 ? 0 : w2_at<HAVE>(st), uo);
        if (st >= A_ST && st < A_ST + 14) LDA(st >= A_ST && st < A_ST + 14 ? st - A_ST : 0);
        SCHED_FENCE();
        acc = mfma32(wv[ks], hb[st & 3], acc);
        SCHED_FENCE();
      }
      if (FW && th == 0) asm volatile("s_waitcnt vmcnt(%0)" :: "i"(W2B - HAVE) : "memory");
#pragma unroll
      for (int sk = 0; sk < 2; ++sk) {
        u4 o;
#pragma unroll
        for (int e = 0; e < 4; ++e) o[e] = nosent(pk(f2{silu_f(acc[8 * sk + 2 * e]), silu_f(acc[8 * sk + 2 * e + 1])}));
        __builtin_amdgcn_raw_buffer_store_b128(o, rs, afo + (th * 14 + sk) * 1024, 0, AUX_SC1);
      }
      SCHED_FENCE();
      if (th == 0) { __builtin_amdgcn_s_barrier(); SCHED_FENCE(); }
    }
    __builtin_amdgcn_s_setprio(0);
    STAMP(3, 6); STAMPT(4, 6, 256);
    __builtin_amdgcn_s_barrier();
    SCHED_FENCE();
  } else {
    __builtin_amdgcn_s_setprio(0);
    __builtin_amdgcn_s_barrier();
    __builtin_amdgcn_s_sleep(S_DELAY);
  }
  asm volatile("" ::: "memory");
  __builtin_amdgcn_s_setprio(3);
#pragma unroll
  for (int i = (CLS == 2 ? 0 : 14); i < (L1_EARLY(CLS) ? 28 : 14); ++i) LDA(i);
  __builtin_amdgcn_s_setprio(0);
  SCHED_FENCE();
  const int thf = w >> 2, nbf = (w >> 1) & 1, hff = w & 1;
  const int tok = 32 * thf + (l & 31);
  const int col = 64 * jj + 32 * nbf + 16 * (l >> 5) + 8 * hff;
  u4 x1v, gbv;
#pragma unroll
  for (int th = 0; th < 2; ++th) {
    if (th == 1) {
      BAR();
      STAMP(3, 7); STAMPT(4, 7, 256);
      x1v = __builtin_amdgcn_raw_buffer_load_b128(rs, (int)OFF_X1 + ((64 * g + tok) * 64 + (col >> 3)) * 16, 0, AUX_SC1);
      gbv = __builtin_amdgcn_raw_buffer_load_b128(rs, (int)OFF_GB + col * 2, 0, 0);
      SCHED_FENCE();
    }
    reload_until_written<14>(av + th * 14, rs, aro + th * 14 * 1024, 1024);
    SCHED_FENCE();
    if (th == 0) { STAMP(3, 12); STAMPT(4, 12, 256); }
    f4* red = (f4*)(smem + (th == 0 ? L_R01 : L_H2)) + w * 512 + l;
#pragma unroll
    for (int nb = 0; nb < 2; ++nb) {
      f16v acc;
#pragma unroll
      for (int i = 0; i < 16; ++i) acc[i] = 0.f;
#pragma unroll
      for (int k = 0; k < 14; ++k) acc = mfma32(uv[2 * k + nb], av[th * 14 + k], acc);
      if (!L1_EARLY(CLS) && th == 0 && nb == (FW ? F_L1NB : (CLS == 1 ? P_L1NB : 1))) {
        SCHED_FENCE();
        if (CLS == 2) { __builtin_amdgcn_s_barrier(); __builtin_amdgcn_s_sleep(S_DELAY2); SCHED_FENCE(); }
        __builtin_amdgcn_s_setprio(3);
#pragma unroll
        for (int i = 14; i < 28; ++i) LDA(i);
        __builtin_amdgcn_s_setprio(0);
        SCHED_FENCE();
      }
#pragma unroll
      for (int q4 = 0; q4 < 4; ++q4)
        red[(nb * 4 + q4) * 64] = f4{acc[4 * q4], acc[4 * q4 + 1], acc[4 * q4 + 2], acc[4 * q4 + 3]};
      SCHED_FENCE();
    }
  }
  STAMP(3, 9); STAMPT(4, 9, 256);
  BAR();
  f4 s0 = f4{0.f, 0.f, 0.f, 0.f}, s1 = s0;
  {
    const f4* red = (const f4*)(smem + (thf == 0 ? L_R01 : L_H2)) + (nbf * 4 + 2 * hff) * 64 + l;
#pragma unroll
    for (int ww = 0; ww < 8; ++ww) {
      s0 += red[ww * 512];
      s1 += red[ww * 512 + 64];
    }
  }
  u4 o;
  o[0] = pk(unpk(x1v[0]) + unpk(gbv[0]) * s0.xy);
  o[1] = pk(unpk(x1v[1]) + unpk(gbv[1]) * s0.zw);
  o[2] = pk(unpk(x1v[2]) + unpk(gbv[2]) * s1.xy);
  o[3] = pk(unpk(x1v[3]) + unpk(gbv[3]) * s1.zw);
  const int srow = 8 * g + (tok >> 3) + 256 * (tok & 7);
  *(u4*)(out + (size_t)srow * D + col) = o;
  STAMP(3, 10); STAMPT(4, 10, 256);
  STAMP_DRAIN();
  STAMP(3, 11); STAMPT(4, 11, 256);
}

__global__ __launch_bounds__(512, 2) void k3_fused(const unsigned* __restrict__ x32, unsigned char* ws,
                                                   unsigned short* __restrict__ out) {
  extern __shared__ __attribute__((aligned(16))) unsigned char smem[];
  const int w = __builtin_amdgcn_readfirstlane(threadIdx.x >> 6);
  STAMP(3, 0);
  if (w < 4) k3_body<0>(x32, ws, out, smem, w);
  else if (w < 7) k3_body<1>(x32, ws, out, smem, w);
  else k3_body<2>(x32, ws, out, smem, w);
}

}

extern "C" void kernel_launch(void* const* d_in, const int* in_sizes, int n_in, void* d_out, int out_size,
                              void* d_ws, size_t ws_size, hipStream_t stream) {
  const unsigned short* x = (const unsigned short*)d_in[0];
  const unsigned short* wr = (const unsigned short*)d_in[1];
  const unsigned short* wi = (const unsigned short*)d_in[2];
  const unsigned short* w1 = (const unsigned short*)d_in[4];
  const unsigned short* w2 = (const unsigned short*)d_in[5];
  const unsigned short* cv = (const unsigned short*)d_in[6];
  const unsigned short* wm = (const unsigned short*)d_in[7];
  unsigned char* ws = (unsigned char*)d_ws;
  (void)hipFuncSetAttribute((const void*)k3_fused, hipFuncAttributeMaxDynamicSharedMemorySize, LDS3_BYTES);
  k0_prep<<<K0_BLOCKS, 256, 0, stream>>>(cv, wm, wr, wi, ws);
  k1_rows<<<256, 640, 0, stream>>>((const unsigned*)x, w1, w2, ws);
  k3_fused<<<256, 512, LDS3_BYTES, stream>>>((const unsigned*)x, ws, (unsigned short*)d_out);
}
```

```cpp
#include <hip/hip_runtime.h>
#include <hip/hip_bf16.h>
#include <stdint.h>
#include <type_traits>

namespace {

typedef float f2 __attribute__((ext_vector_type(2)));
typedef float f4 __attribute__((ext_vector_type(4)));
typedef unsigned u2 __attribute__((ext_vector_type(2)));
typedef unsigned u4 __attribute__((ext_vector_type(4)));
typedef short s8 __attribute__((ext_vector_type(8)));
typedef __bf16 bf2 __attribute__((ext_vector_type(2)));
#define GAS __attribute__((address_space(1)))
typedef GAS unsigned gu32;
#define RLX_AGENT __ATOMIC_RELAXED, __HIP_MEMORY_SCOPE_AGENT
#define SCHED_FENCE() __builtin_amdgcn_sched_barrier(0)

constexpr int S = 2048, D = 512, DF = 257, FF = 1792;

constexpr size_t OFF_F2    = 0;
constexpr size_t OFF_MODK1 = 262144;
constexpr size_t OFF_MODK3 = OFF_MODK1 + 2048;
constexpr size_t OFF_GB    = OFF_MODK3 + 3072;
constexpr size_t OFF_PQ    = OFF_MODK1 + 8192;
constexpr size_t OFF_WF    = OFF_PQ + 32768;
constexpr size_t OFF_T     = OFF_WF + (size_t)256 * 2048 * 4;
constexpr size_t OFF_H2F   = OFF_T + (size_t)256 * 2048 * 8;
constexpr size_t OFF_X1    = OFF_H2F + (size_t)S * D * 2;
constexpr size_t OFF_AF    = OFF_X1 + (size_t)S * D * 2;
constexpr size_t OFF_W1F   = OFF_AF + (size_t)S * FF * 2;
constexpr size_t OFF_W2F   = OFF_W1F + (size_t)D * FF * 2;
constexpr size_t WS_BYTES  = OFF_W2F + (size_t)D * FF * 2;

#define STAMP(kid, i) do {} while (0)
#define STAMPT(kid, i, thr) do {} while (0)
#define STAMP_DRAIN() do {} while (0)

__device__ __forceinline__ float bflo(unsigned u) { return __builtin_bit_cast(float, u << 16); }
__device__ __forceinline__ float bfhi(unsigned u) { return __builtin_bit_cast(float, u & 0xffff0000u); }
__device__ __forceinline__ f2 unpk(unsigned u) { return f2{bflo(u), bfhi(u)}; }
__device__ __forceinline__ unsigned pk(f2 v) {
  bf2 b = __builtin_convertvector(v, bf2);
  return __builtin_bit_cast(unsigned, b);
}
__device__ __forceinline__ f2 rb2(f2 v) { return unpk(pk(v)); }
__device__ __forceinline__ float rb1(float v) { return bflo(pk(f2{v, v})); }

__device__ __forceinline__ f2 tw2048(int t) {
  const float f = (float)t * (1.0f / 2048.0f);
  return f2{__builtin_amdgcn_cosf(f), -__builtin_amdgcn_sinf(f)};
}

__device__ __forceinline__ f2 cmul(f2 a, f2 w) {
  f2 t, d;
  asm("v_pk_mul_f32 %0, %1, %2 op_sel:[1,1] op_sel_hi:[1,0] neg_lo:[0,1]" : "=v"(t) : "v"(a), "v"(w));
  asm("v_pk_fma_f32 %0, %1, %2, %3 op_sel:[0,0,0] op_sel_hi:[0,1,1]" : "=v"(d) : "v"(a), "v"(w), "v"(t));
  return d;
}
__device__ __forceinline__ f2 cmulc(f2 a, f2 w) {
  f2 t, d;
  asm("v_pk_mul_f32 %0, %1, %2 op_sel:[0,0] op_sel_hi:[1,0]" : "=v"(t) : "v"(a), "v"(w));
  asm("v_pk_fma_f32 %0, %1, %2, %3 op_sel:[1,1,0] op_sel_hi:[0,1,1] neg_hi:[1,0,0]" : "=v"(d) : "v"(a), "v"(w), "v"(t));
  return d;
}
template <bool CONJ> __device__ __forceinline__ void cmul3(f2& a0, f2& a1, f2& a2, f2 w0, f2 w1, f2 w2) {
  f2 t0, t1, t2;
  if (!CONJ) {
    asm("v_pk_mul_f32 %3, %0, %6 op_sel:[1,1] op_sel_hi:[1,0] neg_lo:[0,1]\n\t"
        "v_pk_mul_f32 %4, %1, %7 op_sel:[1,1] op_sel_hi:[1,0] neg_lo:[0,1]\n\t"
        "v_pk_mul_f32 %5, %2, %8 op_sel:[1,1] op_sel_hi:[1,0] neg_lo:[0,1]\n\t"
        "v_pk_fma_f32 %0, %0, %6, %3 op_sel:[0,0,0] op_sel_hi:[0,1,1]\n\t"
        "v_pk_fma_f32 %1, %1, %7, %4 op_sel:[0,0,0] op_sel_hi:[0,1,1]\n\t"
        "v_pk_fma_f32 %2, %2, %8, %5 op_sel:[0,0,0] op_sel_hi:[0,1,1]"
        : "+v"(a0), "+v"(a1), "+v"(a2), "=&v"(t0), "=&v"(t1), "=&v"(t2) : "v"(w0), "v"(w1), "v"(w2));
  } else {
    asm("v_pk_mul_f32 %3, %0, %6 op_sel:[0,0] op_sel_hi:[1,0]\n\t"
        "v_pk_mul_f32 %4, %1, %7 op_sel:[0,0] op_sel_hi:[1,0]\n\t"
        "v_pk_mul_f32 %5, %2, %8 op_sel:[0,0] op_sel_hi:[1,0]\n\t"
        "v_pk_fma_f32 %0, %0, %6, %3 op_sel:[1,1,0] op_sel_hi:[0,1,1] neg_hi:[1,0,0]\n\t"
        "v_pk_fma_f32 %1, %1, %7, %4 op_sel:[1,1,0] op_sel_hi:[0,1,1] neg_hi:[1,0,0]\n\t"
        "v_pk_fma_f32 %2, %2, %8, %5 op_sel:[1,1,0] op_sel_hi:[0,1,1] neg_hi:[1,0,0]"
        : "+v"(a0), "+v"(a1), "+v"(a2), "=&v"(t0), "=&v"(t1), "=&v"(t2) : "v"(w0), "v"(w1), "v"(w2));
  }
}
template <bool CONJ> __device__ __forceinline__ void cmul4(f2& a0, f2& a1, f2& a2, f2& a3, f2 w0, f2 w1, f2 w2, f2 w3) {
  f2 t0, t1, t2, t3;
  if (!CONJ) {
    asm("v_pk_mul_f32 %4, %0, %8 op_sel:[1,1] op_sel_hi:[1,0] neg_lo:[0,1]\n\t"
        "v_pk_mul_f32 %5, %1, %9 op_sel:[1,1] op_sel_hi:[1,0] neg_lo:[0,1]\n\t"
        "v_pk_mul_f32 %6, %2, %10 op_sel:[1,1] op_sel_hi:[1,0] neg_lo:[0,1]\n\t"
        "v_pk_mul_f32 %7, %3, %11 op_sel:[1,1] op_sel_hi:[1,0] neg_lo:[0,1]\n\t"
        "v_pk_fma_f32 %0, %0, %8, %4 op_sel:[0,0,0] op_sel_hi:[0,1,1]\n\t"
        "v_pk_fma_f32 %1, %1, %9, %5 op_sel:[0,0,0] op_sel_hi:[0,1,1]\n\t"
        "v_pk_fma_f32 %2, %2, %10, %6 op_sel:[0,0,0] op_sel_hi:[0,1,1]\n\t"
        "v_pk_fma_f32 %3, %3, %11, %7 op_sel:[0,0,0] op_sel_hi:[0,1,1]"
        : "+v"(a0), "+v"(a1), "+v"(a2), "+v"(a3), "=&v"(t0), "=&v"(t1), "=&v"(t2), "=&v"(t3)
        : "v"(w0), "v"(w1), "v"(w2), "v"(w3));
  } else {
    asm("v_pk_mul_f32 %4, %0, %8 op_sel:[0,0] op_sel_hi:[1,0]\n\t"
        "v_pk_mul_f32 %5, %1, %9 op_sel:[0,0] op_sel_hi:[1,0]\n\t"
        "v_pk_mul_f32 %6, %2, %10 op_sel:[0,0] op_sel_hi:[1,0]\n\t"
        "v_pk_mul_f32 %7, %3, %11 op_sel:[0,0] op_sel_hi:[1,0]\n\t"
        "v_pk_fma_f32 %0, %0, %8, %4 op_sel:[1,1,0] op_sel_hi:[0,1,1] neg_hi:[1,0,0]\n\t"
        "v_pk_fma_f32 %1, %1, %9, %5 op_sel:[1,1,0] op_sel_hi:[0,1,1] neg_hi:[1,0,0]\n\t"
        "v_pk_fma_f32 %2, %2, %10, %6 op_sel:[1,1,0] op_sel_hi:[0,1,1] neg_hi:[1,0,0]\n\t"
        "v_pk_fma_f32 %3, %3, %11, %7 op_sel:[1,1,0] op_sel_hi:[0,1,1] neg_hi:[1,0,0]"
        : "+v"(a0), "+v"(a1), "+v"(a2), "+v"(a3), "=&v"(t0), "=&v"(t1), "=&v"(t2), "=&v"(t3)
        : "v"(w0), "v"(w1), "v"(w2), "v"(w3));
  }
}
template <bool CONJ> __device__ __forceinline__ void cmul7(f2 (&a)[8], const f2 (&w)[7]) {
  cmul3<CONJ>(a[1], a[2], a[3], w[0], w[1], w[2]);
  cmul4<CONJ>(a[4], a[5], a[6], a[7], w[3], w[4], w[5], w[6]);
}
__device__ __forceinline__ void herm2(f2& r0, f2& r1, f2 z0, f2 q0, f2 w0, f2 z1, f2 q1, f2 w1) {
  f2 d0, d1;
  asm("v_pk_add_f32 %0, %4, %5 neg_hi:[0,1]\n\t"
      "v_pk_add_f32 %2, %4, %5 neg_lo:[0,1]\n\t"
      "v_pk_add_f32 %1, %7, %8 neg_hi:[0,1]\n\t"
      "v_pk_add_f32 %3, %7, %8 neg_lo:[0,1]\n\t"
      "v_pk_fma_f32 %0, %2, %6, %0 op_sel:[1,1,0] op_sel_hi:[1,0,1] neg_lo:[0,1,0]\n\t"
      "v_pk_fma_f32 %1, %3, %9, %1 op_sel:[1,1,0] op_sel_hi:[1,0,1] neg_lo:[0,1,0]\n\t"
      "v_pk_fma_f32 %0, %2, %6, %0 op_sel:[0,0,0] op_sel_hi:[0,1,1]\n\t"
      "v_pk_fma_f32 %1, %3, %9, %1 op_sel:[0,0,0] op_sel_hi:[0,1,1]"
      : "=&v"(r0), "=&v"(r1), "=&v"(d0), "=&v"(d1) : "v"(z0), "v"(q0), "v"(w0), "v"(z1), "v"(q1), "v"(w1));
}
__device__ __forceinline__ f2 cfma(f2 a, f2 w, f2 c) {
  f2 t, d;
  asm("v_pk_fma_f32 %0, %1, %2, %3 op_sel:[1,1,0] op_sel_hi:[1,0,1] neg_lo:[0,1,0]" : "=v"(t) : "v"(a), "v"(w), "v"(c));
  asm("v_pk_fma_f32 %0, %1, %2, %3 op_sel:[0,0,0] op_sel_hi:[0,1,1]" : "=v"(d) : "v"(a), "v"(w), "v"(t));
  return d;
}
__device__ __forceinline__ f2 add_mi(f2 a, f2 b) {
  f2 d;
  asm("v_pk_add_f32 %0, %1, %2 op_sel:[0,1] op_sel_hi:[1,0] neg_hi:[0,1]" : "=v"(d) : "v"(a), "v"(b));
  return d;
}
__device__ __forceinline__ f2 add_pi(f2 a, f2 b) {
  f2 d;
  asm("v_pk_add_f32 %0, %1, %2 op_sel:[0,1] op_sel_hi:[1,0] neg_lo:[0,1]" : "=v"(d) : "v"(a), "v"(b));
  return d;
}
__device__ __forceinline__ f2 add_cj(f2 a, f2 b) {
  f2 d;
  asm("v_pk_add_f32 %0, %1, %2 neg_hi:[0,1]" : "=v"(d) : "v"(a), "v"(b));
  return d;
}
__device__ __forceinline__ f2 sub_cj(f2 a, f2 b) {
  f2 d;
  asm("v_pk_add_f32 %0, %1, %2 neg_lo:[0,1]" : "=v"(d) : "v"(a), "v"(b));
  return d;
}
template <bool INV> __device__ __forceinline__ f2 addr(f2 a, f2 b) { return INV ? add_pi(a, b) : add_mi(a, b); }
template <bool INV> __device__ __forceinline__ f2 subr(f2 a, f2 b) { return INV ? add_mi(a, b) : add_pi(a, b); }

template <bool INV> __device__ __forceinline__ void bfly4(f2& a0, f2& a1, f2& a2, f2& a3) {
  const f2 t0 = a0 + a2, t1 = a0 - a2, t2 = a1 + a3, u = a1 - a3;
  a0 = t0 + t2; a2 = t0 - t2; a1 = addr<INV>(t1, u); a3 = subr<INV>(t1, u);
}
template <bool INV> __device__ __forceinline__ void bfly8(f2 (&a)[8]) {
  constexpr float R = 0.70710678118654752440f;
  bfly4<INV>(a[0], a[2], a[4], a[6]);
  bfly4<INV>(a[1], a[3], a[5], a[7]);
  const f2 e0 = a[0], e1 = a[2], e2 = a[4], e3 = a[6];
  const f2 o0 = a[1], o2 = a[5];
  const f2 p1 = addr<INV>(a[3], a[3]);
  const f2 p3 = subr<INV>(a[7], a[7]);
  a[0] = e0 + o0; a[4] = e0 - o0;
  a[1] = e1 + R * p1; a[5] = e1 - R * p1;
  a[2] = addr<INV>(e2, o2); a[6] = subr<INV>(e2, o2);
  a[3] = e3 - R * p3; a[7] = e3 + R * p3;
}
#define XL_PAD4(v) asm volatile("s_nop 1" : "+v"(v[0]), "+v"(v[1]), "+v"(v[2]), "+v"(v[3]))
#define XL_PAD8(v) asm volatile("s_nop 1" : "+v"(v[0]), "+v"(v[1]), "+v"(v[2]), "+v"(v[3]), "+v"(v[4]), "+v"(v[5]), "+v"(v[6]), "+v"(v[7]))

__device__ __forceinline__ void swap32(f2& a, f2& b) {
  const u2 au = __builtin_bit_cast(u2, a), bu = __builtin_bit_cast(u2, b);
  const unsigned ax = au[0], ay = au[1], bx = bu[0], by = bu[1];
  auto rx = __builtin_amdgcn_permlane32_swap(ax, bx, false, false);
  auto ry = __builtin_amdgcn_permlane32_swap(ay, by, false, false);
  a = __builtin_bit_cast(f2, u2{(unsigned)rx[0], (unsigned)ry[0]});
  b = __builtin_bit_cast(f2, u2{(unsigned)rx[1], (unsigned)ry[1]});
}
__device__ __forceinline__ void swap16(f2& a, f2& b) {
  const u2 au = __builtin_bit_cast(u2, a), bu = __builtin_bit_cast(u2, b);
  const unsigned ax = au[0], ay = au[1], bx = bu[0], by = bu[1];
  auto rx = __builtin_amdgcn_permlane16_swap(ax, bx, false, false);
  auto ry = __builtin_amdgcn_permlane16_swap(ay, by, false, false);
  a = __builtin_bit_cast(f2, u2{(unsigned)rx[0], (unsigned)ry[0]});
  b = __builtin_bit_cast(f2, u2{(unsigned)rx[1], (unsigned)ry[1]});
}
__device__ __forceinline__ void swap8(f2& a, f2& b) {
  const u2 au = __builtin_bit_cast(u2, a), bu = __builtin_bit_cast(u2, b);
  const int ax = au[0], ay = au[1], bx = bu[0], by = bu[1];
  const int nax = __builtin_amdgcn_update_dpp(ax, bx, 0x128, 0xf, 0xc, false);
  const int nbx = __builtin_amdgcn_update_dpp(bx, ax, 0x128, 0xf, 0x3, false);
  const int nay = __builtin_amdgcn_update_dpp(ay, by, 0x128, 0xf, 0xc, false);
  const int nby = __builtin_amdgcn_update_dpp(by, ay, 0x128, 0xf, 0x3, false);
  a = __builtin_bit_cast(f2, u2{(unsigned)nax, (unsigned)nay});
  b = __builtin_bit_cast(f2, u2{(unsigned)nbx, (unsigned)nby});
}

template <int CTRL> __device__ __forceinline__ float dppmov(float v) {
  return __builtin_bit_cast(float, __builtin_amdgcn_update_dpp(0, __builtin_bit_cast(int, v), CTRL, 0xf, 0xf, true));
}
__device__ __forceinline__ float wave_sum(float v) {
  v += dppmov<0xB1>(v);
  v += dppmov<0x4E>(v);
  v += dppmov<0x141>(v);
  v += dppmov<0x140>(v);
  {
    unsigned u = __builtin_bit_cast(unsigned, v);
    auto r = __builtin_amdgcn_permlane16_swap(u, u, false, false);
    v = __builtin_bit_cast(float, (unsigned)r[0]) + __builtin_bit_cast(float, (unsigned)r[1]);
  }
  {
    unsigned u = __builtin_bit_cast(unsigned, v);
    auto r = __builtin_amdgcn_permlane32_swap(u, u, false, false);
    v = __builtin_bit_cast(float, (unsigned)r[0]) + __builtin_bit_cast(float, (unsigned)r[1]);
  }
  return v;
}

__device__ __forceinline__ f4 mfma(u4 a, u4 b, f4 c) {
  return __builtin_amdgcn_mfma_f32_16x16x32_bf16(__builtin_bit_cast(s8, a), __builtin_bit_cast(s8, b), c, 0, 0, 0);
}
typedef float f16v __attribute__((ext_vector_type(16)));
__device__ __forceinline__ f16v mfma32(u4 a, u4 b, f16v c) {
  return __builtin_amdgcn_mfma_f32_32x32x16_bf16(__builtin_bit_cast(s8, a), __builtin_bit_cast(s8, b), c, 0, 0, 0);
}
__device__ __forceinline__ float silu_f(float v) {
  return v * __builtin_amdgcn_rcpf(1.0f + __expf(-v));
}

__device__ __forceinline__ void wave_sum2(float& a, float& b) {
  a += dppmov<0xB1>(a);  b += dppmov<0xB1>(b);
  a += dppmov<0x4E>(a);  b += dppmov<0x4E>(b);
  a += dppmov<0x141>(a); b += dppmov<0x141>(b);
  a += dppmov<0x140>(a); b += dppmov<0x140>(b);
  unsigned ua = __builtin_bit_cast(unsigned, a), ub = __builtin_bit_cast(unsigned, b);
  auto r = __builtin_amdgcn_permlane16_swap(ua, ub, false, false);
  float c = __builtin_bit_cast(float, (unsigned)r[0]) + __builtin_bit_cast(float, (unsigned)r[1]);
  unsigned uc = __builtin_bit_cast(unsigned, c);
  auto r2 = __builtin_amdgcn_permlane32_swap(uc, uc, false, false);
  c = __builtin_bit_cast(float, (unsigned)r2[0]) + __builtin_bit_cast(float, (unsigned)r2[1]);
  a = __builtin_bit_cast(float, __builtin_amdgcn_readlane(__builtin_bit_cast(int, c), 0));
  b = __builtin_bit_cast(float, __builtin_amdgcn_readlane(__builtin_bit_cast(int, c), 16));
}
__device__ __forceinline__ void ln_mod(const f2 (&xv)[4], const unsigned (&sc1)[4], const unsigned (&sh)[4], f2 (&h)[4]) {
  const float x00 = xv[0].x;
  const float piv = __builtin_bit_cast(float, __builtin_amdgcn_readfirstlane(__builtin_bit_cast(int, x00)));
  f2 d[4];
#pragma unroll
  for (int i = 0; i < 4; ++i) d[i] = xv[i] - piv;
  const f2 s2 = (d[0] + d[1]) + (d[2] + d[3]);
  const f2 q2 = (d[0] * d[0] + d[1] * d[1]) + (d[2] * d[2] + d[3] * d[3]);
  float s1 = s2.x + s2.y, sq = q2.x + q2.y;
  wave_sum2(s1, sq);
  const float dm = s1 * (1.0f / 512.0f);
  const float var = __builtin_fmaxf(sq * (1.0f / 512.0f) - dm * dm, 0.0f);
  const float mbp = rb1(piv + dm) - piv;
  const float sd = rb1(__builtin_amdgcn_sqrtf(rb1(rb1(var) + 1e-5f)));
  const float rs = __builtin_amdgcn_rcpf(sd);
#pragma unroll
  for (int i = 0; i < 4; ++i) h[i] = ((d[i] - mbp) * rs) * unpk(sc1[i]) + unpk(sh[i]);
}

struct Tw256 { f2 a[3], b[3], c[3]; int x2a, x2b, x3a, x3b; };
__device__ __forceinline__ void make_tw256(Tw256& t, int l) {
#pragma unroll
  for (int r = 1; r < 4; ++r) {
    t.a[r - 1] = tw2048(l * r * 8);
    t.b[r - 1] = tw2048((l & 15) * r * 32);
    t.c[r - 1] = tw2048((l & 3) * r * 128);
  }
  const int d2 = l >> 4, d1 = (l >> 2) & 3, d0 = l & 3;
  t.x2a = 80 * d2 + 4 * d1 + d0;
  t.x2b = 80 * d2 + 20 * d1 + d0;
  t.x3a = 80 * d2 + 4 * d1 + d0;
  t.x3b = 80 * d2 + 4 * d1 + 17 * d0;
}
#define PIN_TW(t) do { \
  asm volatile("" : "+v"(t.a[0]), "+v"(t.a[1]), "+v"(t.a[2]), "+v"(t.b[0]), "+v"(t.b[1]), "+v"(t.b[2]), "+v"(t.c[0]), "+v"(t.c[1]), "+v"(t.c[2])); \
  asm volatile("" : "+v"(t.x2a), "+v"(t.x2b), "+v"(t.x3a), "+v"(t.x3b)); } while (0)

__device__ __forceinline__ void fft256_fwd(f2 (&v)[4], const Tw256& t, f2* lds) {
  bfly4<false>(v[0], v[1], v[2], v[3]);
  cmul3<false>(v[1], v[2], v[3], t.a[0], t.a[1], t.a[2]);
  XL_PAD4(v);
  swap32(v[0], v[2]); swap32(v[1], v[3]);
  swap16(v[0], v[1]); swap16(v[2], v[3]);
  bfly4<false>(v[0], v[1], v[2], v[3]);
  cmul3<false>(v[1], v[2], v[3], t.b[0], t.b[1], t.b[2]);
#pragma unroll
  for (int r = 0; r < 4; ++r) lds[t.x2a + 20 * r] = v[r];
  __builtin_amdgcn_wave_barrier();
#pragma unroll
  for (int r = 0; r < 4; ++r) v[r] = lds[t.x2b + 4 * r];
  __builtin_amdgcn_wave_barrier();
  bfly4<false>(v[0], v[1], v[2], v[3]);
  cmul3<false>(v[1], v[2], v[3], t.c[0], t.c[1], t.c[2]);
#pragma unroll
  for (int r = 0; r < 4; ++r) lds[t.x3a + 17 * r] = v[r];
  __builtin_amdgcn_wave_barrier();
#pragma unroll
  for (int r = 0; r < 4; ++r) v[r] = lds[t.x3b + r];
  __builtin_amdgcn_wave_barrier();
  bfly4<false>(v[0], v[1], v[2], v[3]);
}
template <class H> __device__ __forceinline__ void fft256_inv(f2 (&v)[4], const Tw256& t, f2* lds, H hook) {
  bfly4<true>(v[0], v[1], v[2], v[3]);
#pragma unroll
  for (int r = 0; r < 4; ++r) lds[t.x3b + r] = v[r];
  __builtin_amdgcn_wave_barrier();
#pragma unroll
  for (int r = 0; r < 4; ++r) v[r] = lds[t.x3a + 17 * r];
  __builtin_amdgcn_wave_barrier();
  hook(std::integral_constant<int, 0>{});
  cmul3<true>(v[1], v[2], v[3], t.c[0], t.c[1], t.c[2]);
  bfly4<true>(v[0], v[1], v[2], v[3]);
#pragma unroll
  for (int r = 0; r < 4; ++r) lds[t.x2b + 4 * r] = v[r];
  __builtin_amdgcn_wave_barrier();
#pragma unroll
  for (int r = 0; r < 4; ++r) v[r] = lds[t.x2a + 20 * r];
  __builtin_amdgcn_wave_barrier();
  hook(std::integral_constant<int, 1>{});
  cmul3<true>(v[1], v[2], v[3], t.b[0], t.b[1], t.b[2]);
  bfly4<true>(v[0], v[1], v[2], v[3]);
  XL_PAD4(v);
  swap16(v[0], v[1]); swap16(v[2], v[3]);
  swap32(v[0], v[2]); swap32(v[1], v[3]);
  hook(std::integral_constant<int, 2>{});
  cmul3<true>(v[1], v[2], v[3], t.a[0], t.a[1], t.a[2]);
  bfly4<true>(v[0], v[1], v[2], v[3]);
}
template <int RP> __device__ __forceinline__ void fft256_inv2(f2 (&v)[2][4], const Tw256& t, f2* lds) {
#pragma unroll
  for (int u = 0; u < 2; ++u) bfly4<true>(v[u][0], v[u][1], v[u][2], v[u][3]);
#pragma unroll
  for (int u = 0; u < 2; ++u)
#pragma unroll
    for (int r = 0; r < 4; ++r) lds[t.x3b + u * RP + r] = v[u][r];
  __builtin_amdgcn_wave_barrier();
#pragma unroll
  for (int u = 0; u < 2; ++u)
#pragma unroll
    for (int r = 0; r < 4; ++r) v[u][r] = lds[t.x3a + u * RP + 17 * r];
  __builtin_amdgcn_wave_barrier();
#pragma unroll
  for (int u = 0; u < 2; ++u) {
    cmul3<true>(v[u][1], v[u][2], v[u][3], t.c[0], t.c[1], t.c[2]);
    bfly4<true>(v[u][0], v[u][1], v[u][2], v[u][3]);
#pragma unroll
    for (int r = 0; r < 4; ++r) lds[t.x2b + u * RP + 4 * r] = v[u][r];
  }
  __builtin_amdgcn_wave_barrier();
#pragma unroll
  for (int u = 0; u < 2; ++u)
#pragma unroll
    for (int r = 0; r < 4; ++r) v[u][r] = lds[t.x2a + u * RP + 20 * r];
  __builtin_amdgcn_wave_barrier();
#pragma unroll
  for (int u = 0; u < 2; ++u) {
    cmul3<true>(v[u][1], v[u][2], v[u][3], t.b[0], t.b[1], t.b[2]);
    bfly4<true>(v[u][0], v[u][1], v[u][2], v[u][3]);
  }
  XL_PAD4(v[0]); XL_PAD4(v[1]);
#pragma unroll
  for (int u = 0; u < 2; ++u) {
    swap16(v[u][0], v[u][1]); swap16(v[u][2], v[u][3]);
    swap32(v[u][0], v[u][2]); swap32(v[u][1], v[u][3]);
  }
#pragma unroll
  for (int u = 0; u < 2; ++u) {
    cmul3<true>(v[u][1], v[u][2], v[u][3], t.a[0], t.a[1], t.a[2]);
    bfly4<true>(v[u][0], v[u][1], v[u][2], v[u][3]);
  }
}
__device__ __forceinline__ int zpos(int k) { return k + (k >> 4); }

__device__ __host__ __forceinline__ int k2_k1(int tt, int kb) {
  int seq = (tt >> 6) * 8 + (tt & 7);
  int q = seq >> 2, k0 = seq & 3, ka = (tt >> 3) & 7;
  return 8 * (k0 + 4 * ka + 32 * kb) + q;
}

__device__ __forceinline__ void transpose8x8(const u4 (&in)[8], u4 (&out)[8]) {
#pragma unroll
  for (int c2 = 0; c2 < 4; ++c2) {
#pragma unroll
    for (int r2 = 0; r2 < 4; ++r2) {
      unsigned a = in[2 * r2][c2], b = in[2 * r2 + 1][c2];
      out[2 * c2][r2] = __builtin_amdgcn_perm(b, a, 0x05040100u);
      out[2 * c2 + 1][r2] = __builtin_amdgcn_perm(b, a, 0x07060302u);
    }
  }
}

#define K1_COAL 1
#define PREP_AUX 16
#define T_AUX 16
#define T2_AUX 16
#define WF_AUX 16
#define OUT_NT 0
constexpr int AUX_SC1 = 16;
constexpr int OOB = 0x40000000;
__device__ __forceinline__ __amdgpu_buffer_rsrc_t ws_rsrc(unsigned char* ws) {
  return __builtin_amdgcn_make_buffer_rsrc((void*)ws, 0, (int)WS_BYTES, 0x00020000);
}

__device__ __forceinline__ int prep_w1_off(int job, int l, int r) {
  const int cg = job % 7, ks = job / 7, no = l & 31, h = l >> 5;
  return ((16 * ks + 8 * h + r) * FF + cg * 256 + no * 8) * 2;
}
__device__ __forceinline__ void prep_w1_fin(const u4 (&in)[8], __amdgpu_buffer_rsrc_t rs, u4* buf, int job, int l) {
  const int cg = job % 7, ks = job / 7, no = l & 31, h = l >> 5;
  u4 out[8];
  transpose8x8(in, out);
#pragma unroll
  for (int cc = 0; cc < 8; ++cc) buf[((no >> 2) * 64 + 8 * (no & 3) + 32 * h + cc) ^ (no & 7)] = out[cc];
  __builtin_amdgcn_wave_barrier();
#pragma unroll
  for (int f = 0; f < 8; ++f)
    __builtin_amdgcn_raw_buffer_store_b128(buf[(f * 64 + l) ^ ((4 * f + ((l >> 3) & 3)) & 7)], rs,
                                           (int)OFF_W1F + (((cg * 8 + f) * 32 + ks) * 64 + l) * 16, 0, PREP_AUX);
}
__device__ __forceinline__ int prep_w2_off(int job, int l, int e) {
  const int cg = job & 1, cs = job >> 1, no = l & 31, h = l >> 5;
  return ((16 * cs + 8 * (e >> 2) + 4 * h + (e & 3)) * D + cg * 256 + no * 8) * 2;
}
__device__ __forceinline__ void prep_w2_fin(const u4 (&in)[8], __amdgpu_buffer_rsrc_t rs, u4* buf, int job, int l) {
  const int cg = job & 1, cs = job >> 1, no = l & 31, h = l >> 5;
  u4 out[8];
  transpose8x8(in, out);
  const int base = (no >> 2) * 64 + 16 * (no & 1) + 4 * ((no >> 1) & 1) + 32 * h;
#pragma unroll
  for (int cc = 0; cc < 8; ++cc) buf[base + (cc & 3) + 8 * (cc >> 2)] = out[cc];
  __builtin_amdgcn_wave_barrier();
#pragma unroll
  for (int f = 0; f < 8; ++f)
    __builtin_amdgcn_raw_buffer_store_b128(buf[f * 64 + l], rs,
                                           (int)OFF_W2F + ((((cg * 4 + (f >> 1)) * 112 + cs) * 2 + (f & 1)) * 64 + l) * 16, 0, PREP_AUX);
}

constexpr int K0_MOD = 96, K0_WF = 128, K0_MISC = 32;
constexpr int K0_BLOCKS = K0_MOD + K0_WF + K0_MISC;
constexpr unsigned SENT = 0xFFFFFFFFu;
constexpr int FILL_PER_THREAD = (int)((OFF_X1 - OFF_T) / (256 * 256 * 16));
static_assert((size_t)FILL_PER_THREAD * 256 * 256 * 16 == OFF_X1 - OFF_T && K0_BLOCKS == 256, "fill covers the spectrum and h2 hand-off buffers");
#define FILL_AUX 16

__global__ __launch_bounds__(256) void k0_prep(const unsigned short* __restrict__ cvec,
                                               const unsigned short* __restrict__ wmod,
                                               const unsigned short* __restrict__ wr,
                                               const unsigned short* __restrict__ wi,
                                               unsigned char* ws) {
  __shared__ float red[4][32];
  __shared__ u4 wbuf[1024];
  const int tid = threadIdx.x;
  int b = blockIdx.x;
  STAMP(0, 0);
  const __amdgpu_buffer_rsrc_t rs = ws_rsrc(ws);
  auto fill = [&]() {
#pragma unroll
    for (int i = 0; i < FILL_PER_THREAD; ++i)
      __builtin_amdgcn_raw_buffer_store_b128(u4{SENT, SENT, SENT, SENT}, rs,
                                             (int)OFF_T + ((blockIdx.x * FILL_PER_THREAD + i) * 256 + tid) * 16, 0, FILL_AUX);
  };
  if (b < K0_MOD) {
    const int co = tid & 3, rg = tid >> 2;
    u4 wv[8];
    unsigned short cs[8];
#pragma unroll
    for (int i = 0; i < 8; ++i) cs[i] = cvec[rg + 64 * i];
#pragma unroll
    for (int i = 0; i < 8; ++i)
      wv[i] = *(const u4*)(wmod + (size_t)(rg + 64 * i) * 3072 + b * 32 + co * 8);
    SCHED_FENCE();
    fill();
    SCHED_FENCE();
    f2 acc[4];
#pragma unroll
    for (int e = 0; e < 4; ++e) acc[e] = f2{0.f, 0.f};
#pragma unroll
    for (int i = 0; i < 8; ++i) {
      const float cv = bflo(cs[i]);
      const float sv = rb1(silu_f(cv));
#pragma unroll
      for (int e2 = 0; e2 < 4; ++e2) acc[e2] += sv * unpk(wv[i][e2]);
    }
    STAMP(0, 2);
#pragma unroll
    for (int e2 = 0; e2 < 4; ++e2) {
      float v0 = acc[e2].x, v1 = acc[e2].y;
      v0 += dppmov<0x124>(v0); v1 += dppmov<0x124>(v1);
      v0 += dppmov<0x128>(v0); v1 += dppmov<0x128>(v1);
      unsigned u0 = __builtin_bit_cast(unsigned, v0), u1 = __builtin_bit_cast(unsigned, v1);
      auto r = __builtin_amdgcn_permlane16_swap(u0, u1, false, false);
      float c = __builtin_bit_cast(float, (unsigned)r[0]) + __builtin_bit_cast(float, (unsigned)r[1]);
      unsigned uc = __builtin_bit_cast(unsigned, c);
      auto r2 = __builtin_amdgcn_permlane32_swap(uc, uc, false, false);
      c = __builtin_bit_cast(float, (unsigned)r2[0]) + __builtin_bit_cast(float, (unsigned)r2[1]);
      if ((tid & 0x2c) == 0) red[tid >> 6][co * 8 + e2 * 2 + ((tid >> 4) & 1)] = c;
    }
    __syncthreads();
    if (tid < 32) {
      float s = rb1((red[0][tid] + red[1][tid]) + (red[2][tid] + red[3][tid]));
      const int col = b * 32 + tid, p = col >> 9, e = col & 511;
      if (p == 1 || p == 4) s = rb1(s + 1.0f);
      const unsigned short sb = (unsigned short)(__builtin_bit_cast(unsigned, s) >> 16);
      const int n = e >> 1, l = n & 63, i = n >> 6, hf = e & 1;
      if (p < 2) ((unsigned short*)(ws + OFF_MODK1))[((l * 4 + i) * 2 + p) * 2 + hf] = sb;
      else if (p < 5) ((unsigned short*)(ws + OFF_MODK3))[((l * 4 + i) * 3 + (p == 2 ? 0 : (p == 4 ? 1 : 2))) * 2 + hf] = sb;
      else ((unsigned short*)(ws + OFF_GB))[e] = sb;
    }
    STAMP(0, 1);
    return;
  }
  b -= K0_MOD;
  if (b < K0_WF) {
    const int tq = b >> 1, k2 = (b & 1) * 128 + (tid & 127), tp2 = tid >> 7;
    unsigned short vr[16], vi[16];
#pragma unroll
    for (int e = 0; e < 16; ++e) {
      const int k1 = k2_k1(4 * tq + 2 * tp2 + (e >> 3), e & 7);
      vr[e] = wr[k1 * DF + k2];
      vi[e] = wi[k1 * DF + k2];
    }
    SCHED_FENCE();
    fill();
    SCHED_FENCE();
#pragma unroll
    for (int e4 = 0; e4 < 4; ++e4) {
      u4 o;
#pragma unroll
      for (int e = 0; e < 4; ++e) o[e] = (unsigned)vr[e4 * 4 + e] | ((unsigned)vi[e4 * 4 + e] << 16);
      wbuf[(tid & 127) * 8 + ((tp2 * 4 + e4) ^ (tid & 7))] = o;
    }
    __syncthreads();
#pragma unroll
    for (int i = 0; i < 4; ++i) {
      const int kl = i * 32 + (tid >> 3);
      __builtin_amdgcn_raw_buffer_store_b128(wbuf[kl * 8 + ((tid & 7) ^ (kl & 7))], rs,
                                             (int)OFF_WF + (((b & 1) * 128 + kl) * 512 + tq * 8 + (tid & 7)) * 16, 0, WF_AUX);
    }
    STAMP(0, 3);
    STAMP_DRAIN();
    STAMP(0, 5);
    return;
  }
  b -= K0_WF;
  fill();
  if (tid < 64) {
    const int e = b * 64 + tid;
    const int tt = e & 255, kb = e >> 8;
    const int k1 = k2_k1(tt, kb), nk = (2048 - k1) & 2047;
    f2 a0 = f2{bflo(wr[k1 * DF]), bflo(wi[k1 * DF])};
    f2 a1 = f2{bflo(wr[nk * DF]), -bflo(wi[nk * DF])};
    f2 b0 = f2{bflo(wr[k1 * DF + 256]), bflo(wi[k1 * DF + 256])};
    f2 b1 = f2{bflo(wr[nk * DF + 256]), -bflo(wi[nk * DF + 256])};
    f2 w0h = (a0 + a1) * 0.5f, w256h = (b0 + b1) * 0.5f;
    f2 P = (w0h + w256h) * 0.5f, Q = (w0h - w256h) * 0.5f;
    ((f4*)(ws + OFF_PQ))[e] = f4{P.x, P.y, Q.x, Q.y};
    STAMP(0, 4);
  }
}

constexpr unsigned SPIN_CAP = 1u << 18;
__device__ __forceinline__ unsigned nosent(unsigned v) { return v < SENT - 1u ? v : SENT - 1u; }
template <int N> __device__ __forceinline__ bool all_written(const u4* v) {
  unsigned m = 0u;
#pragma unroll
  for (int i = 0; i < N; ++i) {
    const unsigned a = v[i][0] > v[i][1] ? v[i][0] : v[i][1], c = v[i][2] > v[i][3] ? v[i][2] : v[i][3];
    m = m > a ? m : a;
    m = m > c ? m : c;
  }
  return __all(m != SENT);
}
template <int N> __device__ __forceinline__ void reload_until_written(u4* v, __amdgpu_buffer_rsrc_t rs, int off, int stride) {
  if (__builtin_expect(!all_written<N>(v), 0)) {
    unsigned s = 0;
    bool ok;
    do {
      __builtin_amdgcn_s_sleep(1);
      asm volatile("" : "+v"(off) :: "memory");
#pragma unroll
      for (int i = 0; i < N; ++i) v[i] = __builtin_amdgcn_raw_buffer_load_b128(rs, off + i * stride, 0, AUX_SC1);
      ok = all_written<N>(v);
    } while (!ok && ++s < SPIN_CAP);
  }
}

constexpr int SEQP = 64 + 1;
#define PREP_DELAY 60
#define C_DELAY 40
__device__ __forceinline__ void sync4(unsigned* cnt, unsigned& target, int l) {
  target += 4u;
  SCHED_FENCE();
  asm volatile("s_waitcnt lgkmcnt(0)" ::: "memory");
  if (l == 0) __hip_atomic_fetch_add(cnt, 1u, __ATOMIC_RELAXED, __HIP_MEMORY_SCOPE_WORKGROUP);
  for (unsigned sp = 0; sp < (SPIN_CAP << 4); ++sp)
    if (__hip_atomic_load(cnt, __ATOMIC_RELAXED, __HIP_MEMORY_SCOPE_WORKGROUP) >= target) break;
  asm volatile("" ::: "memory");
  SCHED_FENCE();
}
__device__ __forceinline__ void cols_body(unsigned char* ws, f2* lds, unsigned* cnt, int t) {
  unsigned tgt = 0u;
  const int b = blockIdx.x;
  const int l = t & 63;
  const __amdgpu_buffer_rsrc_t rs = ws_rsrc(ws);
  const int k2 = (b & 7) * 32 + (b >> 3);
  const int jr = t >> 2, qp = t & 3;
  const int co = (int)OFF_T + (jr * 1024 + k2 * 4 + qp) * 16;
  u4 wq[8];
  {
    const int wo = k2 != 0 ? (int)OFF_WF + (k2 * 256 + t) * 32 : (int)OFF_PQ + t * 16;
    const int ws1 = k2 != 0 ? 16 : 4096;
    wq[0] = __builtin_amdgcn_raw_buffer_load_b128(rs, wo, 0, 0);
    wq[1] = __builtin_amdgcn_raw_buffer_load_b128(rs, wo + ws1, 0, 0);
    const int po = k2 != 0 ? OOB : wo;
#pragma unroll
    for (int kb = 2; kb < 8; ++kb) wq[kb] = __builtin_amdgcn_raw_buffer_load_b128(rs, po + kb * 4096, 0, 0);
  }
  SCHED_FENCE();
  f2 twA[3], twB[7];
#pragma unroll
  for (int r = 1; r < 4; ++r) twA[r - 1] = tw2048(jr * r * 8);
  const int bb = l >> 3;
#pragma unroll
  for (int r = 1; r < 8; ++r) twB[r - 1] = tw2048(bb * r * 32);
  int pa = (8 * qp) * SEQP + jr;
  int pb = ((t >> 6) * 8 + (t & 7)) * SEQP + bb;
  asm volatile("" : "+v"(twB[0]), "+v"(twB[1]), "+v"(twB[2]), "+v"(twB[3]), "+v"(twB[4]), "+v"(twB[5]), "+v"(twB[6]), "+v"(twA[0]), "+v"(twA[1]), "+v"(twA[2]));
  asm volatile("" : "+v"(pa), "+v"(pb));
  SCHED_FENCE();
  __builtin_amdgcn_s_sleep(C_DELAY);
  asm volatile("" ::: "memory");
  u4 cin[4];
#pragma unroll
  for (int i = 0; i < 4; ++i) cin[i] = __builtin_amdgcn_raw_buffer_load_b128(rs, co + i * (1 << 20), 0, AUX_SC1);
  reload_until_written<4>(cin, rs, co, 1 << 20);
  SCHED_FENCE();
#pragma unroll
  for (int i = 0; i < 4; ++i)
    __builtin_amdgcn_raw_buffer_store_b128(u4{SENT, SENT, SENT, SENT}, rs,
                                           i * 256 + t < 896 ? (int)OFF_AF + ((256 + b) * 896 + i * 256 + t) * 16 : OOB, 0, FILL_AUX);
  SCHED_FENCE();
  STAMPT(2, 0, 256);
  f2 a0[4], a1[4];
#pragma unroll
  for (int i = 0; i < 4; ++i) { const f4 c = __builtin_bit_cast(f4, cin[i]); a0[i] = c.xy; a1[i] = c.zw; }
  bfly4<false>(a0[0], a0[1], a0[2], a0[3]);
  bfly4<false>(a1[0], a1[1], a1[2], a1[3]);
  cmul3<false>(a0[1], a0[2], a0[3], twA[0], twA[1], twA[2]);
  cmul3<false>(a1[1], a1[2], a1[3], twA[0], twA[1], twA[2]);
#pragma unroll
  for (int r = 0; r < 4; ++r) {
    lds[pa + r * SEQP] = a0[r];
    lds[pa + (4 + r) * SEQP] = a1[r];
  }
  sync4(cnt, tgt, l);
  STAMPT(2, 1, 256);
  f2 v[8];
#pragma unroll
  for (int a = 0; a < 8; ++a) v[a] = lds[pb + 8 * a];
  bfly8<false>(v);
  cmul7<false>(v, twB);
  XL_PAD8(v);
#pragma unroll
  for (int i = 0; i < 4; ++i) swap32(v[i], v[i + 4]);
  swap16(v[0], v[2]); swap16(v[1], v[3]); swap16(v[4], v[6]); swap16(v[5], v[7]);
#pragma unroll
  for (int i = 0; i < 8; i += 2) swap8(v[i], v[i + 1]);
  bfly8<false>(v);
  if (k2 != 0) {
    cmul4<false>(v[0], v[1], v[2], v[3], unpk(wq[0][0]), unpk(wq[0][1]), unpk(wq[0][2]), unpk(wq[0][3]));
    cmul4<false>(v[4], v[5], v[6], v[7], unpk(wq[1][0]), unpk(wq[1][1]), unpk(wq[1][2]), unpk(wq[1][3]));
  } else {
    sync4(cnt, tgt, l);
#pragma unroll
    for (int kb = 0; kb < 8; ++kb) lds[k2_k1(t, kb)] = v[kb];
    sync4(cnt, tgt, l);
#pragma unroll
    for (int kb = 0; kb < 8; ++kb) {
      f2 fn = lds[(2048 - k2_k1(t, kb)) & 2047];
      fn.y = -fn.y;
      const f4 c = __builtin_bit_cast(f4, wq[kb]);
      v[kb] = cfma(fn, c.zw, cmul(v[kb], c.xy));
    }
  }
  bfly8<true>(v);
  XL_PAD8(v);
#pragma unroll
  for (int i = 0; i < 8; i += 2) swap8(v[i], v[i + 1]);
  swap16(v[0], v[2]); swap16(v[1], v[3]); swap16(v[4], v[6]); swap16(v[5], v[7]);
#pragma unroll
  for (int i = 0; i < 4; ++i) swap32(v[i], v[i + 4]);
  cmul7<true>(v, twB);
  bfly8<true>(v);
  if (k2 == 0) sync4(cnt, tgt, l);
#pragma unroll
  for (int a = 0; a < 8; ++a) lds[pb + 8 * a] = v[a];
  sync4(cnt, tgt, l);
#pragma unroll
  for (int r = 0; r < 4; ++r) {
    a0[r] = lds[pa + r * SEQP];
    a1[r] = lds[pa + (4 + r) * SEQP];
  }
  cmul3<true>(a0[1], a0[2], a0[3], twA[0], twA[1], twA[2]);
  cmul3<true>(a1[1], a1[2], a1[3], twA[0], twA[1], twA[2]);
  bfly4<true>(a0[0], a0[1], a0[2], a0[3]);
  bfly4<true>(a1[0], a1[1], a1[2], a1[3]);
#pragma unroll
  for (int i = 0; i < 4; ++i)
    __builtin_amdgcn_raw_buffer_store_b128(__builtin_bit_cast(u4, f4{a0[i].x, a0[i].y, a1[i].x, a1[i].y}), rs, co + i * (1 << 20), 0, T2_AUX);
  STAMPT(2, 2, 256);
  STAMP_DRAIN();
  STAMPT(2, 3, 256);
}

constexpr int ROWP = 322;

__global__ __launch_bounds__(640) void k1_rows(const unsigned* __restrict__ x32, const unsigned short* __restrict__ w1,
                                               const unsigned short* __restrict__ w2, unsigned char* ws) {
  __shared__ __attribute__((aligned(16))) f2 lds[8 * ROWP];
  __shared__ u4 pbuf[1024], tbuf[1024];
  __shared__ __attribute__((aligned(16))) f2 ldc[2048 + 64];
  __shared__ unsigned ccnt;
  const int j = blockIdx.x;
  const int tid = threadIdx.x, w = __builtin_amdgcn_readfirstlane(tid >> 6), l = tid & 63;
  const __amdgpu_buffer_rsrc_t rs = ws_rsrc(ws);
  STAMP(1, 0);
  if (w >= 8) {
    if (j >= 224) return;
    u4 pin[8];
    const int jc = j;
    const unsigned char* src = (const unsigned char*)(w == 8 ? w1 : w2);
#pragma unroll
    for (int r = 0; r < 8; ++r) pin[r] = *(const u4*)(src + (w == 8 ? prep_w1_off(jc, l, r) : prep_w2_off(jc, l, r)));
    asm volatile("" ::: "memory");
    SCHED_FENCE();
    __builtin_amdgcn_s_barrier();
    __builtin_amdgcn_s_sleep(PREP_DELAY);
    SCHED_FENCE();
    if (w == 8) prep_w1_fin(pin, rs, pbuf, j, l);
    else prep_w2_fin(pin, rs, pbuf + 512, j, l);
    STAMPT(1, 5, 512);
    STAMP_DRAIN();
    STAMPT(1, 6, 512);
    return;
  }
  if (tid == 0) ccnt = 0u;
  const int s = j + 256 * w;
  unsigned xr[4], sc1[4], sh[4];
#pragma unroll
  for (int i = 0; i < 4; ++i) xr[i] = x32[(size_t)s * 256 + l + 64 * i];
  {
    const u4* mp = (const u4*)(ws + OFF_MODK1) + l * 2;
    const u4 m0 = mp[0], m1 = mp[1];
    sh[0] = m0[0]; sc1[0] = m0[1]; sh[1] = m0[2]; sc1[1] = m0[3];
    sh[2] = m1[0]; sc1[2] = m1[1]; sh[3] = m1[2]; sc1[3] = m1[3];
  }
#pragma unroll
  for (int i = 0; i < 2; ++i)
    __builtin_amdgcn_raw_buffer_store_b128(u4{SENT, SENT, SENT, SENT}, rs, w < 7 ? (int)OFF_AF + ((j * 14 + 2 * w + i) * 64 + l) * 16 : OOB, 0, FILL_AUX);
  SCHED_FENCE();
  Tw256 tw;
  make_tw256(tw, l);
  tw.x2a += w * ROWP; tw.x2b += w * ROWP; tw.x3a += w * ROWP; tw.x3b += w * ROWP;
  const int k2 = tid & 255;
  f2 t512 = tw2048(4 * k2 + 512);
  f2 twq[7];
#pragma unroll
  for (int q = 1; q < 8; ++q) twq[q - 1] = tw2048(j * q);
  int zw = w * ROWP + (l >> 4) + 4 * ((l >> 2) & 3) + 17 * (l & 3);
  int za = zpos(k2), zb = zpos((256 - k2) & 255);
  int to = (int)OFF_T + (j * 256 + k2) * 64;
  asm volatile("" : "+v"(twq[0]), "+v"(twq[1]), "+v"(twq[2]), "+v"(twq[3]), "+v"(twq[4]), "+v"(twq[5]), "+v"(twq[6]), "+v"(t512));
  asm volatile("" : "+v"(zw), "+v"(za), "+v"(zb), "+v"(to));
  PIN_TW(tw);
  SCHED_FENCE();
  f2 xv[4], v[4];
#pragma unroll
  for (int i = 0; i < 4; ++i) xv[i] = unpk(xr[i]);
  ln_mod(xv, sc1, sh, v);
  STAMP(1, 1);
  fft256_fwd(v, tw, lds);
  STAMP(1, 2);
#pragma unroll
  for (int r = 0; r < 4; ++r) lds[zw + 68 * r] = v[r];
  __syncthreads();
  if (w < 4) {
    f2 a[8], z[8], zr[8];
#pragma unroll
    for (int m = 0; m < 8; ++m) { z[m] = lds[m * ROWP + za]; zr[m] = lds[m * ROWP + zb]; }
#pragma unroll
    for (int m = 0; m < 8; m += 2) herm2(a[m], a[m + 1], z[m], zr[m], t512, z[m + 1], zr[m + 1], t512);
    if (w == 0) {
#pragma unroll
      for (int m = 0; m < 8; ++m) a[m].y = k2 == 0 ? 2.0f * (z[m].x - z[m].y) : a[m].y;
    }
    bfly8<false>(a);
    cmul7<false>(a, twq);
    {
      u4* tb = tbuf + w * 256;
#pragma unroll
      for (int q = 0; q < 4; ++q)
      {
        const u4 o = __builtin_bit_cast(u4, f4{a[2 * q].x, a[2 * q].y, a[2 * q + 1].x, a[2 * q + 1].y});
        tb[l * 4 + (q ^ ((l >> 1) & 3))] = u4{nosent(o[0]), nosent(o[1]), nosent(o[2]), nosent(o[3])};
      }
      __builtin_amdgcn_wave_barrier();
#pragma unroll
      for (int i = 0; i < 4; ++i) {
        const int src = 16 * i + (l >> 2);
        __builtin_amdgcn_raw_buffer_store_b128(tb[src * 4 + ((l & 3) ^ ((src >> 1) & 3))], rs,
                                               (int)OFF_T + (j * 1024 + w * 256 + i * 64 + l) * 16, 0, AUX_SC1);
      }
    }
    STAMP(1, 3);
    STAMP_DRAIN();
    STAMP(1, 4);
  } else {
    cols_body(ws, ldc, &ccnt, tid - 256);
  }
}

constexpr int L_H2 = 0;
constexpr int L_R01 = 65536;
constexpr int L_STG = 131072;
constexpr int LDS3_BYTES = 131072 + 8 * ROWP * 8;
#define P_N1 12
#define P_W2A 12
#define W2B 20
#define FLAGSTEP 48
#define A0STEP 16
#define POLLSTEP 62
#define A2STEP 7
#define NPARK 8
#define F_W2A 8
#define F_L1NB 1
#define P_N2 16
#define P_N3 24
#define F_DELAY2 12
#define ENDBAR 1
#define S_DELAY2 24
#define A_ST 49
#define L1_EARLY(c) (!ENDBAR && (c) == 2)
#define P_L1NB 1
#define MIDBAR 1
#define S_DELAY 30
#define P_PRIO 0
#define F_DELAY 2
#define P_DELAY 4
#define BAR() do { SCHED_FENCE(); asm volatile("s_waitcnt lgkmcnt(0)" ::: "memory"); __builtin_amdgcn_s_barrier(); asm volatile("" ::: "memory"); SCHED_FENCE(); } while (0)

template <int NTB> __device__ __forceinline__ void wait_flags(unsigned v, gu32* word) {
  if (__builtin_expect(!__all(v >= (unsigned)NTB), 0)) {
    unsigned s = 0;
    do {
      __builtin_amdgcn_s_sleep(2);
      v = __hip_atomic_load(word, RLX_AGENT);
    } while (!__all(v >= (unsigned)NTB) && ++s < SPIN_CAP);
  }
  __builtin_amdgcn_fence(__ATOMIC_ACQUIRE, "wavefront");
  asm volatile("" ::: "memory");
}
template <int HAVE> constexpr int w2_at(int st) {
  if (st < 32) { const int f = HAVE + st; return f < W2B ? f : -1; }
  const int f = W2B + (st - 33);
  return (st >= 33 && f < 28) ? f : -1;
}
template <int HAVE> constexpr int vm_since(int stEnd) {
  int n = 0;
  for (int st = 32; st <= stEnd; ++st) n += (w2_at<HAVE>(st) >= 0 ? 1 : 0) + (st == POLLSTEP ? 1 : 0);
  return n;
}
static_assert(W2B <= 22 && FLAGSTEP >= 32 && FLAGSTEP < 64, "schedule");

__device__ __forceinline__ void dma16(u4 srd, int voff, int soff, unsigned dst) {
  unsigned keep;
  asm volatile("s_mov_b32 %0, m0\n\ts_mov_b32 m0, %1\n\ts_nop 0\n\tbuffer_load_dwordx4 %2, %3, %4 offen lds\n\ts_mov_b32 m0, %0"
               : "=&s"(keep) : "s"(dst), "v"(voff), "s"(srd), "s"(soff) : "memory");
}
template <int I0, int I1> __device__ __forceinline__ void dma_run(u4 srd, int voff, unsigned dst) {
#pragma unroll
  for (int i = I0; i < I1; ++i) dma16(srd, voff, i * 1024, dst + i * 1024);
}
#define VM_WAIT(n) do { SCHED_FENCE(); asm volatile("s_waitcnt vmcnt(%0)" :: "i"(n) : "memory"); SCHED_FENCE(); } while (0)
__device__ __forceinline__ u4 make_srd(const void* p, unsigned bytes) {
  const unsigned long long a = (unsigned long long)p;
  return u4{(unsigned)a, (unsigned)(a >> 32) & 0xffffu, bytes, 0x00020000u};
}
#define LDW(arr, i, off) arr[i] = __builtin_amdgcn_raw_buffer_load_b128(rs, (off) + (i) * 1024, 0, 0)

template <int CLS>
__device__ __forceinline__ void k3_body(const unsigned* __restrict__ x32, unsigned char* ws,
                                        unsigned short* __restrict__ out, unsigned char* smem, int w) {
  const int b = blockIdx.x;
  const int g = (b & 7) + 8 * (b >> 6), jj = (b >> 3) & 7;
  const int j = 8 * g + jj;
  const int tid = threadIdx.x, l = tid & 63;
  const __amdgpu_buffer_rsrc_t rs = ws_rsrc(ws);
  f2* lds = (f2*)(smem + L_STG);
  u4 wv[32], uv[28];
  constexpr bool FW = CLS == 0;
  const int wo = (int)OFF_W1F + ((7 * jj + w) * 2048 + l) * 16;
  const int uo = (int)OFF_W2F + ((jj * 56 + 7 * w) * 256 + l) * 16;
  constexpr int HAVE = FW ? F_W2A : (CLS == 1 ? 0 : 28);
  if (FW) {
    u4 tin[4];
#pragma unroll
    for (int q = 0; q < 4; ++q) tin[q] = __builtin_amdgcn_raw_buffer_load_b128(rs, (int)OFF_T + (j * 256 + tid) * 64 + q * 16, 0, 0);
    unsigned xr[2][4];
#pragma unroll
    for (int u = 0; u < 2; ++u)
#pragma unroll
      for (int i = 0; i < 4; ++i) xr[u][i] = x32[(size_t)(j + 256 * (2 * w + u)) * 256 + l + 64 * i];
    u4 mk[3];
#pragma unroll
    for (int i = 0; i < 3; ++i) mk[i] = ((const u4*)(ws + OFF_MODK3))[l * 3 + i];
    SCHED_FENCE();
    const int kl = (l >> 4) + 4 * ((l >> 2) & 3) + 16 * (l & 3);
    Tw256 tw;
    make_tw256(tw, l);
    tw.x2a += 2 * w * ROWP; tw.x2b += 2 * w * ROWP; tw.x3a += 2 * w * ROWP; tw.x3b += 2 * w * ROWP;
    f2 t512[4];
    int zk[4], zn[4];
#pragma unroll
    for (int i = 0; i < 4; ++i) {
      t512[i] = tw2048(-4 * (kl + 64 * i) - 512);
      zk[i] = 2 * w * ROWP + zpos(kl + 64 * i);
      zn[i] = 2 * w * ROWP + zpos((256 - kl - 64 * i) & 255);
    }
    f2 twq[7];
#pragma unroll
    for (int q = 1; q < 8; ++q) twq[q - 1] = tw2048(j * q);
    int zc = zpos(tid);
    asm volatile("" : "+v"(twq[0]), "+v"(twq[1]), "+v"(twq[2]), "+v"(twq[3]), "+v"(twq[4]), "+v"(twq[5]), "+v"(twq[6]));
    asm volatile("" : "+v"(t512[0]), "+v"(t512[1]), "+v"(t512[2]), "+v"(t512[3]), "+v"(zc));
    asm volatile("" : "+v"(zk[0]), "+v"(zk[1]), "+v"(zk[2]), "+v"(zk[3]), "+v"(zn[0]), "+v"(zn[1]), "+v"(zn[2]), "+v"(zn[3]));
    PIN_TW(tw);
    SCHED_FENCE();
    {
      f2 a[8];
#pragma unroll
      for (int q = 0; q < 4; ++q) { const f4 c = __builtin_bit_cast(f4, tin[q]); a[2 * q] = c.xy; a[2 * q + 1] = c.zw; }
      cmul7<true>(a, twq);
      bfly8<true>(a);
#pragma unroll
      for (int m = 0; m < 8; ++m) lds[m * ROWP + zc] = a[m];
    }
    BAR();
    STAMP(3, 1);
    f2 v[2][4];
#pragma unroll
    for (int u = 0; u < 2; ++u) {
      f2 xk[4], xn[4];
#pragma unroll
      for (int r = 0; r < 4; ++r) { xk[r] = lds[zk[r] + u * ROWP]; xn[r] = lds[zn[r] + u * ROWP]; }
      herm2(v[u][0], v[u][1], xk[0], xn[0], t512[0], xk[1], xn[1], t512[1]);
      herm2(v[u][2], v[u][3], xk[2], xn[2], t512[2], xk[3], xn[3], t512[3]);
      if (l == 0) v[u][0] = f2{xk[0].x + xk[0].y, xk[0].x - xk[0].y};
    }
    __builtin_amdgcn_wave_barrier();
    fft256_inv2<ROWP>(v, tw, lds);
    unsigned sc1[4], sh[4];
    f2 ga[4];
#pragma unroll
    for (int i = 0; i < 4; ++i) {
      ga[i] = unpk(mk[(3 * i) >> 2][(3 * i) & 3]) * 4.76837158203125e-07f;
      sc1[i] = mk[(3 * i + 1) >> 2][(3 * i + 1) & 3];
      sh[i] = mk[(3 * i + 2) >> 2][(3 * i + 2) & 3];
    }
#pragma unroll
    for (int u = 0; u < 2; ++u) {
      f2 x1[4], h2[4];
      unsigned x1p[4];
#pragma unroll
      for (int i = 0; i < 4; ++i) {
        x1p[i] = pk(unpk(xr[u][i]) + ga[i] * v[u][i]);
        x1[i] = unpk(x1p[i]);
      }
      ln_mod(x1, sc1, sh, h2);
      unsigned* st = (unsigned*)(lds + (2 * w + u) * ROWP);
#pragma unroll
      for (int i = 0; i < 4; ++i) {
        st[l + 64 * i] = x1p[i];
        st[256 + l + 64 * i] = nosent(pk(h2[i]));
      }
    }
    BAR();
    STAMP(3, 2);
    __builtin_amdgcn_s_sleep(F_DELAY);
#pragma unroll
    for (int u = 0; u < 2; ++u)
      __builtin_amdgcn_raw_buffer_store_b128(*(const u4*)((const unsigned*)(lds + (2 * w + u) * ROWP) + 4 * l), rs,
                                             (int)OFF_X1 + ((64 * g + 8 * jj + 2 * w + u) * 64 + l) * 16, 0, AUX_SC1);
    BAR();
#pragma unroll
    for (int i = 0; i < 32; ++i) wv[i] = *(const u4*)(smem + (w * 32 + i) * 1024 + l * 16);
    BAR();
    __builtin_amdgcn_s_sleep(F_DELAY2);
#pragma unroll
    for (int i = 0; i < F_W2A; ++i) LDW(uv, i, uo);
    STAMP(3, 3);
    BAR();
  } else {
    const int pw = w - 4;
    const u4 srd = make_srd(ws, (unsigned)WS_BYTES);
    const int po = (int)OFF_W1F + ((7 * jj + pw) * 2048 + l) * 16;
    dma_run<0, P_N1>(srd, po, pw * 32768);
    BAR();
    dma_run<P_N1, 32>(srd, po, pw * 32768);
    if (CLS == 1) {
#pragma unroll
      for (int i = 0; i < P_N2; ++i) LDW(wv, i, wo);
    } else {
#pragma unroll
      for (int i = 0; i < P_N2; ++i) LDW(uv, i, uo);
    }
    BAR();
    __builtin_amdgcn_s_setprio(3);
    {
      u4 ho[2];
#pragma unroll
      for (int u = 0; u < 2; ++u) {
        const int ch = 8 * (2 * pw + u) + (l >> 3);
        ho[u] = *(const u4*)((const unsigned*)(lds + (l & 7) * ROWP) + 256 + 4 * ch);
      }
      const int ha = (int)OFF_H2F + (((g * 2 + (jj >> 2)) * 32 + 8 * pw + (l >> 4)) * 64 + 8 * (jj & 3) + (l & 7) + 32 * ((l >> 3) & 1)) * 16;
#pragma unroll
      for (int u = 0; u < 2; ++u) __builtin_amdgcn_raw_buffer_store_b128(ho[u], rs, ha + u * 4096, 0, AUX_SC1);
    }
    VM_WAIT(P_N2 + 2);
    BAR();
    BAR();
    if (CLS == 1) {
#pragma unroll
      for (int i = P_N2; i < P_N3; ++i) LDW(wv, i, wo);
    } else {
#pragma unroll
      for (int i = P_N2; i < (P_N3 < 28 ? P_N3 : 28); ++i) LDW(uv, i, uo);
    }
    SCHED_FENCE();
    {
      const int hg = (int)OFF_H2F + g * 65536, hoff = (pw * 1024 + l) * 16;
      u4 hv[16];
      __builtin_amdgcn_s_sleep(P_DELAY);
      asm volatile("" ::: "memory");
#pragma unroll
      for (int i = 0; i < 16; ++i) hv[i] = __builtin_amdgcn_raw_buffer_load_b128(rs, hg + hoff + i * 1024, 0, AUX_SC1);
      STAMPT(3, 4, 256);
      SCHED_FENCE();
      if (CLS == 1) {
#pragma unroll
        for (int i = P_N3; i < 32; ++i) LDW(wv, i, wo);
      } else {
#pragma unroll
        for (int i = P_N3; i < 28; ++i) LDW(uv, i, uo);
      }
      SCHED_FENCE();
      reload_until_written<16>(hv, rs, hg + hoff, 1024);
#pragma unroll
      for (int i = 0; i < 16; ++i) *(u4*)(smem + L_H2 + hoff + i * 1024) = hv[i];
    }
    STAMPT(3, 8, 256);
    BAR();
  }
  STAMP(3, 5); STAMPT(4, 5, 256);
  const int aro = (int)OFF_AF + ((g * 8 + w) * 28 * 64 + l) * 16;
  u4 av[28];
#define LDA(i) av[i] = __builtin_amdgcn_raw_buffer_load_b128(rs, aro + (i) * 1024, 0, AUX_SC1)
  if (CLS != 2) {
    const u4* hs = (const u4*)(smem + L_H2);
    u4 hb[4];
    const int afo = (int)OFF_AF + (((g * 8 + jj) * 28 + 2 * w) * 64 + l) * 16;
#pragma unroll
    for (int st = 0; st < 3; ++st) hb[st] = hs[st * 64 + l];
#pragma unroll
    for (int th = 0; th < 2; ++th) {
      f16v acc;
#pragma unroll
      for (int i = 0; i < 16; ++i) acc[i] = 0.f;
      if (th == 0) __builtin_amdgcn_s_setprio(3); else __builtin_amdgcn_s_setprio(2);
#pragma unroll
      for (int ks = 0; ks < 32; ++ks) {
        const int st = th * 32 + ks;
        if (st + 3 < 64) hb[(st + 3) & 3] = hs[(st + 3) * 64 + l];
        if (w2_at<HAVE>(st) >= 0) LDW(uv, w2_at<HAVE>(st) < 0 ? 0 : w2_at<HAVE>(st), uo);
        if (st >= A_ST && st < A_ST + 14) LDA(st >= A_ST && st < A_ST + 14 ? st - A_ST : 0);
        SCHED_FENCE();
        acc = mfma32(wv[ks], hb[st & 3], acc);
        SCHED_FENCE();
      }
      if (FW && th == 0) asm volatile("s_waitcnt vmcnt(%0)" :: "i"(W2B - HAVE) : "memory");
#pragma unroll
      for (int sk = 0; sk < 2; ++sk) {
        u4 o;
#pragma unroll
        for (int e = 0; e < 4; ++e) o[e] = nosent(pk(f2{silu_f(acc[8 * sk + 2 * e]), silu_f(acc[8 * sk + 2 * e + 1])}));
        __builtin_amdgcn_raw_buffer_store_b128(o, rs, afo + (th * 14 + sk) * 1024, 0, AUX_SC1);
      }
      SCHED_FENCE();
      if (th == 0) { __builtin_amdgcn_s_barrier(); SCHED_FENCE(); }
    }
    __builtin_amdgcn_s_setprio(0);
    STAMP(3, 6); STAMPT(4, 6, 256);
    __builtin_amdgcn_s_barrier();
    SCHED_FENCE();
  } else {
    __builtin_amdgcn_s_setprio(0);
    __builtin_amdgcn_s_barrier();
    __builtin_amdgcn_s_sleep(S_DELAY);
  }
  asm volatile("" ::: "memory");
  __builtin_amdgcn_s_setprio(3);
#pragma unroll
  for (int i = (CLS == 2 ? 0 : 14); i < (L1_EARLY(CLS) ? 28 : 14); ++i) LDA(i);
  __builtin_amdgcn_s_setprio(0);
  SCHED_FENCE();
  const int thf = w >> 2, nbf = (w >> 1) & 1, hff = w & 1;
  const int tok = 32 * thf + (l & 31);
  const int col = 64 * jj + 32 * nbf + 16 * (l >> 5) + 8 * hff;
  u4 x1v, gbv;
#pragma unroll
  for (int th = 0; th < 2; ++th) {
    if (th == 1) {
      BAR();
      STAMP(3, 7); STAMPT(4, 7, 256);
      x1v = __builtin_amdgcn_raw_buffer_load_b128(rs, (int)OFF_X1 + ((64 * g + tok) * 64 + (col >> 3)) * 16, 0, AUX_SC1);
      gbv = __builtin_amdgcn_raw_buffer_load_b128(rs, (int)OFF_GB + col * 2, 0, 0);
      SCHED_FENCE();
    }
    reload_until_written<14>(av + th * 14, rs, aro + th * 14 * 1024, 1024);
    SCHED_FENCE();
    if (th == 0) { STAMP(3, 12); STAMPT(4, 12, 256); }
    f4* red = (f4*)(smem + (th == 0 ? L_R01 : L_H2)) + w * 512 + l;
#pragma unroll
    for (int nb = 0; nb < 2; ++nb) {
      f16v acc;
#pragma unroll
      for (int i = 0; i < 16; ++i) acc[i] = 0.f;
#pragma unroll
      for (int k = 0; k < 14; ++k) acc = mfma32(uv[2 * k + nb], av[th * 14 + k], acc);
      if (!L1_EARLY(CLS) && th == 0 && nb == (FW ? F_L1NB : (CLS == 1 ? P_L1NB : 1))) {
        SCHED_FENCE();
        if (CLS == 2) { __builtin_amdgcn_s_barrier(); __builtin_amdgcn_s_sleep(S_DELAY2); SCHED_FENCE(); }
        __builtin_amdgcn_s_setprio(3);
#pragma unroll
        for (int i = 14; i < 28; ++i) LDA(i);
        __builtin_amdgcn_s_setprio(0);
        SCHED_FENCE();
      }
#pragma unroll
      for (int q4 = 0; q4 < 4; ++q4)
        red[(nb * 4 + q4) * 64] = f4{acc[4 * q4], acc[4 * q4 + 1], acc[4 * q4 + 2], acc[4 * q4 + 3]};
      SCHED_FENCE();
    }
  }
  STAMP(3, 9); STAMPT(4, 9, 256);
  BAR();
  f4 s0 = f4{0.f, 0.f, 0.f, 0.f}, s1 = s0;
  {
    const f4* red = (const f4*)(smem + (thf == 0 ? L_R01 : L_H2)) + (nbf * 4 + 2 * hff) * 64 + l;
#pragma unroll
    for (int ww = 0; ww < 8; ++ww) {
      s0 += red[ww * 512];
      s1 += red[ww * 512 + 64];
    }
  }
  u4 o;
  o[0] = pk(unpk(x1v[0]) + unpk(gbv[0]) * s0.xy);
  o[1] = pk(unpk(x1v[1]) + unpk(gbv[1]) * s0.zw);
  o[2] = pk(unpk(x1v[2]) + unpk(gbv[2]) * s1.xy);
  o[3] = pk(unpk(x1v[3]) + unpk(gbv[3]) * s1.zw);
  const int srow = 8 * g + (tok >> 3) + 256 * (tok & 7);
  *(u4*)(out + (size_t)srow * D + col) = o;
  STAMP(3, 10); STAMPT(4, 10, 256);
  STAMP_DRAIN();
  STAMP(3, 11); STAMPT(4, 11, 256);
}

__global__ __launch_bounds__(512, 2) void k3_fused(const unsigned* __restrict__ x32, unsigned char* ws,
                                                   unsigned short* __restrict__ out) {
  extern __shared__ __attribute__((aligned(16))) unsigned char smem[];
  const int w = __builtin_amdgcn_readfirstlane(threadIdx.x >> 6);
  STAMP(3, 0);
  if (w < 4) k3_body<0>(x32, ws, out, smem, w);
  else if (w < 7) k3_body<1>(x32, ws, out, smem, w);
  else k3_body<2>(x32, ws, out, smem, w);
}

}

extern "C" void kernel_launch(void* const* d_in, const int* in_sizes, int n_in, void* d_out, int out_size,
                              void* d_ws, size_t ws_size, hipStream_t stream) {
  const unsigned short* x = (const unsigned short*)d_in[0];
  const unsigned short* wr = (const unsigned short*)d_in[1];
  const unsigned short* wi = (const unsigned short*)d_in[2];
  const unsigned short* w1 = (const unsigned short*)d_in[4];
  const unsigned short* w2 = (const unsigned short*)d_in[5];
  const unsigned short* cv = (const unsigned short*)d_in[6];
  const unsigned short* wm = (const unsigned short*)d_in[7];
  unsigned char* ws = (unsigned char*)d_ws;
  (void)hipFuncSetAttribute((const void*)k3_fused, hipFuncAttributeMaxDynamicSharedMemorySize, LDS3_BYTES);
  k0_prep<<<K0_BLOCKS, 256, 0, stream>>>(cv, wm, wr, wi, ws);
  k1_rows<<<256, 640, 0, stream>>>((const unsigned*)x, w1, w2, ws);
  k3_fused<<<256, 512, LDS3_BYTES, stream>>>((const unsigned*)x, ws, (unsigned short*)d_out);
}
```

```cpp
#include <hip/hip_runtime.h>
#include <hip/hip_bf16.h>
#include <stdint.h>
#include <type_traits>

namespace {

typedef float f2 __attribute__((ext_vector_type(2)));
typedef float f4 __attribute__((ext_vector_type(4)));
typedef unsigned u2 __attribute__((ext_vector_type(2)));
typedef unsigned u4 __attribute__((ext_vector_type(4)));
typedef short s8 __attribute__((ext_vector_type(8)));
typedef __bf16 bf2 __attribute__((ext_vector_type(2)));
#define GAS __attribute__((address_space(1)))
typedef GAS unsigned gu32;
#define RLX_AGENT __ATOMIC_RELAXED, __HIP_MEMORY_SCOPE_AGENT
#define SCHED_FENCE() __builtin_amdgcn_sched_barrier(0)

constexpr int S = 2048, D = 512, DF = 257, FF = 1792;

constexpr size_t OFF_F2    = 0;
constexpr size_t OFF_MODK1 = 262144;
constexpr size_t OFF_MODK3 = OFF_MODK1 + 2048;
constexpr size_t OFF_GB    = OFF_MODK3 + 3072;
constexpr size_t OFF_PQ    = OFF_MODK1 + 8192;
constexpr size_t OFF_WF    = OFF_PQ + 32768;
constexpr size_t OFF_T     = OFF_WF + (size_t)256 * 2048 * 4;
constexpr size_t OFF_H2F   = OFF_T + (size_t)256 * 2048 * 8;
constexpr size_t OFF_X1    = OFF_H2F + (size_t)S * D * 2;
constexpr size_t OFF_AF    = OFF_X1 + (size_t)S * D * 2;
constexpr size_t OFF_W1F   = OFF_AF + (size_t)S * FF * 2;
constexpr size_t OFF_W2F   = OFF_W1F + (size_t)D * FF * 2;
constexpr size_t WS_BYTES  = OFF_W2F + (size_t)D * FF * 2;

#define STAMP(kid, i) do {} while (0)
#define STAMPT(kid, i, thr) do {} while (0)
#define STAMP_DRAIN() do {} while (0)

__device__ __forceinline__ float bflo(unsigned u) { return __builtin_bit_cast(float, u << 16); }
__device__ __forceinline__ float bfhi(unsigned u) { return __builtin_bit_cast(float, u & 0xffff0000u); }
__device__ __forceinline__ f2 unpk(unsigned u) { return f2{bflo(u), bfhi(u)}; }
__device__ __forceinline__ unsigned pk(f2 v) {
  bf2 b = __builtin_convertvector(v, bf2);
  return __builtin_bit_cast(unsigned, b);
}
__device__ __forceinline__ f2 rb2(f2 v) { return unpk(pk(v)); }
__device__ __forceinline__ float rb1(float v) { return bflo(pk(f2{v, v})); }

__device__ __forceinline__ f2 tw2048(int t) {
  const float f = (float)t * (1.0f / 2048.0f);
  return f2{__builtin_amdgcn_cosf(f), -__builtin_amdgcn_sinf(f)};
}

__device__ __forceinline__ f2 cmul(f2 a, f2 w) {
  f2 t, d;
  asm("v_pk_mul_f32 %0, %1, %2 op_sel:[1,1] op_sel_hi:[1,0] neg_lo:[0,1]" : "=v"(t) : "v"(a), "v"(w));
  asm("v_pk_fma_f32 %0, %1, %2, %3 op_sel:[0,0,0] op_sel_hi:[0,1,1]" : "=v"(d) : "v"(a), "v"(w), "v"(t));
  return d;
}
__device__ __forceinline__ f2 cmulc(f2 a, f2 w) {
  f2 t, d;
  asm("v_pk_mul_f32 %0, %1, %2 op_sel:[0,0] op_sel_hi:[1,0]" : "=v"(t) : "v"(a), "v"(w));
  asm("v_pk_fma_f32 %0, %1, %2, %3 op_sel:[1,1,0] op_sel_hi:[0,1,1] neg_hi:[1,0,0]" : "=v"(d) : "v"(a), "v"(w), "v"(t));
  return d;
}
template <bool CONJ> __device__ __forceinline__ void cmul3(f2& a0, f2& a1, f2& a2, f2 w0, f2 w1, f2 w2) {
  f2 t0, t1, t2;
  if (!CONJ) {
    asm("v_pk_mul_f32 %3, %0, %6 op_sel:[1,1] op_sel_hi:[1,0] neg_lo:[0,1]\n\t"
        "v_pk_mul_f32 %4, %1, %7 op_sel:[1,1] op_sel_hi:[1,0] neg_lo:[0,1]\n\t"
        "v_pk_mul_f32 %5, %2, %8 op_sel:[1,1] op_sel_hi:[1,0] neg_lo:[0,1]\n\t"
        "v_pk_fma_f32 %0, %0, %6, %3 op_sel:[0,0,0] op_sel_hi:[0,1,1]\n\t"
        "v_pk_fma_f32 %1, %1, %7, %4 op_sel:[0,0,0] op_sel_hi:[0,1,1]\n\t"
        "v_pk_fma_f32 %2, %2, %8, %5 op_sel:[0,0,0] op_sel_hi:[0,1,1]"
        : "+v"(a0), "+v"(a1), "+v"(a2), "=&v"(t0), "=&v"(t1), "=&v"(t2) : "v"(w0), "v"(w1), "v"(w2));
  } else {
    asm("v_pk_mul_f32 %3, %0, %6 op_sel:[0,0] op_sel_hi:[1,0]\n\t"
        "v_pk_mul_f32 %4, %1, %7 op_sel:[0,0] op_sel_hi:[1,0]\n\t"
        "v_pk_mul_f32 %5, %2, %8 op_sel:[0,0] op_sel_hi:[1,0]\n\t"
        "v_pk_fma_f32 %0, %0, %6, %3 op_sel:[1,1,0] op_sel_hi:[0,1,1] neg_hi:[1,0,0]\n\t"
        "v_pk_fma_f32 %1, %1, %7, %4 op_sel:[1,1,0] op_sel_hi:[0,1,1] neg_hi:[1,0,0]\n\t"
        "v_pk_fma_f32 %2, %2, %8, %5 op_sel:[1,1,0] op_sel_hi:[0,1,1] neg_hi:[1,0,0]"
        : "+v"(a0), "+v"(a1), "+v"(a2), "=&v"(t0), "=&v"(t1), "=&v"(t2) : "v"(w0), "v"(w1), "v"(w2));
  }
}
template <bool CONJ> __device__ __forceinline__ void cmul4(f2& a0, f2& a1, f2& a2, f2& a3, f2 w0, f2 w1, f2 w2, f2 w3) {
  f2 t0, t1, t2, t3;
  if (!CONJ) {
    asm("v_pk_mul_f32 %4, %0, %8 op_sel:[1,1] op_sel_hi:[1,0] neg_lo:[0,1]\n\t"
        "v_pk_mul_f32 %5, %1, %9 op_sel:[1,1] op_sel_hi:[1,0] neg_lo:[0,1]\n\t"
        "v_pk_mul_f32 %6, %2, %10 op_sel:[1,1] op_sel_hi:[1,0] neg_lo:[0,1]\n\t"
        "v_pk_mul_f32 %7, %3, %11 op_sel:[1,1] op_sel_hi:[1,0] neg_lo:[0,1]\n\t"
        "v_pk_fma_f32 %0, %0, %8, %4 op_sel:[0,0,0] op_sel_hi:[0,1,1]\n\t"
        "v_pk_fma_f32 %1, %1, %9, %5 op_sel:[0,0,0] op_sel_hi:[0,1,1]\n\t"
        "v_pk_fma_f32 %2, %2, %10, %6 op_sel:[0,0,0] op_sel_hi:[0,1,1]\n\t"
        "v_pk_fma_f32 %3, %3, %11, %7 op_sel:[0,0,0] op_sel_hi:[0,1,1]"
        : "+v"(a0), "+v"(a1), "+v"(a2), "+v"(a3), "=&v"(t0), "=&v"(t1), "=&v"(t2), "=&v"(t3)
        : "v"(w0), "v"(w1), "v"(w2), "v"(w3));
  } else {
    asm("v_pk_mul_f32 %4, %0, %8 op_sel:[0,0] op_sel_hi:[1,0]\n\t"
        "v_pk_mul_f32 %5, %1, %9 op_sel:[0,0] op_sel_hi:[1,0]\n\t"
        "v_pk_mul_f32 %6, %2, %10 op_sel:[0,0] op_sel_hi:[1,0]\n\t"
        "v_pk_mul_f32 %7, %3, %11 op_sel:[0,0] op_sel_hi:[1,0]\n\t"
        "v_pk_fma_f32 %0, %0, %8, %4 op_sel:[1,1,0] op_sel_hi:[0,1,1] neg_hi:[1,0,0]\n\t"
        "v_pk_fma_f32 %1, %1, %9, %5 op_sel:[1,1,0] op_sel_hi:[0,1,1] neg_hi:[1,0,0]\n\t"
        "v_pk_fma_f32 %2, %2, %10, %6 op_sel:[1,1,0] op_sel_hi:[0,1,1] neg_hi:[1,0,0]\n\t"
        "v_pk_fma_f32 %3, %3, %11, %7 op_sel:[1,1,0] op_sel_hi:[0,1,1] neg_hi:[1,0,0]"
        : "+v"(a0), "+v"(a1), "+v"(a2), "+v"(a3), "=&v"(t0), "=&v"(t1), "=&v"(t2), "=&v"(t3)
        : "v"(w0), "v"(w1), "v"(w2), "v"(w3));
  }
}
template <bool CONJ> __device__ __forceinline__ void cmul7(f2 (&a)[8], const f2 (&w)[7]) {
  cmul3<CONJ>(a[1], a[2], a[3], w[0], w[1], w[2]);
  cmul4<CONJ>(a[4], a[5], a[6], a[7], w[3], w[4], w[5], w[6]);
}
__device__ __forceinline__ void herm2(f2& r0, f2& r1, f2 z0, f2 q0, f2 w0, f2 z1, f2 q1, f2 w1) {
  f2 d0, d1;
  asm("v_pk_add_f32 %0, %4, %5 neg_hi:[0,1]\n\t"
      "v_pk_add_f32 %2, %4, %5 neg_lo:[0,1]\n\t"
      "v_pk_add_f32 %1, %7, %8 neg_hi:[0,1]\n\t"
      "v_pk_add_f32 %3, %7, %8 neg_lo:[0,1]\n\t"
      "v_pk_fma_f32 %0, %2, %6, %0 op_sel:[1,1,0] op_sel_hi:[1,0,1] neg_lo:[0,1,0]\n\t"
      "v_pk_fma_f32 %1, %3, %9, %1 op_sel:[1,1,0] op_sel_hi:[1,0,1] neg_lo:[0,1,0]\n\t"
      "v_pk_fma_f32 %0, %2, %6, %0 op_sel:[0,0,0] op_sel_hi:[0,1,1]\n\t"
      "v_pk_fma_f32 %1, %3, %9, %1 op_sel:[0,0,0] op_sel_hi:[0,1,1]"
      : "=&v"(r0), "=&v"(r1), "=&v"(d0), "=&v"(d1) : "v"(z0), "v"(q0), "v"(w0), "v"(z1), "v"(q1), "v"(w1));
}
__device__ __forceinline__ f2 cfma(f2 a, f2 w, f2 c) {
  f2 t, d;
  asm("v_pk_fma_f32 %0, %1, %2, %3 op_sel:[1,1,0] op_sel_hi:[1,0,1] neg_lo:[0,1,0]" : "=v"(t) : "v"(a), "v"(w), "v"(c));
  asm("v_pk_fma_f32 %0, %1, %2, %3 op_sel:[0,0,0] op_sel_hi:[0,1,1]" : "=v"(d) : "v"(a), "v"(w), "v"(t));
  return d;
}
__device__ __forceinline__ f2 add_mi(f2 a, f2 b) {
  f2 d;
  asm("v_pk_add_f32 %0, %1, %2 op_sel:[0,1] op_sel_hi:[1,0] neg_hi:[0,1]" : "=v"(d) : "v"(a), "v"(b));
  return d;
}
__device__ __forceinline__ f2 add_pi(f2 a, f2 b) {
  f2 d;
  asm("v_pk_add_f32 %0, %1, %2 op_sel:[0,1] op_sel_hi:[1,0] neg_lo:[0,1]" : "=v"(d) : "v"(a), "v"(b));
  return d;
}
__device__ __forceinline__ f2 add_cj(f2 a, f2 b) {
  f2 d;
  asm("v_pk_add_f32 %0, %1, %2 neg_hi:[0,1]" : "=v"(d) : "v"(a), "v"(b));
  return d;
}
__device__ __forceinline__ f2 sub_cj(f2 a, f2 b) {
  f2 d;
  asm("v_pk_add_f32 %0, %1, %2 neg_lo:[0,1]" : "=v"(d) : "v"(a), "v"(b));
  return d;
}
template <bool INV> __device__ __forceinline__ f2 addr(f2 a, f2 b) { return INV ? add_pi(a, b) : add_mi(a, b); }
template <bool INV> __device__ __forceinline__ f2 subr(f2 a, f2 b) { return INV ? add_mi(a, b) : add_pi(a, b); }

template <bool INV> __device__ __forceinline__ void bfly4(f2& a0, f2& a1, f2& a2, f2& a3) {
  const f2 t0 = a0 + a2, t1 = a0 - a2, t2 = a1 + a3, u = a1 - a3;
  a0 = t0 + t2; a2 = t0 - t2; a1 = addr<INV>(t1, u); a3 = subr<INV>(t1, u);
}
template <bool INV> __device__ __forceinline__ void bfly8(f2 (&a)[8]) {
  constexpr float R = 0.70710678118654752440f;
  bfly4<INV>(a[0], a[2], a[4], a[6]);
  bfly4<INV>(a[1], a[3], a[5], a[7]);
  const f2 e0 = a[0], e1 = a[2], e2 = a[4], e3 = a[6];
  const f2 o0 = a[1], o2 = a[5];
  const f2 p1 = addr<INV>(a[3], a[3]);
  const f2 p3 = subr<INV>(a[7], a[7]);
  a[0] = e0 + o0; a[4] = e0 - o0;
  a[1] = e1 + R * p1; a[5] = e1 - R * p1;
  a[2] = addr<INV>(e2, o2); a[6] = subr<INV>(e2, o2);
  a[3] = e3 - R * p3; a[7] = e3 + R * p3;
}
#define XL_PAD4(v) asm volatile("s_nop 1" : "+v"(v[0]), "+v"(v[1]), "+v"(v[2]), "+v"(v[3]))
#define XL_PAD8(v) asm volatile("s_nop 1" : "+v"(v[0]), "+v"(v[1]), "+v"(v[2]), "+v"(v[3]), "+v"(v[4]), "+v"(v[5]), "+v"(v[6]), "+v"(v[7]))

__device__ __forceinline__ void swap32(f2& a, f2& b) {
  const u2 au = __builtin_bit_cast(u2, a), bu = __builtin_bit_cast(u2, b);
  const unsigned ax = au[0], ay = au[1], bx = bu[0], by = bu[1];
  auto rx = __builtin_amdgcn_permlane32_swap(ax, bx, false, false);
  auto ry = __builtin_amdgcn_permlane32_swap(ay, by, false, false);
  a = __builtin_bit_cast(f2, u2{(unsigned)rx[0], (unsigned)ry[0]});
  b = __builtin_bit_cast(f2, u2{(unsigned)rx[1], (unsigned)ry[1]});
}
__device__ __forceinline__ void swap16(f2& a, f2& b) {
  const u2 au = __builtin_bit_cast(u2, a), bu = __builtin_bit_cast(u2, b);
  const unsigned ax = au[0], ay = au[1], bx = bu[0], by = bu[1];
  auto rx = __builtin_amdgcn_permlane16_swap(ax, bx, false, false);
  auto ry = __builtin_amdgcn_permlane16_swap(ay, by, false, false);
  a = __builtin_bit_cast(f2, u2{(unsigned)rx[0], (unsigned)ry[0]});
  b = __builtin_bit_cast(f2, u2{(unsigned)rx[1], (unsigned)ry[1]});
}
__device__ __forceinline__ void swap8(f2& a, f2& b) {
  const u2 au = __builtin_bit_cast(u2, a), bu = __builtin_bit_cast(u2, b);
  const int ax = au[0], ay = au[1], bx = bu[0], by = bu[1];
  const int nax = __builtin_amdgcn_update_dpp(ax, bx, 0x128, 0xf, 0xc, false);
  const int nbx = __builtin_amdgcn_update_dpp(bx, ax, 0x128, 0xf, 0x3, false);
  const int nay = __builtin_amdgcn_update_dpp(ay, by, 0x128, 0xf, 0xc, false);
  const int nby = __builtin_amdgcn_update_dpp(by, ay, 0x128, 0xf, 0x3, false);
  a = __builtin_bit_cast(f2, u2{(unsigned)nax, (unsigned)nay});
  b = __builtin_bit_cast(f2, u2{(unsigned)nbx, (unsigned)nby});
}

template <int CTRL> __device__ __forceinline__ float dppmov(float v) {
  return __builtin_bit_cast(float, __builtin_amdgcn_update_dpp(0, __builtin_bit_cast(int, v), CTRL, 0xf, 0xf, true));
}
__device__ __forceinline__ float wave_sum(float v) {
  v += dppmov<0xB1>(v);
  v += dppmov<0x4E>(v);
  v += dppmov<0x141>(v);
  v += dppmov<0x140>(v);
  {
    unsigned u = __builtin_bit_cast(unsigned, v);
    auto r = __builtin_amdgcn_permlane16_swap(u, u, false, false);
    v = __builtin_bit_cast(float, (unsigned)r[0]) + __builtin_bit_cast(float, (unsigned)r[1]);
  }
  {
    unsigned u = __builtin_bit_cast(unsigned, v);
    auto r = __builtin_amdgcn_permlane32_swap(u, u, false, false);
    v = __builtin_bit_cast(float, (unsigned)r[0]) + __builtin_bit_cast(float, (unsigned)r[1]);
  }
  return v;
}

__device__ __forceinline__ f4 mfma(u4 a, u4 b, f4 c) {
  return __builtin_amdgcn_mfma_f32_16x16x32_bf16(__builtin_bit_cast(s8, a), __builtin_bit_cast(s8, b), c, 0, 0, 0);
}
typedef float f16v __attribute__((ext_vector_type(16)));
__device__ __forceinline__ f16v mfma32(u4 a, u4 b, f16v c) {
  return __builtin_amdgcn_mfma_f32_32x32x16_bf16(__builtin_bit_cast(s8, a), __builtin_bit_cast(s8, b), c, 0, 0, 0);
}
__device__ __forceinline__ float silu_f(float v) {
  return v * __builtin_amdgcn_rcpf(1.0f + __expf(-v));
}

__device__ __forceinline__ void wave_sum2(float& a, float& b) {
  a += dppmov<0xB1>(a);  b += dppmov<0xB1>(b);
  a += dppmov<0x4E>(a);  b += dppmov<0x4E>(b);
  a += dppmov<0x141>(a); b += dppmov<0x141>(b);
  a += dppmov<0x140>(a); b += dppmov<0x140>(b);
  unsigned ua = __builtin_bit_cast(unsigned, a), ub = __builtin_bit_cast(unsigned, b);
  auto r = __builtin_amdgcn_permlane16_swap(ua, ub, false, false);
  float c = __builtin_bit_cast(float, (unsigned)r[0]) + __builtin_bit_cast(float, (unsigned)r[1]);
  unsigned uc = __builtin_bit_cast(unsigned, c);
  auto r2 = __builtin_amdgcn_permlane32_swap(uc, uc, false, false);
  c = __builtin_bit_cast(float, (unsigned)r2[0]) + __builtin_bit_cast(float, (unsigned)r2[1]);
  a = __builtin_bit_cast(float, __builtin_amdgcn_readlane(__builtin_bit_cast(int, c), 0));
  b = __builtin_bit_cast(float, __builtin_amdgcn_readlane(__builtin_bit_cast(int, c), 16));
}
__device__ __forceinline__ void ln_mod(const f2 (&xv)[4], const unsigned (&sc1)[4], const unsigned (&sh)[4], f2 (&h)[4]) {
  const float x00 = xv[0].x;
  const float piv = __builtin_bit_cast(float, __builtin_amdgcn_readfirstlane(__builtin_bit_cast(int, x00)));
  f2 d[4];
#pragma unroll
  for (int i = 0; i < 4; ++i) d[i] = xv[i] - piv;
  const f2 s2 = (d[0] + d[1]) + (d[2] + d[3]);
  const f2 q2 = (d[0] * d[0] + d[1] * d[1]) + (d[2] * d[2] + d[3] * d[3]);
  float s1 = s2.x + s2.y, sq = q2.x + q2.y;
  wave_sum2(s1, sq);
  const float dm = s1 * (1.0f / 512.0f);
  const float var = __builtin_fmaxf(sq * (1.0f / 512.0f) - dm * dm, 0.0f);
  const float mbp = rb1(piv + dm) - piv;
  const float sd = rb1(__builtin_amdgcn_sqrtf(rb1(rb1(var) + 1e-5f)));
  const float rs = __builtin_amdgcn_rcpf(sd);
#pragma unroll
  for (int i = 0; i < 4; ++i) h[i] = ((d[i] - mbp) * rs) * unpk(sc1[i]) + unpk(sh[i]);
}

struct Tw256 { f2 a[3], b[3], c[3]; int x2a, x2b, x3a, x3b; };
__device__ __forceinline__ void make_tw256(Tw256& t, int l) {
#pragma unroll
  for (int r = 1; r < 4; ++r) {
    t.a[r - 1] = tw2048(l * r * 8);
    t.b[r - 1] = tw2048((l & 15) * r * 32);
    t.c[r - 1] = tw2048((l & 3) * r * 128);
  }
  const int d2 = l >> 4, d1 = (l >> 2) & 3, d0 = l & 3;
  t.x2a = 80 * d2 + 4 * d1 + d0;
  t.x2b = 80 * d2 + 20 * d1 + d0;
  t.x3a = 80 * d2 + 4 * d1 + d0;
  t.x3b = 80 * d2 + 4 * d1 + 17 * d0;
}
#define PIN_TW(t) do { \
  asm volatile("" : "+v"(t.a[0]), "+v"(t.a[1]), "+v"(t.a[2]), "+v"(t.b[0]), "+v"(t.b[1]), "+v"(t.b[2]), "+v"(t.c[0]), "+v"(t.c[1]), "+v"(t.c[2])); \
  asm volatile("" : "+v"(t.x2a), "+v"(t.x2b), "+v"(t.x3a), "+v"(t.x3b)); } while (0)

__device__ __forceinline__ void fft256_fwd(f2 (&v)[4], const Tw256& t, f2* lds) {
  bfly4<false>(v[0], v[1], v[2], v[3]);
  cmul3<false>(v[1], v[2], v[3], t.a[0], t.a[1], t.a[2]);
  XL_PAD4(v);
  swap32(v[0], v[2]); swap32(v[1], v[3]);
  swap16(v[0], v[1]); swap16(v[2], v[3]);
  bfly4<false>(v[0], v[1], v[2], v[3]);
  cmul3<false>(v[1], v[2], v[3], t.b[0], t.b[1], t.b[2]);
#pragma unroll
  for (int r = 0; r < 4; ++r) lds[t.x2a + 20 * r] = v[r];
  __builtin_amdgcn_wave_barrier();
#pragma unroll
  for (int r = 0; r < 4; ++r) v[r] = lds[t.x2b + 4 * r];
  __builtin_amdgcn_wave_barrier();
  bfly4<false>(v[0], v[1], v[2], v[3]);
  cmul3<false>(v[1], v[2], v[3], t.c[0], t.c[1], t.c[2]);
#pragma unroll
  for (int r = 0; r < 4; ++r) lds[t.x3a + 17 * r] = v[r];
  __builtin_amdgcn_wave_barrier();
#pragma unroll
  for (int r = 0; r < 4; ++r) v[r] = lds[t.x3b + r];
  __builtin_amdgcn_wave_barrier();
  bfly4<false>(v[0], v[1], v[2], v[3]);
}
template <class H> __device__ __forceinline__ void fft256_inv(f2 (&v)[4], const Tw256& t, f2* lds, H hook) {
  bfly4<true>(v[0], v[1], v[2], v[3]);
#pragma unroll
  for (int r = 0; r < 4; ++r) lds[t.x3b + r] = v[r];
  __builtin_amdgcn_wave_barrier();
#pragma unroll
  for (int r = 0; r < 4; ++r) v[r] = lds[t.x3a + 17 * r];
  __builtin_amdgcn_wave_barrier();
  hook(std::integral_constant<int, 0>{});
  cmul3<true>(v[1], v[2], v[3], t.c[0], t.c[1], t.c[2]);
  bfly4<true>(v[0], v[1], v[2], v[3]);
#pragma unroll
  for (int r = 0; r < 4; ++r) lds[t.x2b + 4 * r] = v[r];
  __builtin_amdgcn_wave_barrier();
#pragma unroll
  for (int r = 0; r < 4; ++r) v[r] = lds[t.x2a + 20 * r];
  __builtin_amdgcn_wave_barrier();
  hook(std::integral_constant<int, 1>{});
  cmul3<true>(v[1], v[2], v[3], t.b[0], t.b[1], t.b[2]);
  bfly4<true>(v[0], v[1], v[2], v[3]);
  XL_PAD4(v);
  swap16(v[0], v[1]); swap16(v[2], v[3]);
  swap32(v[0], v[2]); swap32(v[1], v[3]);
  hook(std::integral_constant<int, 2>{});
  cmul3<true>(v[1], v[2], v[3], t.a[0], t.a[1], t.a[2]);
  bfly4<true>(v[0], v[1], v[2], v[3]);
}
template <int RP> __device__ __forceinline__ void fft256_inv2(f2 (&v)[2][4], const Tw256& t, f2* lds) {
#pragma unroll
  for (int u = 0; u < 2; ++u) bfly4<true>(v[u][0], v[u][1], v[u][2], v[u][3]);
#pragma unroll
  for (int u = 0; u < 2; ++u)
#pragma unroll
    for (int r = 0; r < 4; ++r) lds[t.x3b + u * RP + r] = v[u][r];
  __builtin_amdgcn_wave_barrier();
#pragma unroll
  for (int u = 0; u < 2; ++u)
#pragma unroll
    for (int r = 0; r < 4; ++r) v[u][r] = lds[t.x3a + u * RP + 17 * r];
  __builtin_amdgcn_wave_barrier();
#pragma unroll
  for (int u = 0; u < 2; ++u) {
    cmul3<true>(v[u][1], v[u][2], v[u][3], t.c[0], t.c[1], t.c[2]);
    bfly4<true>(v[u][0], v[u][1], v[u][2], v[u][3]);
#pragma unroll
    for (int r = 0; r < 4; ++r) lds[t.x2b + u * RP + 4 * r] = v[u][r];
  }
  __builtin_amdgcn_wave_barrier();
#pragma unroll
  for (int u = 0; u < 2; ++u)
#pragma unroll
    for (int r = 0; r < 4; ++r) v[u][r] = lds[t.x2a + u * RP + 20 * r];
  __builtin_amdgcn_wave_barrier();
#pragma unroll
  for (int u = 0; u < 2; ++u) {
    cmul3<true>(v[u][1], v[u][2], v[u][3], t.b[0], t.b[1], t.b[2]);
    bfly4<true>(v[u][0], v[u][1], v[u][2], v[u][3]);
  }
  XL_PAD4(v[0]); XL_PAD4(v[1]);
#pragma unroll
  for (int u = 0; u < 2; ++u) {
    swap16(v[u][0], v[u][1]); swap16(v[u][2], v[u][3]);
    swap32(v[u][0], v[u][2]); swap32(v[u][1], v[u][3]);
  }
#pragma unroll
  for (int u = 0; u < 2; ++u) {
    cmul3<true>(v[u][1], v[u][2], v[u][3], t.a[0], t.a[1], t.a[2]);
    bfly4<true>(v[u][0], v[u][1], v[u][2], v[u][3]);
  }
}
__device__ __forceinline__ int zpos(int k) { return k + (k >> 4); }

__device__ __host__ __forceinline__ int k2_k1(int tt, int kb) {
  int seq = (tt >> 6) * 8 + (tt & 7);
  int q = seq >> 2, k0 = seq & 3, ka = (tt >> 3) & 7;
  return 8 * (k0 + 4 * ka + 32 * kb) + q;
}

__device__ __forceinline__ void transpose8x8(const u4 (&in)[8], u4 (&out)[8]) {
#pragma unroll
  for (int c2 = 0; c2 < 4; ++c2) {
#pragma unroll
    for (int r2 = 0; r2 < 4; ++r2) {
      unsigned a = in[2 * r2][c2], b = in[2 * r2 + 1][c2];
      out[2 * c2][r2] = __builtin_amdgcn_perm(b, a, 0x05040100u);
      out[2 * c2 + 1][r2] = __builtin_amdgcn_perm(b, a, 0x07060302u);
    }
  }
}

#define K1_COAL 1
#define MOD_FILL_LATE 0
#define CODE_PF 1
#define PREP_AUX 16
#define T_AUX 16
#define T2_AUX 16
#define WF_AUX 16
#define OUT_NT 0
constexpr int AUX_SC1 = 16;
constexpr int OOB = 0x40000000;
__device__ __forceinline__ __amdgpu_buffer_rsrc_t ws_rsrc(unsigned char* ws) {
  return __builtin_amdgcn_make_buffer_rsrc((void*)ws, 0, (int)WS_BYTES, 0x00020000);
}

__device__ __forceinline__ int prep_w1_off(int job, int l, int r) {
  const int cg = job % 7, ks = job / 7, no = l & 31, h = l >> 5;
  return ((16 * ks + 8 * h + r) * FF + cg * 256 + no * 8) * 2;
}
__device__ __forceinline__ void prep_w1_fin(const u4 (&in)[8], __amdgpu_buffer_rsrc_t rs, u4* buf, int job, int l) {
  const int cg = job % 7, ks = job / 7, no = l & 31, h = l >> 5;
  u4 out[8];
  transpose8x8(in, out);
#pragma unroll
  for (int cc = 0; cc < 8; ++cc) buf[((no >> 2) * 64 + 8 * (no & 3) + 32 * h + cc) ^ (no & 7)] = out[cc];
  __builtin_amdgcn_wave_barrier();
#pragma unroll
  for (int f = 0; f < 8; ++f)
    __builtin_amdgcn_raw_buffer_store_b128(buf[(f * 64 + l) ^ ((4 * f + ((l >> 3) & 3)) & 7)], rs,
                                           (int)OFF_W1F + (((cg * 8 + f) * 32 + ks) * 64 + l) * 16, 0, PREP_AUX);
}
__device__ __forceinline__ int prep_w2_off(int job, int l, int e) {
  const int cg = job & 1, cs = job >> 1, no = l & 31, h = l >> 5;
  return ((16 * cs + 8 * (e >> 2) + 4 * h + (e & 3)) * D + cg * 256 + no * 8) * 2;
}
__device__ __forceinline__ void prep_w2_fin(const u4 (&in)[8], __amdgpu_buffer_rsrc_t rs, u4* buf, int job, int l) {
  const int cg = job & 1, cs = job >> 1, no = l & 31, h = l >> 5;
  u4 out[8];
  transpose8x8(in, out);
  const int base = (no >> 2) * 64 + 16 * (no & 1) + 4 * ((no >> 1) & 1) + 32 * h;
#pragma unroll
  for (int cc = 0; cc < 8; ++cc) buf[base + (cc & 3) + 8 * (cc >> 2)] = out[cc];
  __builtin_amdgcn_wave_barrier();
#pragma unroll
  for (int f = 0; f < 8; ++f)
    __builtin_amdgcn_raw_buffer_store_b128(buf[f * 64 + l], rs,
                                           (int)OFF_W2F + ((((cg * 4 + (f >> 1)) * 112 + cs) * 2 + (f & 1)) * 64 + l) * 16, 0, PREP_AUX);
}

__device__ __forceinline__ __amdgpu_buffer_rsrc_t code_rsrc(int& ob) {
  unsigned long long pc;
  int oe;
  asm volatile("s_getpc_b64 %0\n1:\n\ts_mov_b32 %1, gf_code_begin-1b\n\ts_mov_b32 %2, gf_code_end-1b" : "=s"(pc), "=s"(ob), "=s"(oe));
  return __builtin_amdgcn_make_buffer_rsrc((void*)pc, 0, oe, 0x00020000);
}

constexpr int K0_MOD = 96, K0_WF = 128, K0_MISC = 32;
constexpr int K0_BLOCKS = K0_MOD + K0_WF + K0_MISC;
constexpr unsigned SENT = 0xFFFFFFFFu;
constexpr int FILL_PER_THREAD = (int)((OFF_H2F - OFF_T) / (256 * 256 * 16));
static_assert((size_t)FILL_PER_THREAD * 256 * 256 * 16 == OFF_H2F - OFF_T && K0_BLOCKS == 256, "fill covers the spectrum hand-off buffer");
#define FILL_AUX 16

__global__ __launch_bounds__(256) void k0_prep(const unsigned short* __restrict__ cvec,
                                               const unsigned short* __restrict__ wmod,
                                               const unsigned short* __restrict__ wr,
                                               const unsigned short* __restrict__ wi,
                                               unsigned char* ws) {
  __shared__ float red[4][32];
  __shared__ u4 wbuf[1024];
  const int tid = threadIdx.x;
  int b = blockIdx.x;
  unsigned tcode;
  {
    int ob;
    const __amdgpu_buffer_rsrc_t cr = code_rsrc(ob);
    int co_ = tid < 128 ? (tid * 64 < ob ? tid * 64 : OOB) : (b >= K0_MOD && tid < 192 ? ob + (((b - K0_MOD) >> 3) * 64 + (tid - 128)) * 64 : OOB);
    tcode = __builtin_amdgcn_raw_buffer_load_b32(cr, co_, 0, 0);
  }
  SCHED_FENCE();
  STAMP(0, 0);
  const __amdgpu_buffer_rsrc_t rs = ws_rsrc(ws);
  auto fill = [&]() {
    asm volatile("" :: "v"(tcode));

#pragma unroll
    for (int i = 0; i < FILL_PER_THREAD; ++i)
      __builtin_amdgcn_raw_buffer_store_b128(u4{SENT, SENT, SENT, SENT}, rs,
                                             (int)OFF_T + ((blockIdx.x * FILL_PER_THREAD + i) * 256 + tid) * 16, 0, FILL_AUX);
  };
  if (b < K0_MOD) {
    const int co = tid & 3, rg = tid >> 2;
    u4 wv[8];
    unsigned short cs[8];
#pragma unroll
    for (int i = 0; i < 8; ++i) cs[i] = cvec[rg + 64 * i];
#pragma unroll
    for (int i = 0; i < 8; ++i)
      wv[i] = *(const u4*)(wmod + (size_t)(rg + 64 * i) * 3072 + b * 32 + co * 8);
    SCHED_FENCE();
    fill();
    SCHED_FENCE();
    f2 acc[4];
#pragma unroll
    for (int e = 0; e < 4; ++e) acc[e] = f2{0.f, 0.f};
#pragma unroll
    for (int i = 0; i < 8; ++i) {
      const float cv = bflo(cs[i]);
      const float sv = rb1(silu_f(cv));
#pragma unroll
      for (int e2 = 0; e2 < 4; ++e2) acc[e2] += sv * unpk(wv[i][e2]);
    }
    STAMP(0, 2);
#pragma unroll
    for (int e2 = 0; e2 < 4; ++e2) {
      float v0 = acc[e2].x, v1 = acc[e2].y;
      v0 += dppmov<0x124>(v0); v1 += dppmov<0x124>(v1);
      v0 += dppmov<0x128>(v0); v1 += dppmov<0x128>(v1);
      unsigned u0 = __builtin_bit_cast(unsigned, v0), u1 = __builtin_bit_cast(unsigned, v1);
      auto r = __builtin_amdgcn_permlane16_swap(u0, u1, false, false);
      float c = __builtin_bit_cast(float, (unsigned)r[0]) + __builtin_bit_cast(float, (unsigned)r[1]);
      unsigned uc = __builtin_bit_cast(unsigned, c);
      auto r2 = __builtin_amdgcn_permlane32_swap(uc, uc, false, false);
      c = __builtin_bit_cast(float, (unsigned)r2[0]) + __builtin_bit_cast(float, (unsigned)r2[1]);
      if ((tid & 0x2c) == 0) red[tid >> 6][co * 8 + e2 * 2 + ((tid >> 4) & 1)] = c;
    }
    __syncthreads();
    if (tid < 32) {
      float s = rb1((red[0][tid] + red[1][tid]) + (red[2][tid] + red[3][tid]));
      const int col = b * 32 + tid, p = col >> 9, e = col & 511;
      if (p == 1 || p == 4) s = rb1(s + 1.0f);
      const unsigned short sb = (unsigned short)(__builtin_bit_cast(unsigned, s) >> 16);
      const int n = e >> 1, l = n & 63, i = n >> 6, hf = e & 1;
      if (p < 2) ((unsigned short*)(ws + OFF_MODK1))[((l * 4 + i) * 2 + p) * 2 + hf] = sb;
      else if (p < 5) ((unsigned short*)(ws + OFF_MODK3))[((l * 4 + i) * 3 + (p == 2 ? 0 : (p == 4 ? 1 : 2))) * 2 + hf] = sb;
      else ((unsigned short*)(ws + OFF_GB))[e] = sb;
    }
    STAMP(0, 1);
    return;
  }
  b -= K0_MOD;
  if (b < K0_WF) {
    const int tq = b >> 1, k2 = (b & 1) * 128 + (tid & 127), tp2 = tid >> 7;
    unsigned short vr[16], vi[16];
#pragma unroll
    for (int e = 0; e < 16; ++e) {
      const int k1 = k2_k1(4 * tq + 2 * tp2 + (e >> 3), e & 7);
      vr[e] = wr[k1 * DF + k2];
      vi[e] = wi[k1 * DF + k2];
    }
    SCHED_FENCE();
    fill();
    SCHED_FENCE();
#pragma unroll
    for (int e4 = 0; e4 < 4; ++e4) {
      u4 o;
#pragma unroll
      for (int e = 0; e < 4; ++e) o[e] = (unsigned)vr[e4 * 4 + e] | ((unsigned)vi[e4 * 4 + e] << 16);
      wbuf[(tid & 127) * 8 + ((tp2 * 4 + e4) ^ (tid & 7))] = o;
    }
    __syncthreads();
#pragma unroll
    for (int i = 0; i < 4; ++i) {
      const int kl = i * 32 + (tid >> 3);
      __builtin_amdgcn_raw_buffer_store_b128(wbuf[kl * 8 + ((tid & 7) ^ (kl & 7))], rs,
                                             (int)OFF_WF + (((b & 1) * 128 + kl) * 512 + tq * 8 + (tid & 7)) * 16, 0, WF_AUX);
    }
    STAMP(0, 3);
    STAMP_DRAIN();
    STAMP(0, 5);
    return;
  }
  b -= K0_WF;
  fill();
  if (tid < 64) {
    const int e = b * 64 + tid;
    const int tt = e & 255, kb = e >> 8;
    const int k1 = k2_k1(tt, kb), nk = (2048 - k1) & 2047;
    f2 a0 = f2{bflo(wr[k1 * DF]), bflo(wi[k1 * DF])};
    f2 a1 = f2{bflo(wr[nk * DF]), -bflo(wi[nk * DF])};
    f2 b0 = f2{bflo(wr[k1 * DF + 256]), bflo(wi[k1 * DF + 256])};
    f2 b1 = f2{bflo(wr[nk * DF + 256]), -bflo(wi[nk * DF + 256])};
    f2 w0h = (a0 + a1) * 0.5f, w256h = (b0 + b1) * 0.5f;
    f2 P = (w0h + w256h) * 0.5f, Q = (w0h - w256h) * 0.5f;
    ((f4*)(ws + OFF_PQ))[e] = f4{P.x, P.y, Q.x, Q.y};
    STAMP(0, 4);
  }
}

constexpr unsigned SPIN_CAP = 1u << 18;
__device__ __forceinline__ unsigned nosent(unsigned v) { return v < SENT - 1u ? v : SENT - 1u; }
template <int N> __device__ __forceinline__ bool all_written(const u4* v) {
  unsigned m = 0u;
#pragma unroll
  for (int i = 0; i < N; ++i) {
    const unsigned a = v[i][0] > v[i][1] ? v[i][0] : v[i][1], c = v[i][2] > v[i][3] ? v[i][2] : v[i][3];
    m = m > a ? m : a;
    m = m > c ? m : c;
  }
  return __all(m != SENT);
}
template <int N> __device__ __forceinline__ void reload_until_written(u4* v, __amdgpu_buffer_rsrc_t rs, int off, int stride) {
  if (__builtin_expect(!all_written<N>(v), 0)) {
    unsigned s = 0;
    bool ok;
    do {
      __builtin_amdgcn_s_sleep(1);
      asm volatile("" : "+v"(off) :: "memory");
#pragma unroll
      for (int i = 0; i < N; ++i) v[i] = __builtin_amdgcn_raw_buffer_load_b128(rs, off + i * stride, 0, AUX_SC1);
      ok = all_written<N>(v);
    } while (!ok && ++s < SPIN_CAP);
  }
}

constexpr int SEQP = 64 + 1;
#define PREP_DELAY 60
#define C_DELAY 40
__device__ __forceinline__ void sync4(unsigned* cnt, unsigned& target, int l) {
  target += 4u;
  SCHED_FENCE();
  asm volatile("s_waitcnt lgkmcnt(0)" ::: "memory");
  if (l == 0) __hip_atomic_fetch_add(cnt, 1u, __ATOMIC_RELAXED, __HIP_MEMORY_SCOPE_WORKGROUP);
  for (unsigned sp = 0; sp < (SPIN_CAP << 4); ++sp)
    if (__hip_atomic_load(cnt, __ATOMIC_RELAXED, __HIP_MEMORY_SCOPE_WORKGROUP) >= target) break;
  asm volatile("" ::: "memory");
  SCHED_FENCE();
}
__device__ __forceinline__ void cols_body(unsigned char* ws, f2* lds, unsigned* cnt, int t) {
  unsigned tgt = 0u;
  const int b = blockIdx.x;
  const int l = t & 63;
  const __amdgpu_buffer_rsrc_t rs = ws_rsrc(ws);
  const int k2 = (b & 7) * 32 + (b >> 3);
  const int jr = t >> 2, qp = t & 3;
  const int co = (int)OFF_T + (jr * 1024 + k2 * 4 + qp) * 16;
  u4 wq[8];
  {
    const int wo = k2 != 0 ? (int)OFF_WF + (k2 * 256 + t) * 32 : (int)OFF_PQ + t * 16;
    const int ws1 = k2 != 0 ? 16 : 4096;
    wq[0] = __builtin_amdgcn_raw_buffer_load_b128(rs, wo, 0, 0);
    wq[1] = __builtin_amdgcn_raw_buffer_load_b128(rs, wo + ws1, 0, 0);
    const int po = k2 != 0 ? OOB : wo;
#pragma unroll
    for (int kb = 2; kb < 8; ++kb) wq[kb] = __builtin_amdgcn_raw_buffer_load_b128(rs, po + kb * 4096, 0, 0);
  }
  SCHED_FENCE();
  f2 twA[3], twB[7];
#pragma unroll
  for (int r = 1; r < 4; ++r) twA[r - 1] = tw2048(jr * r * 8);
  const int bb = l >> 3;
#pragma unroll
  for (int r = 1; r < 8; ++r) twB[r - 1] = tw2048(bb * r * 32);
  int pa = (8 * qp) * SEQP + jr;
  int pb = ((t >> 6) * 8 + (t & 7)) * SEQP + bb;
  asm volatile("" : "+v"(twB[0]), "+v"(twB[1]), "+v"(twB[2]), "+v"(twB[3]), "+v"(twB[4]), "+v"(twB[5]), "+v"(twB[6]), "+v"(twA[0]), "+v"(twA[1]), "+v"(twA[2]));
  asm volatile("" : "+v"(pa), "+v"(pb));
  SCHED_FENCE();
  __builtin_amdgcn_s_sleep(C_DELAY);
  asm volatile("" ::: "memory");
  u4 cin[4];
#pragma unroll
  for (int i = 0; i < 4; ++i) cin[i] = __builtin_amdgcn_raw_buffer_load_b128(rs, co + i * (1 << 20), 0, AUX_SC1);
  reload_until_written<4>(cin, rs, co, 1 << 20);
  SCHED_FENCE();
#pragma unroll
  for (int i = 0; i < 2; ++i)
    __builtin_amdgcn_raw_buffer_store_b128(u4{SENT, SENT, SENT, SENT}, rs, (int)OFF_H2F + (b * 512 + i * 256 + t) * 16, 0, FILL_AUX);
#pragma unroll
  for (int i = 0; i < 4; ++i)
    __builtin_amdgcn_raw_buffer_store_b128(u4{SENT, SENT, SENT, SENT}, rs,
                                           i * 256 + t < 896 ? (int)OFF_AF + ((256 + b) * 896 + i * 256 + t) * 16 : OOB, 0, FILL_AUX);
  SCHED_FENCE();
  STAMPT(2, 0, 256);
  f2 a0[4], a1[4];
#pragma unroll
  for (int i = 0; i < 4; ++i) { const f4 c = __builtin_bit_cast(f4, cin[i]); a0[i] = c.xy; a1[i] = c.zw; }
  bfly4<false>(a0[0], a0[1], a0[2], a0[3]);
  bfly4<false>(a1[0], a1[1], a1[2], a1[3]);
  cmul3<false>(a0[1], a0[2], a0[3], twA[0], twA[1], twA[2]);
  cmul3<false>(a1[1], a1[2], a1[3], twA[0], twA[1], twA[2]);
#pragma unroll
  for (int r = 0; r < 4; ++r) {
    lds[pa + r * SEQP] = a0[r];
    lds[pa + (4 + r) * SEQP] = a1[r];
  }
  sync4(cnt, tgt, l);
  STAMPT(2, 1, 256);
  f2 v[8];
#pragma unroll
  for (int a = 0; a < 8; ++a) v[a] = lds[pb + 8 * a];
  bfly8<false>(v);
  cmul7<false>(v, twB);
  XL_PAD8(v);
#pragma unroll
  for (int i = 0; i < 4; ++i) swap32(v[i], v[i + 4]);
  swap16(v[0], v[2]); swap16(v[1], v[3]); swap16(v[4], v[6]); swap16(v[5], v[7]);
#pragma unroll
  for (int i = 0; i < 8; i += 2) swap8(v[i], v[i + 1]);
  bfly8<false>(v);
  if (k2 != 0) {
    cmul4<false>(v[0], v[1], v[2], v[3], unpk(wq[0][0]), unpk(wq[0][1]), unpk(wq[0][2]), unpk(wq[0][3]));
    cmul4<false>(v[4], v[5], v[6], v[7], unpk(wq[1][0]), unpk(wq[1][1]), unpk(wq[1][2]), unpk(wq[1][3]));
  } else {
    sync4(cnt, tgt, l);
#pragma unroll
    for (int kb = 0; kb < 8; ++kb) lds[k2_k1(t, kb)] = v[kb];
    sync4(cnt, tgt, l);
#pragma unroll
    for (int kb = 0; kb < 8; ++kb) {
      f2 fn = lds[(2048 - k2_k1(t, kb)) & 2047];
      fn.y = -fn.y;
      const f4 c = __builtin_bit_cast(f4, wq[kb]);
      v[kb] = cfma(fn, c.zw, cmul(v[kb], c.xy));
    }
  }
  bfly8<true>(v);
  XL_PAD8(v);
#pragma unroll
  for (int i = 0; i < 8; i += 2) swap8(v[i], v[i + 1]);
  swap16(v[0], v[2]); swap16(v[1], v[3]); swap16(v[4], v[6]); swap16(v[5], v[7]);
#pragma unroll
  for (int i = 0; i < 4; ++i) swap32(v[i], v[i + 4]);
  cmul7<true>(v, twB);
  bfly8<true>(v);
  if (k2 == 0) sync4(cnt, tgt, l);
#pragma unroll
  for (int a = 0; a < 8; ++a) lds[pb + 8 * a] = v[a];
  sync4(cnt, tgt, l);
#pragma unroll
  for (int r = 0; r < 4; ++r) {
    a0[r] = lds[pa + r * SEQP];
    a1[r] = lds[pa + (4 + r) * SEQP];
  }
  cmul3<true>(a0[1], a0[2], a0[3], twA[0], twA[1], twA[2]);
  cmul3<true>(a1[1], a1[2], a1[3], twA[0], twA[1], twA[2]);
  bfly4<true>(a0[0], a0[1], a0[2], a0[3]);
  bfly4<true>(a1[0], a1[1], a1[2], a1[3]);
#pragma unroll
  for (int i = 0; i < 4; ++i)
    __builtin_amdgcn_raw_buffer_store_b128(__builtin_bit_cast(u4, f4{a0[i].x, a0[i].y, a1[i].x, a1[i].y}), rs, co + i * (1 << 20), 0, T2_AUX);
  STAMPT(2, 2, 256);
  STAMP_DRAIN();
  STAMPT(2, 3, 256);
}

constexpr int ROWP = 322;

__global__ __launch_bounds__(640) void k1_rows(const unsigned* __restrict__ x32, const unsigned short* __restrict__ w1,
                                               const unsigned short* __restrict__ w2, unsigned char* ws) {
  __shared__ __attribute__((aligned(16))) f2 lds[8 * ROWP];
  __shared__ u4 pbuf[1024], tbuf[1024];
  __shared__ __attribute__((aligned(16))) f2 ldc[2048 + 64];
  __shared__ unsigned ccnt;
  asm volatile("gf_code_begin:");
  const int j = blockIdx.x;
  const int tid = threadIdx.x, w = __builtin_amdgcn_readfirstlane(tid >> 6), l = tid & 63;
  const __amdgpu_buffer_rsrc_t rs = ws_rsrc(ws);
  STAMP(1, 0);
  if (w >= 8) {
    if (j >= 224) return;
    u4 pin[8];
    const int jc = j;
    const unsigned char* src = (const unsigned char*)(w == 8 ? w1 : w2);
#pragma unroll
    for (int r = 0; r < 8; ++r) pin[r] = *(const u4*)(src + (w == 8 ? prep_w1_off(jc, l, r) : prep_w2_off(jc, l, r)));
    asm volatile("" ::: "memory");
    SCHED_FENCE();
    __builtin_amdgcn_s_barrier();
    __builtin_amdgcn_s_sleep(PREP_DELAY);
    SCHED_FENCE();
    if (w == 8) prep_w1_fin(pin, rs, pbuf, j, l);
    else prep_w2_fin(pin, rs, pbuf + 512, j, l);
    STAMPT(1, 5, 512);
    STAMP_DRAIN();
    STAMPT(1, 6, 512);
    return;
  }
  if (tid == 0) ccnt = 0u;
  const int s = j + 256 * w;
  unsigned xr[4], sc1[4], sh[4];
#pragma unroll
  for (int i = 0; i < 4; ++i) xr[i] = x32[(size_t)s * 256 + l + 64 * i];
  {
    const u4* mp = (const u4*)(ws + OFF_MODK1) + l * 2;
    const u4 m0 = mp[0], m1 = mp[1];
    sh[0] = m0[0]; sc1[0] = m0[1]; sh[1] = m0[2]; sc1[1] = m0[3];
    sh[2] = m1[0]; sc1[2] = m1[1]; sh[3] = m1[2]; sc1[3] = m1[3];
  }
#pragma unroll
  for (int i = 0; i < 2; ++i)
    __builtin_amdgcn_raw_buffer_store_b128(u4{SENT, SENT, SENT, SENT}, rs, w < 7 ? (int)OFF_AF + ((j * 14 + 2 * w + i) * 64 + l) * 16 : OOB, 0, FILL_AUX);
  SCHED_FENCE();
  Tw256 tw;
  make_tw256(tw, l);
  tw.x2a += w * ROWP; tw.x2b += w * ROWP; tw.x3a += w * ROWP; tw.x3b += w * ROWP;
  const int k2 = tid & 255;
  f2 t512 = tw2048(4 * k2 + 512);
  f2 twq[7];
#pragma unroll
  for (int q = 1; q < 8; ++q) twq[q - 1] = tw2048(j * q);
  int zw = w * ROWP + (l >> 4) + 4 * ((l >> 2) & 3) + 17 * (l & 3);
  int za = zpos(k2), zb = zpos((256 - k2) & 255);
  int to = (int)OFF_T + (j * 256 + k2) * 64;
  asm volatile("" : "+v"(twq[0]), "+v"(twq[1]), "+v"(twq[2]), "+v"(twq[3]), "+v"(twq[4]), "+v"(twq[5]), "+v"(twq[6]), "+v"(t512));
  asm volatile("" : "+v"(zw), "+v"(za), "+v"(zb), "+v"(to));
  PIN_TW(tw);
  SCHED_FENCE();
  f2 xv[4], v[4];
#pragma unroll
  for (int i = 0; i < 4; ++i) xv[i] = unpk(xr[i]);
  ln_mod(xv, sc1, sh, v);
  STAMP(1, 1);
  fft256_fwd(v, tw, lds);
  STAMP(1, 2);
#pragma unroll
  for (int r = 0; r < 4; ++r) lds[zw + 68 * r] = v[r];
  __syncthreads();
  if (w < 4) {
    f2 a[8], z[8], zr[8];
#pragma unroll
    for (int m = 0; m < 8; ++m) { z[m] = lds[m * ROWP + za]; zr[m] = lds[m * ROWP + zb]; }
#pragma unroll
    for (int m = 0; m < 8; m += 2) herm2(a[m], a[m + 1], z[m], zr[m], t512, z[m + 1], zr[m + 1], t512);
    if (w == 0) {
#pragma unroll
      for (int m = 0; m < 8; ++m) a[m].y = k2 == 0 ? 2.0f * (z[m].x - z[m].y) : a[m].y;
    }
    bfly8<false>(a);
    cmul7<false>(a, twq);
    {
      u4* tb = tbuf + w * 256;
#pragma unroll
      for (int q = 0; q < 4; ++q)
      {
        const u4 o = __builtin_bit_cast(u4, f4{a[2 * q].x, a[2 * q].y, a[2 * q + 1].x, a[2 * q + 1].y});
        tb[l * 4 + (q ^ ((l >> 1) & 3))] = u4{nosent(o[0]), nosent(o[1]), nosent(o[2]), nosent(o[3])};
      }
      __builtin_amdgcn_wave_barrier();
#pragma unroll
      for (int i = 0; i < 4; ++i) {
        const int src = 16 * i + (l >> 2);
        __builtin_amdgcn_raw_buffer_store_b128(tb[src * 4 + ((l & 3) ^ ((src >> 1) & 3))], rs,
                                               (int)OFF_T + (j * 1024 + w * 256 + i * 64 + l) * 16, 0, AUX_SC1);
      }
    }
    STAMP(1, 3);
    STAMP_DRAIN();
    STAMP(1, 4);
  } else {
    cols_body(ws, ldc, &ccnt, tid - 256);
  }
}

constexpr int L_H2 = 0;
constexpr int L_R01 = 65536;
constexpr int L_STG = 131072;
constexpr int LDS3_BYTES = 131072 + 8 * ROWP * 8;
#define P_N1 12
#define P_W2A 12
#define W2B 20
#define FLAGSTEP 48
#define A0STEP 16
#define POLLSTEP 62
#define A2STEP 7
#define NPARK 8
#define F_W2A 8
#define F_L1NB 1
#define P_N2 16
#define P_N3 24
#define F_DELAY2 12
#define ENDBAR 1
#define S_DELAY2 24
#define A_ST 49
#define L1_EARLY(c) (!ENDBAR && (c) == 2)
#define P_L1NB 1
#define MIDBAR 1
#define S_DELAY 30
#define P_PRIO 0
#define F_DELAY 2
#define P_DELAY 4
#define BAR() do { SCHED_FENCE(); asm volatile("s_waitcnt lgkmcnt(0)" ::: "memory"); __builtin_amdgcn_s_barrier(); asm volatile("" ::: "memory"); SCHED_FENCE(); } while (0)

template <int NTB> __device__ __forceinline__ void wait_flags(unsigned v, gu32* word) {
  if (__builtin_expect(!__all(v >= (unsigned)NTB), 0)) {
    unsigned s = 0;
    do {
      __builtin_amdgcn_s_sleep(2);
      v = __hip_atomic_load(word, RLX_AGENT);
    } while (!__all(v >= (unsigned)NTB) && ++s < SPIN_CAP);
  }
  __builtin_amdgcn_fence(__ATOMIC_ACQUIRE, "wavefront");
  asm volatile("" ::: "memory");
}
template <int HAVE> constexpr int w2_at(int st) {
  if (st < 32) { const int f = HAVE + st; return f < W2B ? f : -1; }
  const int f = W2B + (st - 33);
  return (st >= 33 && f < 28) ? f : -1;
}
template <int HAVE> constexpr int vm_since(int stEnd) {
  int n = 0;
  for (int st = 32; st <= stEnd; ++st) n += (w2_at<HAVE>(st) >= 0 ? 1 : 0) + (st == POLLSTEP ? 1 : 0);
  return n;
}
static_assert(W2B <= 22 && FLAGSTEP >= 32 && FLAGSTEP < 64, "schedule");

__device__ __forceinline__ void dma16(u4 srd, int voff, int soff, unsigned dst) {
  unsigned keep;
  asm volatile("s_mov_b32 %0, m0\n\ts_mov_b32 m0, %1\n\ts_nop 0\n\tbuffer_load_dwordx4 %2, %3, %4 offen lds\n\ts_mov_b32 m0, %0"
               : "=&s"(keep) : "s"(dst), "v"(voff), "s"(srd), "s"(soff) : "memory");
}
template <int I0, int I1> __device__ __forceinline__ void dma_run(u4 srd, int voff, unsigned dst) {
#pragma unroll
  for (int i = I0; i < I1; ++i) dma16(srd, voff, i * 1024, dst + i * 1024);
}
#define VM_WAIT(n) do { SCHED_FENCE(); asm volatile("s_waitcnt vmcnt(%0)" :: "i"(n) : "memory"); SCHED_FENCE(); } while (0)
__device__ __forceinline__ u4 make_srd(const void* p, unsigned bytes) {
  const unsigned long long a = (unsigned long long)p;
  return u4{(unsigned)a, (unsigned)(a >> 32) & 0xffffu, bytes, 0x00020000u};
}
#define LDW(arr, i, off) arr[i] = __builtin_amdgcn_raw_buffer_load_b128(rs, (off) + (i) * 1024, 0, 0)

template <int CLS>
__device__ __forceinline__ void k3_body(const unsigned* __restrict__ x32, unsigned char* ws,
                                        unsigned short* __restrict__ out, unsigned char* smem, int w) {
  const int b = blockIdx.x;
  const int g = (b & 7) + 8 * (b >> 6), jj = (b >> 3) & 7;
  const int j = 8 * g + jj;
  const int tid = threadIdx.x, l = tid & 63;
  const __amdgpu_buffer_rsrc_t rs = ws_rsrc(ws);
  f2* lds = (f2*)(smem + L_STG);
  u4 wv[32], uv[28];
  constexpr bool FW = CLS == 0;
  const int wo = (int)OFF_W1F + ((7 * jj + w) * 2048 + l) * 16;
  const int uo = (int)OFF_W2F + ((jj * 56 + 7 * w) * 256 + l) * 16;
  constexpr int HAVE = FW ? F_W2A : (CLS == 1 ? 0 : 28);
  if (FW) {
    u4 tin[4];
#pragma unroll
    for (int q = 0; q < 4; ++q) tin[q] = __builtin_amdgcn_raw_buffer_load_b128(rs, (int)OFF_T + (j * 256 + tid) * 64 + q * 16, 0, 0);
    unsigned xr[2][4];
#pragma unroll
    for (int u = 0; u < 2; ++u)
#pragma unroll
      for (int i = 0; i < 4; ++i) xr[u][i] = x32[(size_t)(j + 256 * (2 * w + u)) * 256 + l + 64 * i];
    u4 mk[3];
#pragma unroll
    for (int i = 0; i < 3; ++i) mk[i] = ((const u4*)(ws + OFF_MODK3))[l * 3 + i];
    SCHED_FENCE();
    const int kl = (l >> 4) + 4 * ((l >> 2) & 3) + 16 * (l & 3);
    Tw256 tw;
    make_tw256(tw, l);
    tw.x2a += 2 * w * ROWP; tw.x2b += 2 * w * ROWP; tw.x3a += 2 * w * ROWP; tw.x3b += 2 * w * ROWP;
    f2 t512[4];
    int zk[4], zn[4];
#pragma unroll
    for (int i = 0; i < 4; ++i) {
      t512[i] = tw2048(-4 * (kl + 64 * i) - 512);
      zk[i] = 2 * w * ROWP + zpos(kl + 64 * i);
      zn[i] = 2 * w * ROWP + zpos((256 - kl - 64 * i) & 255);
    }
    f2 twq[7];
#pragma unroll
    for (int q = 1; q < 8; ++q) twq[q - 1] = tw2048(j * q);
    int zc = zpos(tid);
    asm volatile("" : "+v"(twq[0]), "+v"(twq[1]), "+v"(twq[2]), "+v"(twq[3]), "+v"(twq[4]), "+v"(twq[5]), "+v"(twq[6]));
    asm volatile("" : "+v"(t512[0]), "+v"(t512[1]), "+v"(t512[2]), "+v"(t512[3]), "+v"(zc));
    asm volatile("" : "+v"(zk[0]), "+v"(zk[1]), "+v"(zk[2]), "+v"(zk[3]), "+v"(zn[0]), "+v"(zn[1]), "+v"(zn[2]), "+v"(zn[3]));
    PIN_TW(tw);
    SCHED_FENCE();
    {
      f2 a[8];
#pragma unroll
      for (int q = 0; q < 4; ++q) { const f4 c = __builtin_bit_cast(f4, tin[q]); a[2 * q] = c.xy; a[2 * q + 1] = c.zw; }
      cmul7<true>(a, twq);
      bfly8<true>(a);
#pragma unroll
      for (int m = 0; m < 8; ++m) lds[m * ROWP + zc] = a[m];
    }
    BAR();
    STAMP(3, 1);
    f2 v[2][4];
#pragma unroll
    for (int u = 0; u < 2; ++u) {
      f2 xk[4], xn[4];
#pragma unroll
      for (int r = 0; r < 4; ++r) { xk[r] = lds[zk[r] + u * ROWP]; xn[r] = lds[zn[r] + u * ROWP]; }
      herm2(v[u][0], v[u][1], xk[0], xn[0], t512[0], xk[1], xn[1], t512[1]);
      herm2(v[u][2], v[u][3], xk[2], xn[2], t512[2], xk[3], xn[3], t512[3]);
      if (l == 0) v[u][0] = f2{xk[0].x + xk[0].y, xk[0].x - xk[0].y};
    }
    __builtin_amdgcn_wave_barrier();
    fft256_inv2<ROWP>(v, tw, lds);
    unsigned sc1[4], sh[4];
    f2 ga[4];
#pragma unroll
    for (int i = 0; i < 4; ++i) {
      ga[i] = unpk(mk[(3 * i) >> 2][(3 * i) & 3]) * 4.76837158203125e-07f;
      sc1[i] = mk[(3 * i + 1) >> 2][(3 * i + 1) & 3];
      sh[i] = mk[(3 * i + 2) >> 2][(3 * i + 2) & 3];
    }
#pragma unroll
    for (int u = 0; u < 2; ++u) {
      f2 x1[4], h2[4];
      unsigned x1p[4];
#pragma unroll
      for (int i = 0; i < 4; ++i) {
        x1p[i] = pk(unpk(xr[u][i]) + ga[i] * v[u][i]);
        x1[i] = unpk(x1p[i]);
      }
      ln_mod(x1, sc1, sh, h2);
      unsigned* st = (unsigned*)(lds + (2 * w + u) * ROWP);
#pragma unroll
      for (int i = 0; i < 4; ++i) {
        st[l + 64 * i] = x1p[i];
        st[256 + l + 64 * i] = nosent(pk(h2[i]));
      }
    }
    BAR();
    STAMP(3, 2);
    __builtin_amdgcn_s_sleep(F_DELAY);
#pragma unroll
    for (int u = 0; u < 2; ++u)
      __builtin_amdgcn_raw_buffer_store_b128(*(const u4*)((const unsigned*)(lds + (2 * w + u) * ROWP) + 4 * l), rs,
                                             (int)OFF_X1 + ((64 * g + 8 * jj + 2 * w + u) * 64 + l) * 16, 0, AUX_SC1);
    BAR();
#pragma unroll
    for (int i = 0; i < 32; ++i) wv[i] = *(const u4*)(smem + (w * 32 + i) * 1024 + l * 16);
    BAR();
    __builtin_amdgcn_s_sleep(F_DELAY2);
#pragma unroll
    for (int i = 0; i < F_W2A; ++i) LDW(uv, i, uo);
    STAMP(3, 3);
    BAR();
  } else {
    const int pw = w - 4;
    const u4 srd = make_srd(ws, (unsigned)WS_BYTES);
    const int po = (int)OFF_W1F + ((7 * jj + pw) * 2048 + l) * 16;
    dma_run<0, P_N1>(srd, po, pw * 32768);
    BAR();
    dma_run<P_N1, 32>(srd, po, pw * 32768);
    if (CLS == 1) {
#pragma unroll
      for (int i = 0; i < P_N2; ++i) LDW(wv, i, wo);
    } else {
#pragma unroll
      for (int i = 0; i < P_N2; ++i) LDW(uv, i, uo);
    }
    BAR();
    __builtin_amdgcn_s_setprio(3);
    {
      u4 ho[2];
#pragma unroll
      for (int u = 0; u < 2; ++u) {
        const int ch = 8 * (2 * pw + u) + (l >> 3);
        ho[u] = *(const u4*)((const unsigned*)(lds + (l & 7) * ROWP) + 256 + 4 * ch);
      }
      const int ha = (int)OFF_H2F + (((g * 2 + (jj >> 2)) * 32 + 8 * pw + (l >> 4)) * 64 + 8 * (jj & 3) + (l & 7) + 32 * ((l >> 3) & 1)) * 16;
#pragma unroll
      for (int u = 0; u < 2; ++u) __builtin_amdgcn_raw_buffer_store_b128(ho[u], rs, ha + u * 4096, 0, AUX_SC1);
    }
    VM_WAIT(P_N2 + 2);
    BAR();
    BAR();
    if (CLS == 1) {
#pragma unroll
      for (int i = P_N2; i < P_N3; ++i) LDW(wv, i, wo);
    } else {
#pragma unroll
      for (int i = P_N2; i < (P_N3 < 28 ? P_N3 : 28); ++i) LDW(uv, i, uo);
    }
    SCHED_FENCE();
    {
      const int hg = (int)OFF_H2F + g * 65536, hoff = (pw * 1024 + l) * 16;
      u4 hv[16];
      __builtin_amdgcn_s_sleep(P_DELAY);
      asm volatile("" ::: "memory");
#pragma unroll
      for (int i = 0; i < 16; ++i) hv[i] = __builtin_amdgcn_raw_buffer_load_b128(rs, hg + hoff + i * 1024, 0, AUX_SC1);
      STAMPT(3, 4, 256);
      SCHED_FENCE();
      if (CLS == 1) {
#pragma unroll
        for (int i = P_N3; i < 32; ++i) LDW(wv, i, wo);
      } else {
#pragma unroll
        for (int i = P_N3; i < 28; ++i) LDW(uv, i, uo);
      }
      SCHED_FENCE();
      reload_until_written<16>(hv, rs, hg + hoff, 1024);
#pragma unroll
      for (int i = 0; i < 16; ++i) *(u4*)(smem + L_H2 + hoff + i * 1024) = hv[i];
    }
    STAMPT(3, 8, 256);
    BAR();
  }
  STAMP(3, 5); STAMPT(4, 5, 256);
  const int aro = (int)OFF_AF + ((g * 8 + w) * 28 * 64 + l) * 16;
  u4 av[28];
#define LDA(i) av[i] = __builtin_amdgcn_raw_buffer_load_b128(rs, aro + (i) * 1024, 0, AUX_SC1)
  if (CLS != 2) {
    const u4* hs = (const u4*)(smem + L_H2);
    u4 hb[4];
    const int afo = (int)OFF_AF + (((g * 8 + jj) * 28 + 2 * w) * 64 + l) * 16;
#pragma unroll
    for (int st = 0; st < 3; ++st) hb[st] = hs[st * 64 + l];
#pragma unroll
    for (int th = 0; th < 2; ++th) {
      f16v acc;
#pragma unroll
      for (int i = 0; i < 16; ++i) acc[i] = 0.f;
      if (th == 0) __builtin_amdgcn_s_setprio(3); else __builtin_amdgcn_s_setprio(2);
#pragma unroll
      for (int ks = 0; ks < 32; ++ks) {
        const int st = th * 32 + ks;
        if (st + 3 < 64) hb[(st + 3) & 3] = hs[(st + 3) * 64 + l];
        if (w2_at<HAVE>(st) >= 0) LDW(uv, w2_at<HAVE>(st) < 0 ? 0 : w2_at<HAVE>(st), uo);
        if (st >= A_ST && st < A_ST + 14) LDA(st >= A_ST && st < A_ST + 14 ? st - A_ST : 0);
        SCHED_FENCE();
        acc = mfma32(wv[ks], hb[st & 3], acc);
        SCHED_FENCE();
      }
      if (FW && th == 0) asm volatile("s_waitcnt vmcnt(%0)" :: "i"(W2B - HAVE) : "memory");
#pragma unroll
      for (int sk = 0; sk < 2; ++sk) {
        u4 o;
#pragma unroll
        for (int e = 0; e < 4; ++e) o[e] = nosent(pk(f2{silu_f(acc[8 * sk + 2 * e]), silu_f(acc[8 * sk + 2 * e + 1])}));
        __builtin_amdgcn_raw_buffer_store_b128(o, rs, afo + (th * 14 + sk) * 1024, 0, AUX_SC1);
      }
      SCHED_FENCE();
      if (th == 0) { __builtin_amdgcn_s_barrier(); SCHED_FENCE(); }
    }
    __builtin_amdgcn_s_setprio(0);
    STAMP(3, 6); STAMPT(4, 6, 256);
    __builtin_amdgcn_s_barrier();
    SCHED_FENCE();
  } else {
    __builtin_amdgcn_s_setprio(0);
    __builtin_amdgcn_s_barrier();
    __builtin_amdgcn_s_sleep(S_DELAY);
  }
  asm volatile("" ::: "memory");
  __builtin_amdgcn_s_setprio(3);
#pragma unroll
  for (int i = (CLS == 2 ? 0 : 14); i < (L1_EARLY(CLS) ? 28 : 14); ++i) LDA(i);
  __builtin_amdgcn_s_setprio(0);
  SCHED_FENCE();
  const int thf = w >> 2, nbf = (w >> 1) & 1, hff = w & 1;
  const int tok = 32 * thf + (l & 31);
  const int col = 64 * jj + 32 * nbf + 16 * (l >> 5) + 8 * hff;
  u4 x1v, gbv;
#pragma unroll
  for (int th = 0; th < 2; ++th) {
    if (th == 1) {
      BAR();
      STAMP(3, 7); STAMPT(4, 7, 256);
      x1v = __builtin_amdgcn_raw_buffer_load_b128(rs, (int)OFF_X1 + ((64 * g + tok) * 64 + (col >> 3)) * 16, 0, AUX_SC1);
      gbv = __builtin_amdgcn_raw_buffer_load_b128(rs, (int)OFF_GB + col * 2, 0, 0);
      SCHED_FENCE();
    }
    reload_until_written<14>(av + th * 14, rs, aro + th * 14 * 1024, 1024);
    SCHED_FENCE();
    if (th == 0) { STAMP(3, 12); STAMPT(4, 12, 256); }
    f4* red = (f4*)(smem + (th == 0 ? L_R01 : L_H2)) + w * 512 + l;
#pragma unroll
    for (int nb = 0; nb < 2; ++nb) {
      f16v acc;
#pragma unroll
      for (int i = 0; i < 16; ++i) acc[i] = 0.f;
#pragma unroll
      for (int k = 0; k < 14; ++k) acc = mfma32(uv[2 * k + nb], av[th * 14 + k], acc);
      if (!L1_EARLY(CLS) && th == 0 && nb == (FW ? F_L1NB : (CLS == 1 ? P_L1NB : 1))) {
        SCHED_FENCE();
        if (CLS == 2) { __builtin_amdgcn_s_barrier(); __builtin_amdgcn_s_sleep(S_DELAY2); SCHED_FENCE(); }
        __builtin_amdgcn_s_setprio(3);
#pragma unroll
        for (int i = 14; i < 28; ++i) LDA(i);
        __builtin_amdgcn_s_setprio(0);
        SCHED_FENCE();
      }
#pragma unroll
      for (int q4 = 0; q4 < 4; ++q4)
        red[(nb * 4 + q4) * 64] = f4{acc[4 * q4], acc[4 * q4 + 1], acc[4 * q4 + 2], acc[4 * q4 + 3]};
      SCHED_FENCE();
    }
  }
  STAMP(3, 9); STAMPT(4, 9, 256);
  BAR();
  f4 s0 = f4{0.f, 0.f, 0.f, 0.f}, s1 = s0;
  {
    const f4* red = (const f4*)(smem + (thf == 0 ? L_R01 : L_H2)) + (nbf * 4 + 2 * hff) * 64 + l;
#pragma unroll
    for (int ww = 0; ww < 8; ++ww) {
      s0 += red[ww * 512];
      s1 += red[ww * 512 + 64];
    }
  }
  u4 o;
  o[0] = pk(unpk(x1v[0]) + unpk(gbv[0]) * s0.xy);
  o[1] = pk(unpk(x1v[1]) + unpk(gbv[1]) * s0.zw);
  o[2] = pk(unpk(x1v[2]) + unpk(gbv[2]) * s1.xy);
  o[3] = pk(unpk(x1v[3]) + unpk(gbv[3]) * s1.zw);
  const int srow = 8 * g + (tok >> 3) + 256 * (tok & 7);
  *(u4*)(out + (size_t)srow * D + col) = o;
  STAMP(3, 10); STAMPT(4, 10, 256);
  STAMP_DRAIN();
  STAMP(3, 11); STAMPT(4, 11, 256);
}

__global__ __launch_bounds__(512, 2) void k3_fused(const unsigned* __restrict__ x32, unsigned char* ws,
                                                   unsigned short* __restrict__ out) {
  extern __shared__ __attribute__((aligned(16))) unsigned char smem[];
  const int w = __builtin_amdgcn_readfirstlane(threadIdx.x >> 6);
  STAMP(3, 0);
  if (w < 4) k3_body<0>(x32, ws, out, smem, w);
  else if (w < 7) k3_body<1>(x32, ws, out, smem, w);
  else k3_body<2>(x32, ws, out, smem, w);
}

__global__ void kz_end() { asm volatile("gf_code_end:"); }

}

extern "C" void kernel_launch(void* const* d_in, const int* in_sizes, int n_in, void* d_out, int out_size,
                              void* d_ws, size_t ws_size, hipStream_t stream) {
  const unsigned short* x = (const unsigned short*)d_in[0];
  const unsigned short* wr = (const unsigned short*)d_in[1];
  const unsigned short* wi = (const unsigned short*)d_in[2];
  const unsigned short* w1 = (const unsigned short*)d_in[4];
  const unsigned short* w2 = (const unsigned short*)d_in[5];
  const unsigned short* cv = (const unsigned short*)d_in[6];
  const unsigned short* wm = (const unsigned short*)d_in[7];
  unsigned char* ws = (unsigned char*)d_ws;
  (void)hipFuncSetAttribute((const void*)k3_fused, hipFuncAttributeMaxDynamicSharedMemorySize, LDS3_BYTES);
  k0_prep<<<K0_BLOCKS, 256, 0, stream>>>(cv, wm, wr, wi, ws);
  k1_rows<<<256, 640, 0, stream>>>((const unsigned*)x, w1, w2, ws);
  k3_fused<<<256, 512, LDS3_BYTES, stream>>>((const unsigned*)x, ws, (unsigned short*)d_out);
}
```

```cpp
#include <hip/hip_runtime.h>
#include <hip/hip_bf16.h>
#include <stdint.h>
#include <type_traits>

namespace {

typedef float f2 __attribute__((ext_vector_type(2)));
typedef float f4 __attribute__((ext_vector_type(4)));
typedef unsigned u2 __attribute__((ext_vector_type(2)));
typedef unsigned u4 __attribute__((ext_vector_type(4)));
typedef short s8 __attribute__((ext_vector_type(8)));
typedef __bf16 bf2 __attribute__((ext_vector_type(2)));
#define GAS __attribute__((address_space(1)))
typedef GAS unsigned gu32;
#define RLX_AGENT __ATOMIC_RELAXED, __HIP_MEMORY_SCOPE_AGENT
#define SCHED_FENCE() __builtin_amdgcn_sched_barrier(0)

#define BAR() do { SCHED_FENCE(); asm volatile("s_waitcnt lgkmcnt(0)" ::: "memory"); __builtin_amdgcn_s_barrier(); asm volatile("" ::: "memory"); SCHED_FENCE(); } while (0)

constexpr int S = 2048, D = 512, DF = 257, FF = 1792;

constexpr size_t OFF_PART  = 0;
constexpr size_t OFF_MODK1 = 262144;
constexpr size_t OFF_MODK3 = OFF_MODK1 + 2048;
constexpr size_t OFF_GB    = OFF_MODK3 + 3072;
constexpr size_t OFF_PQ    = OFF_MODK1 + 8192;
constexpr size_t OFF_WF    = OFF_PQ + 32768;
constexpr size_t OFF_T     = OFF_WF + (size_t)256 * 2048 * 4;
constexpr size_t OFF_H2F   = OFF_T + (size_t)256 * 2048 * 8;
constexpr size_t OFF_X1    = OFF_H2F + (size_t)S * D * 2;
constexpr size_t OFF_AF    = OFF_X1 + (size_t)S * D * 2;
constexpr size_t OFF_W1F   = OFF_AF + (size_t)S * FF * 2;
constexpr size_t OFF_W2F   = OFF_W1F + (size_t)D * FF * 2;
constexpr size_t WS_BYTES  = OFF_W2F + (size_t)D * FF * 2;

__device__ __forceinline__ float bflo(unsigned u) { return __builtin_bit_cast(float, u << 16); }
__device__ __forceinline__ float bfhi(unsigned u) { return __builtin_bit_cast(float, u & 0xffff0000u); }
__device__ __forceinline__ f2 unpk(unsigned u) { return f2{bflo(u), bfhi(u)}; }
__device__ __forceinline__ unsigned pk(f2 v) {
  bf2 b = __builtin_convertvector(v, bf2);
  return __builtin_bit_cast(unsigned, b);
}
__device__ __forceinline__ f2 rb2(f2 v) { return unpk(pk(v)); }
__device__ __forceinline__ float rb1(float v) { return bflo(pk(f2{v, v})); }

__device__ __forceinline__ f2 tw2048(int t) {
  const float f = (float)t * (1.0f / 2048.0f);
  return f2{__builtin_amdgcn_cosf(f), -__builtin_amdgcn_sinf(f)};
}

__device__ __forceinline__ f2 cmul(f2 a, f2 w) {
  f2 t, d;
  asm("v_pk_mul_f32 %0, %1, %2 op_sel:[1,1] op_sel_hi:[1,0] neg_lo:[0,1]" : "=v"(t) : "v"(a), "v"(w));
  asm("v_pk_fma_f32 %0, %1, %2, %3 op_sel:[0,0,0] op_sel_hi:[0,1,1]" : "=v"(d) : "v"(a), "v"(w), "v"(t));
  return d;
}
__device__ __forceinline__ f2 cmulc(f2 a, f2 w) {
  f2 t, d;
  asm("v_pk_mul_f32 %0, %1, %2 op_sel:[0,0] op_sel_hi:[1,0]" : "=v"(t) : "v"(a), "v"(w));
  asm("v_pk_fma_f32 %0, %1, %2, %3 op_sel:[1,1,0] op_sel_hi:[0,1,1] neg_hi:[1,0,0]" : "=v"(d) : "v"(a), "v"(w), "v"(t));
  return d;
}
template <bool CONJ> __device__ __forceinline__ void cmul3(f2& a0, f2& a1, f2& a2, f2 w0, f2 w1, f2 w2) {
  f2 t0, t1, t2;
  if (!CONJ) {
    asm("v_pk_mul_f32 %3, %0, %6 op_sel:[1,1] op_sel_hi:[1,0] neg_lo:[0,1]\n\t"
        "v_pk_mul_f32 %4, %1, %7 op_sel:[1,1] op_sel_hi:[1,0] neg_lo:[0,1]\n\t"
        "v_pk_mul_f32 %5, %2, %8 op_sel:[1,1] op_sel_hi:[1,0] neg_lo:[0,1]\n\t"
        "v_pk_fma_f32 %0, %0, %6, %3 op_sel:[0,0,0] op_sel_hi:[0,1,1]\n\t"
        "v_pk_fma_f32 %1, %1, %7, %4 op_sel:[0,0,0] op_sel_hi:[0,1,1]\n\t"
        "v_pk_fma_f32 %2, %2, %8, %5 op_sel:[0,0,0] op_sel_hi:[0,1,1]"
        : "+v"(a0), "+v"(a1), "+v"(a2), "=&v"(t0), "=&v"(t1), "=&v"(t2) : "v"(w0), "v"(w1), "v"(w2));
  } else {
    asm("v_pk_mul_f32 %3, %0, %6 op_sel:[0,0] op_sel_hi:[1,0]\n\t"
        "v_pk_mul_f32 %4, %1, %7 op_sel:[0,0] op_sel_hi:[1,0]\n\t"
        "v_pk_mul_f32 %5, %2, %8 op_sel:[0,0] op_sel_hi:[1,0]\n\t"
        "v_pk_fma_f32 %0, %0, %6, %3 op_sel:[1,1,0] op_sel_hi:[0,1,1] neg_hi:[1,0,0]\n\t"
        "v_pk_fma_f32 %1, %1, %7, %4 op_sel:[1,1,0] op_sel_hi:[0,1,1] neg_hi:[1,0,0]\n\t"
        "v_pk_fma_f32 %2, %2, %8, %5 op_sel:[1,1,0] op_sel_hi:[0,1,1] neg_hi:[1,0,0]"
        : "+v"(a0), "+v"(a1), "+v"(a2), "=&v"(t0), "=&v"(t1), "=&v"(t2) : "v"(w0), "v"(w1), "v"(w2));
  }
}
template <bool CONJ> __device__ __forceinline__ void cmul4(f2& a0, f2& a1, f2& a2, f2& a3, f2 w0, f2 w1, f2 w2, f2 w3) {
  f2 t0, t1, t2, t3;
  if (!CONJ) {
    asm("v_pk_mul_f32 %4, %0, %8 op_sel:[1,1] op_sel_hi:[1,0] neg_lo:[0,1]\n\t"
        "v_pk_mul_f32 %5, %1, %9 op_sel:[1,1] op_sel_hi:[1,0] neg_lo:[0,1]\n\t"
        "v_pk_mul_f32 %6, %2, %10 op_sel:[1,1] op_sel_hi:[1,0] neg_lo:[0,1]\n\t"
        "v_pk_mul_f32 %7, %3, %11 op_sel:[1,1] op_sel_hi:[1,0] neg_lo:[0,1]\n\t"
        "v_pk_fma_f32 %0, %0, %8, %4 op_sel:[0,0,0] op_sel_hi:[0,1,1]\n\t"
        "v_pk_fma_f32 %1, %1, %9, %5 op_sel:[0,0,0] op_sel_hi:[0,1,1]\n\t"
        "v_pk_fma_f32 %2, %2, %10, %6 op_sel:[0,0,0] op_sel_hi:[0,1,1]\n\t"
        "v_pk_fma_f32 %3, %3, %11, %7 op_sel:[0,0,0] op_sel_hi:[0,1,1]"
        : "+v"(a0), "+v"(a1), "+v"(a2), "+v"(a3), "=&v"(t0), "=&v"(t1), "=&v"(t2), "=&v"(t3)
        : "v"(w0), "v"(w1), "v"(w2), "v"(w3));
  } else {
    asm("v_pk_mul_f32 %4, %0, %8 op_sel:[0,0] op_sel_hi:[1,0]\n\t"
        "v_pk_mul_f32 %5, %1, %9 op_sel:[0,0] op_sel_hi:[1,0]\n\t"
        "v_pk_mul_f32 %6, %2, %10 op_sel:[0,0] op_sel_hi:[1,0]\n\t"
        "v_pk_mul_f32 %7, %3, %11 op_sel:[0,0] op_sel_hi:[1,0]\n\t"
        "v_pk_fma_f32 %0, %0, %8, %4 op_sel:[1,1,0] op_sel_hi:[0,1,1] neg_hi:[1,0,0]\n\t"
        "v_pk_fma_f32 %1, %1, %9, %5 op_sel:[1,1,0] op_sel_hi:[0,1,1] neg_hi:[1,0,0]\n\t"
        "v_pk_fma_f32 %2, %2, %10, %6 op_sel:[1,1,0] op_sel_hi:[0,1,1] neg_hi:[1,0,0]\n\t"
        "v_pk_fma_f32 %3, %3, %11, %7 op_sel:[1,1,0] op_sel_hi:[0,1,1] neg_hi:[1,0,0]"
        : "+v"(a0), "+v"(a1), "+v"(a2), "+v"(a3), "=&v"(t0), "=&v"(t1), "=&v"(t2), "=&v"(t3)
        : "v"(w0), "v"(w1), "v"(w2), "v"(w3));
  }
}
template <bool CONJ> __device__ __forceinline__ void cmul7(f2 (&a)[8], const f2 (&w)[7]) {
  cmul3<CONJ>(a[1], a[2], a[3], w[0], w[1], w[2]);
  cmul4<CONJ>(a[4], a[5], a[6], a[7], w[3], w[4], w[5], w[6]);
}
__device__ __forceinline__ void herm2(f2& r0, f2& r1, f2 z0, f2 q0, f2 w0, f2 z1, f2 q1, f2 w1) {
  f2 d0, d1;
  asm("v_pk_add_f32 %0, %4, %5 neg_hi:[0,1]\n\t"
      "v_pk_add_f32 %2, %4, %5 neg_lo:[0,1]\n\t"
      "v_pk_add_f32 %1, %7, %8 neg_hi:[0,1]\n\t"
      "v_pk_add_f32 %3, %7, %8 neg_lo:[0,1]\n\t"
      "v_pk_fma_f32 %0, %2, %6, %0 op_sel:[1,1,0] op_sel_hi:[1,0,1] neg_lo:[0,1,0]\n\t"
      "v_pk_fma_f32 %1, %3, %9, %1 op_sel:[1,1,0] op_sel_hi:[1,0,1] neg_lo:[0,1,0]\n\t"
      "v_pk_fma_f32 %0, %2, %6, %0 op_sel:[0,0,0] op_sel_hi:[0,1,1]\n\t"
      "v_pk_fma_f32 %1, %3, %9, %1 op_sel:[0,0,0] op_sel_hi:[0,1,1]"
      : "=&v"(r0), "=&v"(r1), "=&v"(d0), "=&v"(d1) : "v"(z0), "v"(q0), "v"(w0), "v"(z1), "v"(q1), "v"(w1));
}
__device__ __forceinline__ f2 cfma(f2 a, f2 w, f2 c) {
  f2 t, d;
  asm("v_pk_fma_f32 %0, %1, %2, %3 op_sel:[1,1,0] op_sel_hi:[1,0,1] neg_lo:[0,1,0]" : "=v"(t) : "v"(a), "v"(w), "v"(c));
  asm("v_pk_fma_f32 %0, %1, %2, %3 op_sel:[0,0,0] op_sel_hi:[0,1,1]" : "=v"(d) : "v"(a), "v"(w), "v"(t));
  return d;
}
__device__ __forceinline__ f2 add_mi(f2 a, f2 b) {
  f2 d;
  asm("v_pk_add_f32 %0, %1, %2 op_sel:[0,1] op_sel_hi:[1,0] neg_hi:[0,1]" : "=v"(d) : "v"(a), "v"(b));
  return d;
}
__device__ __forceinline__ f2 add_pi(f2 a, f2 b) {
  f2 d;
  asm("v_pk_add_f32 %0, %1, %2 op_sel:[0,1] op_sel_hi:[1,0] neg_lo:[0,1]" : "=v"(d) : "v"(a), "v"(b));
  return d;
}
__device__ __forceinline__ f2 add_cj(f2 a, f2 b) {
  f2 d;
  asm("v_pk_add_f32 %0, %1, %2 neg_hi:[0,1]" : "=v"(d) : "v"(a), "v"(b));
  return d;
}
__device__ __forceinline__ f2 sub_cj(f2 a, f2 b) {
  f2 d;
  asm("v_pk_add_f32 %0, %1, %2 neg_lo:[0,1]" : "=v"(d) : "v"(a), "v"(b));
  return d;
}
template <bool INV> __device__ __forceinline__ f2 addr(f2 a, f2 b) { return INV ? add_pi(a, b) : add_mi(a, b); }
template <bool INV> __device__ __forceinline__ f2 subr(f2 a, f2 b) { return INV ? add_mi(a, b) : add_pi(a, b); }

template <bool INV> __device__ __forceinline__ void bfly4(f2& a0, f2& a1, f2& a2, f2& a3) {
  const f2 t0 = a0 + a2, t1 = a0 - a2, t2 = a1 + a3, u = a1 - a3;
  a0 = t0 + t2; a2 = t0 - t2; a1 = addr<INV>(t1, u); a3 = subr<INV>(t1, u);
}
template <bool INV> __device__ __forceinline__ void bfly8(f2 (&a)[8]) {
  constexpr float R = 0.70710678118654752440f;
  bfly4<INV>(a[0], a[2], a[4], a[6]);
  bfly4<INV>(a[1], a[3], a[5], a[7]);
  const f2 e0 = a[0], e1 = a[2], e2 = a[4], e3 = a[6];
  const f2 o0 = a[1], o2 = a[5];
  const f2 p1 = addr<INV>(a[3], a[3]);
  const f2 p3 = subr<INV>(a[7], a[7]);
  a[0] = e0 + o0; a[4] = e0 - o0;
  a[1] = e1 + R * p1; a[5] = e1 - R * p1;
  a[2] = addr<INV>(e2, o2); a[6] = subr<INV>(e2, o2);
  a[3] = e3 - R * p3; a[7] = e3 + R * p3;
}
#define XL_PAD4(v) asm volatile("s_nop 1" : "+v"(v[0]), "+v"(v[1]), "+v"(v[2]), "+v"(v[3]))
#define XL_PAD8(v) asm volatile("s_nop 1" : "+v"(v[0]), "+v"(v[1]), "+v"(v[2]), "+v"(v[3]), "+v"(v[4]), "+v"(v[5]), "+v"(v[6]), "+v"(v[7]))

__device__ __forceinline__ void swap32(f2& a, f2& b) {
  const u2 au = __builtin_bit_cast(u2, a), bu = __builtin_bit_cast(u2, b);
  const unsigned ax = au[0], ay = au[1], bx = bu[0], by = bu[1];
  auto rx = __builtin_amdgcn_permlane32_swap(ax, bx, false, false);
  auto ry = __builtin_amdgcn_permlane32_swap(ay, by, false, false);
  a = __builtin_bit_cast(f2, u2{(unsigned)rx[0], (unsigned)ry[0]});
  b = __builtin_bit_cast(f2, u2{(unsigned)rx[1], (unsigned)ry[1]});
}
__device__ __forceinline__ void swap16(f2& a, f2& b) {
  const u2 au = __builtin_bit_cast(u2, a), bu = __builtin_bit_cast(u2, b);
  const unsigned ax = au[0], ay = au[1], bx = bu[0], by = bu[1];
  auto rx = __builtin_amdgcn_permlane16_swap(ax, bx, false, false);
  auto ry = __builtin_amdgcn_permlane16_swap(ay, by, false, false);
  a = __builtin_bit_cast(f2, u2{(unsigned)rx[0], (unsigned)ry[0]});
  b = __builtin_bit_cast(f2, u2{(unsigned)rx[1], (unsigned)ry[1]});
}
__device__ __forceinline__ void swap8(f2& a, f2& b) {
  const u2 au = __builtin_bit_cast(u2, a), bu = __builtin_bit_cast(u2, b);
  const int ax = au[0], ay = au[1], bx = bu[0], by = bu[1];
  const int nax = __builtin_amdgcn_update_dpp(ax, bx, 0x128, 0xf, 0xc, false);
  const int nbx = __builtin_amdgcn_update_dpp(bx, ax, 0x128, 0xf, 0x3, false);
  const int nay = __builtin_amdgcn_update_dpp(ay, by, 0x128, 0xf, 0xc, false);
  const int nby = __builtin_amdgcn_update_dpp(by, ay, 0x128, 0xf, 0x3, false);
  a = __builtin_bit_cast(f2, u2{(unsigned)nax, (unsigned)nay});
  b = __builtin_bit_cast(f2, u2{(unsigned)nbx, (unsigned)nby});
}

template <int CTRL> __device__ __forceinline__ float dppmov(float v) {
  return __builtin_bit_cast(float, __builtin_amdgcn_update_dpp(0, __builtin_bit_cast(int, v), CTRL, 0xf, 0xf, true));
}
__device__ __forceinline__ float wave_sum(float v) {
  v += dppmov<0xB1>(v);
  v += dppmov<0x4E>(v);
  v += dppmov<0x141>(v);
  v += dppmov<0x140>(v);
  {
    unsigned u = __builtin_bit_cast(unsigned, v);
    auto r = __builtin_amdgcn_permlane16_swap(u, u, false, false);
    v = __builtin_bit_cast(float, (unsigned)r[0]) + __builtin_bit_cast(float, (unsigned)r[1]);
  }
  {
    unsigned u = __builtin_bit_cast(unsigned, v);
    auto r = __builtin_amdgcn_permlane32_swap(u, u, false, false);
    v = __builtin_bit_cast(float, (unsigned)r[0]) + __builtin_bit_cast(float, (unsigned)r[1]);
  }
  return v;
}

__device__ __forceinline__ f4 mfma(u4 a, u4 b, f4 c) {
  return __builtin_amdgcn_mfma_f32_16x16x32_bf16(__builtin_bit_cast(s8, a), __builtin_bit_cast(s8, b), c, 0, 0, 0);
}
typedef float f16v __attribute__((ext_vector_type(16)));
__device__ __forceinline__ f16v mfma32(u4 a, u4 b, f16v c) {
  return __builtin_amdgcn_mfma_f32_32x32x16_bf16(__builtin_bit_cast(s8, a), __builtin_bit_cast(s8, b), c, 0, 0, 0);
}
__device__ __forceinline__ float silu_f(float v) {
  return v * __builtin_amdgcn_rcpf(1.0f + __expf(-v));
}

__device__ __forceinline__ void wave_sum2(float& a, float& b) {
  a += dppmov<0xB1>(a);  b += dppmov<0xB1>(b);
  a += dppmov<0x4E>(a);  b += dppmov<0x4E>(b);
  a += dppmov<0x141>(a); b += dppmov<0x141>(b);
  a += dppmov<0x140>(a); b += dppmov<0x140>(b);
  unsigned ua = __builtin_bit_cast(unsigned, a), ub = __builtin_bit_cast(unsigned, b);
  auto r = __builtin_amdgcn_permlane16_swap(ua, ub, false, false);
  float c = __builtin_bit_cast(float, (unsigned)r[0]) + __builtin_bit_cast(float, (unsigned)r[1]);
  unsigned uc = __builtin_bit_cast(unsigned, c);
  auto r2 = __builtin_amdgcn_permlane32_swap(uc, uc, false, false);
  c = __builtin_bit_cast(float, (unsigned)r2[0]) + __builtin_bit_cast(float, (unsigned)r2[1]);
  a = __builtin_bit_cast(float, __builtin_amdgcn_readlane(__builtin_bit_cast(int, c), 0));
  b = __builtin_bit_cast(float, __builtin_amdgcn_readlane(__builtin_bit_cast(int, c), 16));
}
__device__ __forceinline__ void ln_norm(const f2 (&xv)[4], f2 (&n)[4]) {
  const float x00 = xv[0].x;
  const float piv = __builtin_bit_cast(float, __builtin_amdgcn_readfirstlane(__builtin_bit_cast(int, x00)));
  f2 d[4];
#pragma unroll
  for (int i = 0; i < 4; ++i) d[i] = xv[i] - piv;
  const f2 s2 = (d[0] + d[1]) + (d[2] + d[3]);
  const f2 q2 = (d[0] * d[0] + d[1] * d[1]) + (d[2] * d[2] + d[3] * d[3]);
  float s1 = s2.x + s2.y, sq = q2.x + q2.y;
  wave_sum2(s1, sq);
  const float dm = s1 * (1.0f / 512.0f);
  const float var = __builtin_fmaxf(sq * (1.0f / 512.0f) - dm * dm, 0.0f);
  const float mbp = rb1(piv + dm) - piv;
  const float sd = rb1(__builtin_amdgcn_sqrtf(rb1(rb1(var) + 1e-5f)));
  const float rs = __builtin_amdgcn_rcpf(sd);
#pragma unroll
  for (int i = 0; i < 4; ++i) n[i] = (d[i] - mbp) * rs;
}
__device__ __forceinline__ void ln_mod(const f2 (&xv)[4], const unsigned (&sc1)[4], const unsigned (&sh)[4], f2 (&h)[4]) {
  f2 n[4];
  ln_norm(xv, n);
#pragma unroll
  for (int i = 0; i < 4; ++i) h[i] = n[i] * unpk(sc1[i]) + unpk(sh[i]);
}

struct Tw256 { f2 a[3], b[3], c[3]; int x2a, x2b, x3a, x3b; };
__device__ __forceinline__ void make_tw256(Tw256& t, int l) {
#pragma unroll
  for (int r = 1; r < 4; ++r) {
    t.a[r - 1] = tw2048(l * r * 8);
    t.b[r - 1] = tw2048((l & 15) * r * 32);
    t.c[r - 1] = tw2048((l & 3) * r * 128);
  }
  const int d2 = l >> 4, d1 = (l >> 2) & 3, d0 = l & 3;
  t.x2a = 80 * d2 + 4 * d1 + d0;
  t.x2b = 80 * d2 + 20 * d1 + d0;
  t.x3a = 80 * d2 + 4 * d1 + d0;
  t.x3b = 80 * d2 + 4 * d1 + 17 * d0;
}
#define PIN_TW(t) do { \
  asm volatile("" : "+v"(t.a[0]), "+v"(t.a[1]), "+v"(t.a[2]), "+v"(t.b[0]), "+v"(t.b[1]), "+v"(t.b[2]), "+v"(t.c[0]), "+v"(t.c[1]), "+v"(t.c[2])); \
  asm volatile("" : "+v"(t.x2a), "+v"(t.x2b), "+v"(t.x3a), "+v"(t.x3b)); } while (0)

__device__ __forceinline__ void fft256_fwd(f2 (&v)[4], const Tw256& t, f2* lds) {
  bfly4<false>(v[0], v[1], v[2], v[3]);
  cmul3<false>(v[1], v[2], v[3], t.a[0], t.a[1], t.a[2]);
  XL_PAD4(v);
  swap32(v[0], v[2]); swap32(v[1], v[3]);
  swap16(v[0], v[1]); swap16(v[2], v[3]);
  bfly4<false>(v[0], v[1], v[2], v[3]);
  cmul3<false>(v[1], v[2], v[3], t.b[0], t.b[1], t.b[2]);
#pragma unroll
  for (int r = 0; r < 4; ++r) lds[t.x2a + 20 * r] = v[r];
  __builtin_amdgcn_wave_barrier();
#pragma unroll
  for (int r = 0; r < 4; ++r) v[r] = lds[t.x2b + 4 * r];
  __builtin_amdgcn_wave_barrier();
  bfly4<false>(v[0], v[1], v[2], v[3]);
  cmul3<false>(v[1], v[2], v[3], t.c[0], t.c[1], t.c[2]);
#pragma unroll
  for (int r = 0; r < 4; ++r) lds[t.x3a + 17 * r] = v[r];
  __builtin_amdgcn_wave_barrier();
#pragma unroll
  for (int r = 0; r < 4; ++r) v[r] = lds[t.x3b + r];
  __builtin_amdgcn_wave_barrier();
  bfly4<false>(v[0], v[1], v[2], v[3]);
}
template <class H> __device__ __forceinline__ void fft256_inv(f2 (&v)[4], const Tw256& t, f2* lds, H hook) {
  bfly4<true>(v[0], v[1], v[2], v[3]);
#pragma unroll
  for (int r = 0; r < 4; ++r) lds[t.x3b + r] = v[r];
  __builtin_amdgcn_wave_barrier();
#pragma unroll
  for (int r = 0; r < 4; ++r) v[r] = lds[t.x3a + 17 * r];
  __builtin_amdgcn_wave_barrier();
  hook(std::integral_constant<int, 0>{});
  cmul3<true>(v[1], v[2], v[3], t.c[0], t.c[1], t.c[2]);
  bfly4<true>(v[0], v[1], v[2], v[3]);
#pragma unroll
  for (int r = 0; r < 4; ++r) lds[t.x2b + 4 * r] = v[r];
  __builtin_amdgcn_wave_barrier();
#pragma unroll
  for (int r = 0; r < 4; ++r) v[r] = lds[t.x2a + 20 * r];
  __builtin_amdgcn_wave_barrier();
  hook(std::integral_constant<int, 1>{});
  cmul3<true>(v[1], v[2], v[3], t.b[0], t.b[1], t.b[2]);
  bfly4<true>(v[0], v[1], v[2], v[3]);
  XL_PAD4(v);
  swap16(v[0], v[1]); swap16(v[2], v[3]);
  swap32(v[0], v[2]); swap32(v[1], v[3]);
  hook(std::integral_constant<int, 2>{});
  cmul3<true>(v[1], v[2], v[3], t.a[0], t.a[1], t.a[2]);
  bfly4<true>(v[0], v[1], v[2], v[3]);
}
template <int RP> __device__ __forceinline__ void fft256_inv2(f2 (&v)[2][4], const Tw256& t, f2* lds) {
#pragma unroll
  for (int u = 0; u < 2; ++u) bfly4<true>(v[u][0], v[u][1], v[u][2], v[u][3]);
#pragma unroll
  for (int u = 0; u < 2; ++u)
#pragma unroll
    for (int r = 0; r < 4; ++r) lds[t.x3b + u * RP + r] = v[u][r];
  __builtin_amdgcn_wave_barrier();
#pragma unroll
  for (int u = 0; u < 2; ++u)
#pragma unroll
    for (int r = 0; r < 4; ++r) v[u][r] = lds[t.x3a + u * RP + 17 * r];
  __builtin_amdgcn_wave_barrier();
#pragma unroll
  for (int u = 0; u < 2; ++u) {
    cmul3<true>(v[u][1], v[u][2], v[u][3], t.c[0], t.c[1], t.c[2]);
    bfly4<true>(v[u][0], v[u][1], v[u][2], v[u][3]);
#pragma unroll
    for (int r = 0; r < 4; ++r) lds[t.x2b + u * RP + 4 * r] = v[u][r];
  }
  __builtin_amdgcn_wave_barrier();
#pragma unroll
  for (int u = 0; u < 2; ++u)
#pragma unroll
    for (int r = 0; r < 4; ++r) v[u][r] = lds[t.x2a + u * RP + 20 * r];
  __builtin_amdgcn_wave_barrier();
#pragma unroll
  for (int u = 0; u < 2; ++u) {
    cmul3<true>(v[u][1], v[u][2], v[u][3], t.b[0], t.b[1], t.b[2]);
    bfly4<true>(v[u][0], v[u][1], v[u][2], v[u][3]);
  }
  XL_PAD4(v[0]); XL_PAD4(v[1]);
#pragma unroll
  for (int u = 0; u < 2; ++u) {
    swap16(v[u][0], v[u][1]); swap16(v[u][2], v[u][3]);
    swap32(v[u][0], v[u][2]); swap32(v[u][1], v[u][3]);
  }
#pragma unroll
  for (int u = 0; u < 2; ++u) {
    cmul3<true>(v[u][1], v[u][2], v[u][3], t.a[0], t.a[1], t.a[2]);
    bfly4<true>(v[u][0], v[u][1], v[u][2], v[u][3]);
  }
}
__device__ __forceinline__ int zpos(int k) { return k + (k >> 4); }

__device__ __host__ __forceinline__ int k2_k1(int tt, int kb) {
  int seq = (tt >> 6) * 8 + (tt & 7);
  int q = seq >> 2, k0 = seq & 3, ka = (tt >> 3) & 7;
  return 8 * (k0 + 4 * ka + 32 * kb) + q;
}

__device__ __forceinline__ void transpose8x8(const u4 (&in)[8], u4 (&out)[8]) {
#pragma unroll
  for (int c2 = 0; c2 < 4; ++c2) {
#pragma unroll
    for (int r2 = 0; r2 < 4; ++r2) {
      unsigned a = in[2 * r2][c2], b = in[2 * r2 + 1][c2];
      out[2 * c2][r2] = __builtin_amdgcn_perm(b, a, 0x05040100u);
      out[2 * c2 + 1][r2] = __builtin_amdgcn_perm(b, a, 0x07060302u);
    }
  }
}

#define PREP_AUX 16
#define T2_AUX 16
#define WF_AUX 16
constexpr int AUX_SC1 = 16;
constexpr int OOB = 0x40000000;
__device__ __forceinline__ __amdgpu_buffer_rsrc_t ws_rsrc(unsigned char* ws) {
  return __builtin_amdgcn_make_buffer_rsrc((void*)ws, 0, (int)WS_BYTES, 0x00020000);
}

__device__ __forceinline__ int prep_w1_off(int job, int l, int r) {
  const int cg = job % 7, ks = job / 7, no = l & 31, h = l >> 5;
  return ((16 * ks + 8 * h + r) * FF + cg * 256 + no * 8) * 2;
}
__device__ __forceinline__ void prep_w1_fin(const u4 (&in)[8], __amdgpu_buffer_rsrc_t rs, u4* buf, int job, int l) {
  const int cg = job % 7, ks = job / 7, no = l & 31, h = l >> 5;
  u4 out[8];
  transpose8x8(in, out);
#pragma unroll
  for (int cc = 0; cc < 8; ++cc) buf[((no >> 2) * 64 + 8 * (no & 3) + 32 * h + cc) ^ (no & 7)] = out[cc];
  __builtin_amdgcn_wave_barrier();
#pragma unroll
  for (int f = 0; f < 8; ++f)
    __builtin_amdgcn_raw_buffer_store_b128(buf[(f * 64 + l) ^ ((4 * f + ((l >> 3) & 3)) & 7)], rs,
                                           (int)OFF_W1F + (((cg * 8 + f) * 32 + ks) * 64 + l) * 16, 0, PREP_AUX);
}
__device__ __forceinline__ int prep_w2_off(int job, int l, int e) {
  const int cg = job & 1, cs = job >> 1, no = l & 31, h = l >> 5;
  return ((16 * cs + 8 * (e >> 2) + 4 * h + (e & 3)) * D + cg * 256 + no * 8) * 2;
}
__device__ __forceinline__ void prep_w2_fin(const u4 (&in)[8], __amdgpu_buffer_rsrc_t rs, u4* buf, int job, int l) {
  const int cg = job & 1, cs = job >> 1, no = l & 31, h = l >> 5;
  u4 out[8];
  transpose8x8(in, out);
  const int base = (no >> 2) * 64 + 16 * (no & 1) + 4 * ((no >> 1) & 1) + 32 * h;
#pragma unroll
  for (int cc = 0; cc < 8; ++cc) buf[base + (cc & 3) + 8 * (cc >> 2)] = out[cc];
  __builtin_amdgcn_wave_barrier();
#pragma unroll
  for (int f = 0; f < 8; ++f)
    __builtin_amdgcn_raw_buffer_store_b128(buf[f * 64 + l], rs,
                                           (int)OFF_W2F + ((((cg * 4 + (f >> 1)) * 112 + cs) * 2 + (f & 1)) * 64 + l) * 16, 0, PREP_AUX);
}

__device__ __forceinline__ __amdgpu_buffer_rsrc_t code_rsrc(int& ob) {
  unsigned long long pc;
  int oe;
  asm volatile("s_getpc_b64 %0\n1:\n\ts_mov_b32 %1, gf_code_begin-1b\n\ts_mov_b32 %2, gf_code_end-1b" : "=s"(pc), "=s"(ob), "=s"(oe));
  if (!(ob >= 0 && oe >= ob && oe < (1 << 20))) oe = 0;
  return __builtin_amdgcn_make_buffer_rsrc((void*)pc, 0, oe, 0x00020000);
}

constexpr int K0_MOD = 96, K0_WF = 128, K0_MISC = 32;
constexpr int K0_BLOCKS = K0_MOD + K0_WF + K0_MISC;
constexpr unsigned SENT = 0xFFFFFFFFu;
constexpr int FILL_PER_THREAD = (int)((OFF_H2F - OFF_T) / (256 * 256 * 16));
static_assert((size_t)FILL_PER_THREAD * 256 * 256 * 16 == OFF_H2F - OFF_T && K0_BLOCKS == 256, "fill covers the spectrum hand-off buffer");
#define FILL_AUX 16

__global__ __launch_bounds__(256) void k0_prep(const unsigned short* __restrict__ cvec,
                                               const unsigned short* __restrict__ wmod,
                                               const unsigned short* __restrict__ wr,
                                               const unsigned short* __restrict__ wi,
                                               const u4* __restrict__ xq,
                                               unsigned char* ws) {
  __shared__ float red[4][128];
  __shared__ u4 wbuf[1024];
  const int tid = threadIdx.x;
  int b = blockIdx.x;
  unsigned tcode;
  {
    int ob;
    const __amdgpu_buffer_rsrc_t cr = code_rsrc(ob);
    int co_ = tid < 128 ? (tid * 64 < ob ? tid * 64 : OOB) : (b >= K0_MOD && tid < 192 ? ob + (((b - K0_MOD) >> 3) * 64 + (tid - 128)) * 64 : OOB);
    tcode = __builtin_amdgcn_raw_buffer_load_b32(cr, co_, 0, 0);
  }
  SCHED_FENCE();
  const __amdgpu_buffer_rsrc_t rs = ws_rsrc(ws);
  auto fill = [&]() {
    asm volatile("" :: "v"(tcode));
#pragma unroll
    for (int i = 0; i < FILL_PER_THREAD; ++i)
      __builtin_amdgcn_raw_buffer_store_b128(u4{SENT, SENT, SENT, SENT}, rs,
                                             (int)OFF_T + ((blockIdx.x * FILL_PER_THREAD + i) * 256 + tid) * 16, 0, FILL_AUX);
  };
  if (b < K0_MOD) {
    const int kg = b & 3, cg = b >> 2, co = tid & 15, rr = tid >> 4;
    u4 wv[8];
    unsigned short cs[8];
#pragma unroll
    for (int i = 0; i < 8; ++i)
      wv[i] = *(const u4*)(wmod + (size_t)(128 * kg + rr + 16 * i) * 3072 + cg * 128 + co * 8);
#pragma unroll
    for (int i = 0; i < 8; ++i) cs[i] = cvec[128 * kg + rr + 16 * i];
    SCHED_FENCE();
    fill();
    SCHED_FENCE();
    f2 acc[4];
#pragma unroll
    for (int e = 0; e < 4; ++e) acc[e] = f2{0.f, 0.f};
#pragma unroll
    for (int i = 0; i < 8; ++i) {
      const float cv = bflo(cs[i]);
      const float sv = rb1(silu_f(cv));
#pragma unroll
      for (int e2 = 0; e2 < 4; ++e2) acc[e2] += sv * unpk(wv[i][e2]);
    }
#pragma unroll
    for (int e2 = 0; e2 < 4; ++e2) {
      const float v0 = acc[e2].x, v1 = acc[e2].y;
      unsigned u0 = __builtin_bit_cast(unsigned, v0), u1 = __builtin_bit_cast(unsigned, v1);
      auto r = __builtin_amdgcn_permlane16_swap(u0, u1, false, false);
      float c = __builtin_bit_cast(float, (unsigned)r[0]) + __builtin_bit_cast(float, (unsigned)r[1]);
      unsigned uc = __builtin_bit_cast(unsigned, c);
      auto r2 = __builtin_amdgcn_permlane32_swap(uc, uc, false, false);
      c = __builtin_bit_cast(float, (unsigned)r2[0]) + __builtin_bit_cast(float, (unsigned)r2[1]);
      if ((tid & 32) == 0) red[tid >> 6][co * 8 + e2 * 2 + ((tid >> 4) & 1)] = c;
    }
    BAR();
    if (tid < 128)
      ((float*)(ws + OFF_PART))[kg * 3072 + cg * 128 + tid] = (red[0][tid] + red[1][tid]) + (red[2][tid] + red[3][tid]);
    return;
  }
  b -= K0_MOD;
  if (b < K0_WF) {
    const int tq = b >> 1, k2 = (b & 1) * 128 + (tid & 127), tp2 = tid >> 7;
    unsigned short vr[16], vi[16];
#pragma unroll
    for (int e = 0; e < 16; ++e) {
      const int k1 = k2_k1(4 * tq + 2 * tp2 + (e >> 3), e & 7);
      vr[e] = wr[k1 * DF + k2];
      vi[e] = wi[k1 * DF + k2];
    }
    SCHED_FENCE();
    fill();
    u4 xt[4];
    {
      const __amdgpu_buffer_rsrc_t xr = __builtin_amdgcn_make_buffer_rsrc((void*)xq, 0, S * D * 2, 0x00020000);
#pragma unroll
      for (int i = 0; i < 4; ++i) xt[i] = __builtin_amdgcn_raw_buffer_load_b128(xr, ((b * 4 + i) * 256 + tid) * 16, 0, 0);
    }
    SCHED_FENCE();
#pragma unroll
    for (int e4 = 0; e4 < 4; ++e4) {
      u4 o;
#pragma unroll
      for (int e = 0; e < 4; ++e) o[e] = (unsigned)vr[e4 * 4 + e] | ((unsigned)vi[e4 * 4 + e] << 16);
      wbuf[(tid & 127) * 8 + ((tp2 * 4 + e4) ^ (tid & 7))] = o;
    }
    BAR();
#pragma unroll
    for (int i = 0; i < 4; ++i) {
      const int kl = i * 32 + (tid >> 3);
      __builtin_amdgcn_raw_buffer_store_b128(wbuf[kl * 8 + ((tid & 7) ^ (kl & 7))], rs,
                                             (int)OFF_WF + (((b & 1) * 128 + kl) * 512 + tq * 8 + (tid & 7)) * 16, 0, WF_AUX);
    }
    asm volatile("" :: "v"(xt[0]), "v"(xt[1]), "v"(xt[2]), "v"(xt[3]));
    return;
  }
  b -= K0_WF;
  fill();
  if (tid < 64) {
    const int e = b * 64 + tid;
    const int tt = e & 255, kb = e >> 8;
    const int k1 = k2_k1(tt, kb), nk = (2048 - k1) & 2047;
    f2 a0 = f2{bflo(wr[k1 * DF]), bflo(wi[k1 * DF])};
    f2 a1 = f2{bflo(wr[nk * DF]), -bflo(wi[nk * DF])};
    f2 b0 = f2{bflo(wr[k1 * DF + 256]), bflo(wi[k1 * DF + 256])};
    f2 b1 = f2{bflo(wr[nk * DF + 256]), -bflo(wi[nk * DF + 256])};
    f2 w0h = (a0 + a1) * 0.5f, w256h = (b0 + b1) * 0.5f;
    f2 P = (w0h + w256h) * 0.5f, Q = (w0h - w256h) * 0.5f;
    ((f4*)(ws + OFF_PQ))[e] = f4{P.x, P.y, Q.x, Q.y};
  }
}

constexpr unsigned SPIN_CAP = 1u << 18;
__device__ __forceinline__ unsigned nosent(unsigned v) { return v < SENT - 1u ? v : SENT - 1u; }
template <int N> __device__ __forceinline__ bool all_written(const u4* v) {
  unsigned m = 0u;
#pragma unroll
  for (int i = 0; i < N; ++i) {
    const unsigned a = v[i][0] > v[i][1] ? v[i][0] : v[i][1], c = v[i][2] > v[i][3] ? v[i][2] : v[i][3];
    m = m > a ? m : a;
    m = m > c ? m : c;
  }
  return __all(m != SENT);
}
template <int N> __device__ __forceinline__ void reload_until_written(u4* v, __amdgpu_buffer_rsrc_t rs, int off, int stride) {
  if (__builtin_expect(!all_written<N>(v), 0)) {
    unsigned s = 0;
    bool ok;
    do {
      __builtin_amdgcn_s_sleep(1);
      asm volatile("" : "+v"(off) :: "memory");
#pragma unroll
      for (int i = 0; i < N; ++i) v[i] = __builtin_amdgcn_raw_buffer_load_b128(rs, off + i * stride, 0, AUX_SC1);
      ok = all_written<N>(v);
    } while (!ok && ++s < SPIN_CAP);
  }
}

constexpr int SEQP = 64 + 1;
#define PREP_DELAY 75
#define C_STAG 12
#define C_DELAY 32
__device__ __forceinline__ void sync4(unsigned* cnt, unsigned& target, int l) {
  target += 4u;
  SCHED_FENCE();
  asm volatile("s_waitcnt lgkmcnt(0)" ::: "memory");
  if (l == 0) __hip_atomic_fetch_add(cnt, 1u, __ATOMIC_RELAXED, __HIP_MEMORY_SCOPE_WORKGROUP);
  for (unsigned sp = 0; sp < (SPIN_CAP << 4); ++sp)
    if (__hip_atomic_load(cnt, __ATOMIC_RELAXED, __HIP_MEMORY_SCOPE_WORKGROUP) >= target) break;
  asm volatile("" ::: "memory");
  SCHED_FENCE();
}
__device__ __forceinline__ void cols_body(unsigned char* ws, f2* lds, unsigned* cnt, int t) {
  unsigned tgt = 0u;
  const int b = blockIdx.x;
  const int l = t & 63;
  const __amdgpu_buffer_rsrc_t rs = ws_rsrc(ws);
  const int k2 = (b & 7) * 32 + (b >> 3);
  const int jr = t >> 2, qp = t & 3;
  const int co = (int)OFF_T + (jr * 1024 + k2 * 4 + qp) * 16;
  u4 wq[8];
  {
    const int wo = k2 != 0 ? (int)OFF_WF + (k2 * 256 + t) * 32 : (int)OFF_PQ + t * 16;
    const int ws1 = k2 != 0 ? 16 : 4096;
    wq[0] = __builtin_amdgcn_raw_buffer_load_b128(rs, wo, 0, 0);
    wq[1] = __builtin_amdgcn_raw_buffer_load_b128(rs, wo + ws1, 0, 0);
    const int po = k2 != 0 ? OOB : wo;
#pragma unroll
    for (int kb = 2; kb < 8; ++kb) wq[kb] = __builtin_amdgcn_raw_buffer_load_b128(rs, po + kb * 4096, 0, 0);
  }
  SCHED_FENCE();
  f2 twA[3], twB[7];
#pragma unroll
  for (int r = 1; r < 4; ++r) twA[r - 1] = tw2048(jr * r * 8);
  const int bb = l >> 3;
#pragma unroll
  for (int r = 1; r < 8; ++r) twB[r - 1] = tw2048(bb * r * 32);
  int pa = (8 * qp) * SEQP + jr;
  int pb = ((t >> 6) * 8 + (t & 7)) * SEQP + bb;
  asm volatile("" : "+v"(twB[0]), "+v"(twB[1]), "+v"(twB[2]), "+v"(twB[3]), "+v"(twB[4]), "+v"(twB[5]), "+v"(twB[6]), "+v"(twA[0]), "+v"(twA[1]), "+v"(twA[2]));
  asm volatile("" : "+v"(pa), "+v"(pb));
  SCHED_FENCE();
  __builtin_amdgcn_s_sleep(C_DELAY);
  asm volatile("" ::: "memory");
  u4 cin[4];
#pragma unroll
  for (int i = 0; i < 4; ++i) cin[i] = __builtin_amdgcn_raw_buffer_load_b128(rs, co + i * (1 << 20), 0, AUX_SC1);
  {
    u4 cin2[4];
    int co2 = co;
    __builtin_amdgcn_s_sleep(C_STAG);
    asm volatile("" : "+v"(co2) :: "memory");
#pragma unroll
    for (int i = 0; i < 4; ++i) cin2[i] = __builtin_amdgcn_raw_buffer_load_b128(rs, co2 + i * (1 << 20), 0, AUX_SC1);
    SCHED_FENCE();
    if (!all_written<4>(cin)) {
#pragma unroll
      for (int i = 0; i < 4; ++i) cin[i] = cin2[i];
      reload_until_written<4>(cin, rs, co, 1 << 20);
    }
  }
  SCHED_FENCE();
#pragma unroll
  for (int i = 0; i < 2; ++i)
    __builtin_amdgcn_raw_buffer_store_b128(u4{SENT, SENT, SENT, SENT}, rs, (int)OFF_H2F + (b * 512 + i * 256 + t) * 16, 0, FILL_AUX);
#pragma unroll
  for (int i = 0; i < 4; ++i)
    __builtin_amdgcn_raw_buffer_store_b128(u4{SENT, SENT, SENT, SENT}, rs,
                                           i * 256 + t < 896 ? (int)OFF_AF + ((256 + b) * 896 + i * 256 + t) * 16 : OOB, 0, FILL_AUX);
  SCHED_FENCE();
  f2 a0[4], a1[4];
#pragma unroll
  for (int i = 0; i < 4; ++i) { const f4 c = __builtin_bit_cast(f4, cin[i]); a0[i] = c.xy; a1[i] = c.zw; }
  bfly4<false>(a0[0], a0[1], a0[2], a0[3]);
  bfly4<false>(a1[0], a1[1], a1[2], a1[3]);
  cmul3<false>(a0[1], a0[2], a0[3], twA[0], twA[1], twA[2]);
  cmul3<false>(a1[1], a1[2], a1[3], twA[0], twA[1], twA[2]);
#pragma unroll
  for (int r = 0; r < 4; ++r) {
    lds[pa + r * SEQP] = a0[r];
    lds[pa + (4 + r) * SEQP] = a1[r];
  }
  sync4(cnt, tgt, l);
  f2 v[8];
#pragma unroll
  for (int a = 0; a < 8; ++a) v[a] = lds[pb + 8 * a];
  bfly8<false>(v);
  cmul7<false>(v, twB);
  XL_PAD8(v);
#pragma unroll
  for (int i = 0; i < 4; ++i) swap32(v[i], v[i + 4]);
  swap16(v[0], v[2]); swap16(v[1], v[3]); swap16(v[4], v[6]); swap16(v[5], v[7]);
#pragma unroll
  for (int i = 0; i < 8; i += 2) swap8(v[i], v[i + 1]);
  bfly8<false>(v);
  if (k2 != 0) {
    cmul4<false>(v[0], v[1], v[2], v[3], unpk(wq[0][0]), unpk(wq[0][1]), unpk(wq[0][2]), unpk(wq[0][3]));
    cmul4<false>(v[4], v[5], v[6], v[7], unpk(wq[1][0]), unpk(wq[1][1]), unpk(wq[1][2]), unpk(wq[1][3]));
  } else {
    sync4(cnt, tgt, l);
#pragma unroll
    for (int kb = 0; kb < 8; ++kb) lds[k2_k1(t, kb)] = v[kb];
    sync4(cnt, tgt, l);
#pragma unroll
    for (int kb = 0; kb < 8; ++kb) {
      f2 fn = lds[(2048 - k2_k1(t, kb)) & 2047];
      fn.y = -fn.y;
      const f4 c = __builtin_bit_cast(f4, wq[kb]);
      v[kb] = cfma(fn, c.zw, cmul(v[kb], c.xy));
    }
  }
  bfly8<true>(v);
  XL_PAD8(v);
#pragma unroll
  for (int i = 0; i < 8; i += 2) swap8(v[i], v[i + 1]);
  swap16(v[0], v[2]); swap16(v[1], v[3]); swap16(v[4], v[6]); swap16(v[5], v[7]);
#pragma unroll
  for (int i = 0; i < 4; ++i) swap32(v[i], v[i + 4]);
  cmul7<true>(v, twB);
  bfly8<true>(v);
  if (k2 == 0) sync4(cnt, tgt, l);
#pragma unroll
  for (int a = 0; a < 8; ++a) lds[pb + 8 * a] = v[a];
  sync4(cnt, tgt, l);
#pragma unroll
  for (int r = 0; r < 4; ++r) {
    a0[r] = lds[pa + r * SEQP];
    a1[r] = lds[pa + (4 + r) * SEQP];
  }
  cmul3<true>(a0[1], a0[2], a0[3], twA[0], twA[1], twA[2]);
  cmul3<true>(a1[1], a1[2], a1[3], twA[0], twA[1], twA[2]);
  bfly4<true>(a0[0], a0[1], a0[2], a0[3]);
  bfly4<true>(a1[0], a1[1], a1[2], a1[3]);
#pragma unroll
  for (int i = 0; i < 4; ++i)
    __builtin_amdgcn_raw_buffer_store_b128(__builtin_bit_cast(u4, f4{a0[i].x, a0[i].y, a1[i].x, a1[i].y}), rs, co + i * (1 << 20), 0, T2_AUX);
}

constexpr int ROWP = 322;

__global__ __launch_bounds__(640) void k1_rows(const unsigned* __restrict__ x32, const unsigned short* __restrict__ w1,
                                               const unsigned short* __restrict__ w2, unsigned char* ws) {
  __shared__ __attribute__((aligned(16))) f2 lds[8 * ROWP];
  __shared__ u4 pbuf[1024], tbuf[1024];
  __shared__ __attribute__((aligned(16))) f2 ldc[2048 + 64];
  __shared__ unsigned ccnt;
  __shared__ f2 modl[512];
  asm volatile("gf_code_begin:");
  const int j = blockIdx.x;
  const int tid = threadIdx.x, w = __builtin_amdgcn_readfirstlane(tid >> 6), l = tid & 63;
  const __amdgpu_buffer_rsrc_t rs = ws_rsrc(ws);
  if (w >= 8) {
    if (j >= 224) {
      if (j >= 248) {
        const int n = (j - 248) * 128 + (tid - 512), p = n >> 8, e2 = n & 255;
        f2 m = f2{0.f, 0.f};
#pragma unroll
        for (int q = 0; q < 4; ++q) m += ((const f2*)(ws + OFF_PART))[q * 1536 + 512 + n];
        m = rb2(m);
        if (p == 2) m += 1.0f;
        if (p < 3) ((unsigned*)(ws + OFF_MODK3))[((e2 & 63) * 4 + (e2 >> 6)) * 3 + (p == 0 ? 0 : (p == 2 ? 1 : 2))] = pk(m);
        else ((unsigned*)(ws + OFF_GB))[e2] = pk(m);
      }
      return;
    }
    u4 pin[8];
    const unsigned char* src = (const unsigned char*)(w == 8 ? w1 : w2);
#pragma unroll
    for (int r = 0; r < 8; ++r) pin[r] = *(const u4*)(src + (w == 8 ? prep_w1_off(j, l, r) : prep_w2_off(j, l, r)));
    asm volatile("" ::: "memory");
    SCHED_FENCE();
    __builtin_amdgcn_s_barrier();
    __builtin_amdgcn_s_barrier();
    __builtin_amdgcn_s_sleep(PREP_DELAY);
    SCHED_FENCE();
    if (w == 8) prep_w1_fin(pin, rs, pbuf, j, l);
    else prep_w2_fin(pin, rs, pbuf + 512, j, l);
    return;
  }
  if (tid == 0) ccnt = 0u;
  const int s = j + 256 * w;
  unsigned xr[4];
  f2 pm[4];
#pragma unroll
  for (int q = 0; q < 4; ++q) pm[q] = ((const f2*)(ws + OFF_PART))[q * 1536 + tid];
#pragma unroll
  for (int i = 0; i < 4; ++i) xr[i] = x32[(size_t)s * 256 + l + 64 * i];
#pragma unroll
  for (int i = 0; i < 2; ++i)
    __builtin_amdgcn_raw_buffer_store_b128(u4{SENT, SENT, SENT, SENT}, rs, w < 7 ? (int)OFF_AF + ((j * 14 + 2 * w + i) * 64 + l) * 16 : OOB, 0, FILL_AUX);
  SCHED_FENCE();
  Tw256 tw;
  make_tw256(tw, l);
  tw.x2a += w * ROWP; tw.x2b += w * ROWP; tw.x3a += w * ROWP; tw.x3b += w * ROWP;
  const int k2 = tid & 255;
  f2 t512 = tw2048(4 * k2 + 512);
  f2 twq[7];
#pragma unroll
  for (int q = 1; q < 8; ++q) twq[q - 1] = tw2048(j * q);
  int zw = w * ROWP + (l >> 4) + 4 * ((l >> 2) & 3) + 17 * (l & 3);
  int za = zpos(k2), zb = zpos((256 - k2) & 255);
  int to = (int)OFF_T + (j * 256 + k2) * 64;
  asm volatile("" : "+v"(twq[0]), "+v"(twq[1]), "+v"(twq[2]), "+v"(twq[3]), "+v"(twq[4]), "+v"(twq[5]), "+v"(twq[6]), "+v"(t512));
  asm volatile("" : "+v"(zw), "+v"(za), "+v"(zb), "+v"(to));
  PIN_TW(tw);
  SCHED_FENCE();
  {
    f2 m = rb2((pm[0] + pm[1]) + (pm[2] + pm[3]));
    if (w >= 4) m += 1.0f;
    modl[tid] = rb2(m);
  }
  f2 xv[4], v[4];
#pragma unroll
  for (int i = 0; i < 4; ++i) xv[i] = unpk(xr[i]);
  ln_norm(xv, v);
  BAR();
#pragma unroll
  for (int i = 0; i < 4; ++i) v[i] = v[i] * modl[256 + l + 64 * i] + modl[l + 64 * i];
  fft256_fwd(v, tw, lds);
#pragma unroll
  for (int r = 0; r < 4; ++r) lds[zw + 68 * r] = v[r];
  BAR();
  if (w < 4) {
    f2 a[8], z[8], zr[8];
#pragma unroll
    for (int m = 0; m < 8; ++m) { z[m] = lds[m * ROWP + za]; zr[m] = lds[m * ROWP + zb]; }
#pragma unroll
    for (int m = 0; m < 8; m += 2) herm2(a[m], a[m + 1], z[m], zr[m], t512, z[m + 1], zr[m + 1], t512);
    if (w == 0) {
#pragma unroll
      for (int m = 0; m < 8; ++m) a[m].y = k2 == 0 ? 2.0f * (z[m].x - z[m].y) : a[m].y;
    }
    bfly8<false>(a);
    cmul7<false>(a, twq);
    {
      u4* tb = tbuf + w * 256;
#pragma unroll
      for (int q = 0; q < 4; ++q)
      {
        const u4 o = __builtin_bit_cast(u4, f4{a[2 * q].x, a[2 * q].y, a[2 * q + 1].x, a[2 * q + 1].y});
        tb[l * 4 + (q ^ ((l >> 1) & 3))] = u4{nosent(o[0]), nosent(o[1]), nosent(o[2]), nosent(o[3])};
      }
      __builtin_amdgcn_wave_barrier();
#pragma unroll
      for (int i = 0; i < 4; ++i) {
        const int src = 16 * i + (l >> 2);
        __builtin_amdgcn_raw_buffer_store_b128(tb[src * 4 + ((l & 3) ^ ((src >> 1) & 3))], rs,
                                               (int)OFF_T + (j * 1024 + w * 256 + i * 64 + l) * 16, 0, AUX_SC1);
      }
    }
  } else {
    cols_body(ws, ldc, &ccnt, tid - 256);
  }
}

constexpr int L_H2 = 0;
constexpr int L_R01 = 65536;
constexpr int L_STG = 131072;
constexpr int LDS3_BYTES = 131072 + 8 * ROWP * 8;
#define P_N1 12
#define W2B 20
#define F_W2A 8
#define P_N2 16
#define P_N3 24
#define F_DELAY2 12
#define S_DELAY2 24
#define OUT_TOUCH 1
#define A_ST 49
#define S_DELAY 30
#define F_DELAY 2
#define P_DELAY 4

template <int HAVE> constexpr int w2_at(int st) {
  if (st < 32) { const int f = HAVE + st; return f < W2B ? f : -1; }
  const int f = W2B + (st - 33);
  return (st >= 33 && f < 28) ? f : -1;
}
static_assert(W2B <= 22, "register budget");

__device__ __forceinline__ void dma16(u4 srd, int voff, int soff, unsigned dst) {
  unsigned keep;
  asm volatile("s_mov_b32 %0, m0\n\ts_mov_b32 m0, %1\n\ts_nop 0\n\tbuffer_load_dwordx4 %2, %3, %4 offen lds\n\ts_mov_b32 m0, %0"
               : "=&s"(keep) : "s"(dst), "v"(voff), "s"(srd), "s"(soff) : "memory");
}
template <int I0, int I1> __device__ __forceinline__ void dma_run(u4 srd, int voff, unsigned dst) {
#pragma unroll
  for (int i = I0; i < I1; ++i) dma16(srd, voff, i * 1024, dst + i * 1024);
}
#define VM_WAIT(n) do { SCHED_FENCE(); asm volatile("s_waitcnt vmcnt(%0)" :: "i"(n) : "memory"); SCHED_FENCE(); } while (0)
__device__ __forceinline__ u4 make_srd(const void* p, unsigned bytes) {
  const unsigned long long a = (unsigned long long)p;
  return u4{(unsigned)a, (unsigned)(a >> 32) & 0xffffu, bytes, 0x00020000u};
}
#define LDW(arr, i, off) arr[i] = __builtin_amdgcn_raw_buffer_load_b128(rs, (off) + (i) * 1024, 0, 0)

template <int CLS>
__device__ __forceinline__ void k3_body(const unsigned* __restrict__ x32, unsigned char* ws,
                                        unsigned short* __restrict__ out, unsigned char* smem, int w) {
  const int b = blockIdx.x;
  const int g = (b & 7) + 8 * (b >> 6), jj = (b >> 3) & 7;
  const int j = 8 * g + jj;
  const int tid = threadIdx.x, l = tid & 63;
  const __amdgpu_buffer_rsrc_t rs = ws_rsrc(ws);
  f2* lds = (f2*)(smem + L_STG);
  u4 wv[32], uv[28];
  constexpr bool FW = CLS == 0;
  const int wo = (int)OFF_W1F + ((7 * jj + w) * 2048 + l) * 16;
  const int uo = (int)OFF_W2F + ((jj * 56 + 7 * w) * 256 + l) * 16;
  constexpr int HAVE = FW ? F_W2A : (CLS == 1 ? 0 : 28);
  if (FW) {
    u4 tin[4];
#pragma unroll
    for (int q = 0; q < 4; ++q) tin[q] = __builtin_amdgcn_raw_buffer_load_b128(rs, (int)OFF_T + (j * 256 + tid) * 64 + q * 16, 0, 0);
    unsigned xr[2][4];
#pragma unroll
    for (int u = 0; u < 2; ++u)
#pragma unroll
      for (int i = 0; i < 4; ++i) xr[u][i] = x32[(size_t)(j + 256 * (2 * w + u)) * 256 + l + 64 * i];
    u4 mk[3];
#pragma unroll
    for (int i = 0; i < 3; ++i) mk[i] = ((const u4*)(ws + OFF_MODK3))[l * 3 + i];
    SCHED_FENCE();
    const int kl = (l >> 4) + 4 * ((l >> 2) & 3) + 16 * (l & 3);
    Tw256 tw;
    make_tw256(tw, l);
    tw.x2a += 2 * w * ROWP; tw.x2b += 2 * w * ROWP; tw.x3a += 2 * w * ROWP; tw.x3b += 2 * w * ROWP;
    f2 t512[4];
    int zk[4], zn[4];
#pragma unroll
    for (int i = 0; i < 4; ++i) {
      t512[i] = tw2048(-4 * (kl + 64 * i) - 512);
      zk[i] = 2 * w * ROWP + zpos(kl + 64 * i);
      zn[i] = 2 * w * ROWP + zpos((256 - kl - 64 * i) & 255);
    }
    f2 twq[7];
#pragma unroll
    for (int q = 1; q < 8; ++q) twq[q - 1] = tw2048(j * q);
    int zc = zpos(tid);
    asm volatile("" : "+v"(twq[0]), "+v"(twq[1]), "+v"(twq[2]), "+v"(twq[3]), "+v"(twq[4]), "+v"(twq[5]), "+v"(twq[6]));
    asm volatile("" : "+v"(t512[0]), "+v"(t512[1]), "+v"(t512[2]), "+v"(t512[3]), "+v"(zc));
    asm volatile("" : "+v"(zk[0]), "+v"(zk[1]), "+v"(zk[2]), "+v"(zk[3]), "+v"(zn[0]), "+v"(zn[1]), "+v"(zn[2]), "+v"(zn[3]));
    PIN_TW(tw);
    SCHED_FENCE();
    {
      f2 a[8];
#pragma unroll
      for (int q = 0; q < 4; ++q) { const f4 c = __builtin_bit_cast(f4, tin[q]); a[2 * q] = c.xy; a[2 * q + 1] = c.zw; }
      cmul7<true>(a, twq);
      bfly8<true>(a);
#pragma unroll
      for (int m = 0; m < 8; ++m) lds[m * ROWP + zc] = a[m];
    }
    BAR();
    f2 v[2][4];
#pragma unroll
    for (int u = 0; u < 2; ++u) {
      f2 xk[4], xn[4];
#pragma unroll
      for (int r = 0; r < 4; ++r) { xk[r] = lds[zk[r] + u * ROWP]; xn[r] = lds[zn[r] + u * ROWP]; }
      herm2(v[u][0], v[u][1], xk[0], xn[0], t512[0], xk[1], xn[1], t512[1]);
      herm2(v[u][2], v[u][3], xk[2], xn[2], t512[2], xk[3], xn[3], t512[3]);
      if (l == 0) v[u][0] = f2{xk[0].x + xk[0].y, xk[0].x - xk[0].y};
    }
    __builtin_amdgcn_wave_barrier();
    fft256_inv2<ROWP>(v, tw, lds);
    unsigned sc1[4], sh[4];
    f2 ga[4];
#pragma unroll
    for (int i = 0; i < 4; ++i) {
      ga[i] = unpk(mk[(3 * i) >> 2][(3 * i) & 3]) * 4.76837158203125e-07f;
      sc1[i] = mk[(3 * i + 1) >> 2][(3 * i + 1) & 3];
      sh[i] = mk[(3 * i + 2) >> 2][(3 * i + 2) & 3];
    }
#pragma unroll
    for (int u = 0; u < 2; ++u) {
      f2 x1[4], h2[4];
      unsigned x1p[4];
#pragma unroll
      for (int i = 0; i < 4; ++i) {
        x1p[i] = pk(unpk(xr[u][i]) + ga[i] * v[u][i]);
        x1[i] = unpk(x1p[i]);
      }
      ln_mod(x1, sc1, sh, h2);
      unsigned* st = (unsigned*)(lds + (2 * w + u) * ROWP);
#pragma unroll
      for (int i = 0; i < 4; ++i) {
        st[l + 64 * i] = x1p[i];
        st[256 + l + 64 * i] = nosent(pk(h2[i]));
      }
    }
    BAR();
    __builtin_amdgcn_s_sleep(F_DELAY);
#pragma unroll
    for (int u = 0; u < 2; ++u)
      __builtin_amdgcn_raw_buffer_store_b128(*(const u4*)((const unsigned*)(lds + (2 * w + u) * ROWP) + 4 * l), rs,
                                             (int)OFF_X1 + ((64 * g + 8 * jj + 2 * w + u) * 64 + l) * 16, 0, AUX_SC1);
    BAR();
#pragma unroll
    for (int i = 0; i < 32; ++i) wv[i] = *(const u4*)(smem + (w * 32 + i) * 1024 + l * 16);
    BAR();
    __builtin_amdgcn_s_sleep(F_DELAY2);
#pragma unroll
    for (int i = 0; i < F_W2A; ++i) LDW(uv, i, uo);
    BAR();
  } else {
    const int pw = w - 4;
    const u4 srd = make_srd(ws, (unsigned)WS_BYTES);
    const int po = (int)OFF_W1F + ((7 * jj + pw) * 2048 + l) * 16;
    dma_run<0, P_N1>(srd, po, pw * 32768);
    BAR();
    dma_run<P_N1, 32>(srd, po, pw * 32768);
    if (CLS == 1) {
#pragma unroll
      for (int i = 0; i < P_N2; ++i) LDW(wv, i, wo);
    } else {
#pragma unroll
      for (int i = 0; i < P_N2; ++i) LDW(uv, i, uo);
    }
    BAR();
    __builtin_amdgcn_s_setprio(3);
    {
      u4 ho[2];
#pragma unroll
      for (int u = 0; u < 2; ++u) {
        const int ch = 8 * (2 * pw + u) + (l >> 3);
        ho[u] = *(const u4*)((const unsigned*)(lds + (l & 7) * ROWP) + 256 + 4 * ch);
      }
      const int ha = (int)OFF_H2F + (((g * 2 + (jj >> 2)) * 32 + 8 * pw + (l >> 4)) * 64 + 8 * (jj & 3) + (l & 7) + 32 * ((l >> 3) & 1)) * 16;
#pragma unroll
      for (int u = 0; u < 2; ++u) __builtin_amdgcn_raw_buffer_store_b128(ho[u], rs, ha + u * 4096, 0, AUX_SC1);
    }
    VM_WAIT(P_N2 + 2);
    BAR();
    BAR();
    if (CLS == 1) {
#pragma unroll
      for (int i = P_N2; i < P_N3; ++i) LDW(wv, i, wo);
    } else {
#pragma unroll
      for (int i = P_N2; i < (P_N3 < 28 ? P_N3 : 28); ++i) LDW(uv, i, uo);
    }
    SCHED_FENCE();
    {
      const int hg = (int)OFF_H2F + g * 65536, hoff = (pw * 1024 + l) * 16;
      u4 hv[16];
      __builtin_amdgcn_s_sleep(P_DELAY);
      asm volatile("" ::: "memory");
#pragma unroll
      for (int i = 0; i < 16; ++i) hv[i] = __builtin_amdgcn_raw_buffer_load_b128(rs, hg + hoff + i * 1024, 0, AUX_SC1);
      SCHED_FENCE();
      if (CLS == 1) {
#pragma unroll
        for (int i = P_N3; i < 32; ++i) LDW(wv, i, wo);
      } else {
#pragma unroll
        for (int i = P_N3; i < 28; ++i) LDW(uv, i, uo);
      }
      SCHED_FENCE();
      reload_until_written<16>(hv, rs, hg + hoff, 1024);
#pragma unroll
      for (int i = 0; i < 16; ++i) *(u4*)(smem + L_H2 + hoff + i * 1024) = hv[i];
    }
    BAR();
  }
  const int aro = (int)OFF_AF + ((g * 8 + w) * 28 * 64 + l) * 16;
  u4 av[28];
#define LDA(i) av[i] = __builtin_amdgcn_raw_buffer_load_b128(rs, aro + (i) * 1024, 0, AUX_SC1)
  if (CLS != 2) {
    const u4* hs = (const u4*)(smem + L_H2);
    u4 hb[4];
    const int afo = (int)OFF_AF + (((g * 8 + jj) * 28 + 2 * w) * 64 + l) * 16;
#pragma unroll
    for (int st = 0; st < 3; ++st) hb[st] = hs[st * 64 + l];
#pragma unroll
    for (int th = 0; th < 2; ++th) {
      f16v acc;
#pragma unroll
      for (int i = 0; i < 16; ++i) acc[i] = 0.f;
      if (th == 0) __builtin_amdgcn_s_setprio(3); else __builtin_amdgcn_s_setprio(2);
#pragma unroll
      for (int ks = 0; ks < 32; ++ks) {
        const int st = th * 32 + ks;
        if (st + 3 < 64) hb[(st + 3) & 3] = hs[(st + 3) * 64 + l];
        if (w2_at<HAVE>(st) >= 0) LDW(uv, w2_at<HAVE>(st) < 0 ? 0 : w2_at<HAVE>(st), uo);
        if (st >= A_ST && st < A_ST + 14) LDA(st >= A_ST && st < A_ST + 14 ? st - A_ST : 0);
        SCHED_FENCE();
        acc = mfma32(wv[ks], hb[st & 3], acc);
        SCHED_FENCE();
      }
      if (FW && th == 0) asm volatile("s_waitcnt vmcnt(%0)" :: "i"(W2B - HAVE) : "memory");
#pragma unroll
      for (int sk = 0; sk < 2; ++sk) {
        u4 o;
#pragma unroll
        for (int e = 0; e < 4; ++e) o[e] = nosent(pk(f2{silu_f(acc[8 * sk + 2 * e]), silu_f(acc[8 * sk + 2 * e + 1])}));
        __builtin_amdgcn_raw_buffer_store_b128(o, rs, afo + (th * 14 + sk) * 1024, 0, AUX_SC1);
      }
      SCHED_FENCE();
      if (th == 0) { __builtin_amdgcn_s_barrier(); SCHED_FENCE(); }
    }
    __builtin_amdgcn_s_setprio(0);
    __builtin_amdgcn_s_barrier();
    SCHED_FENCE();
  } else {
    __builtin_amdgcn_s_setprio(0);
    __builtin_amdgcn_s_barrier();
    {
      const unsigned tt = *(const GAS unsigned*)(out + (size_t)(8 * g + (l >> 3) + 256 * (l & 7)) * D + 64 * jj);
      asm volatile("" :: "v"(tt));
    }
    __builtin_amdgcn_s_sleep(S_DELAY);
  }
  asm volatile("" ::: "memory");
  __builtin_amdgcn_s_setprio(3);
#pragma unroll
  for (int i = (CLS == 2 ? 0 : 14); i < 14; ++i) LDA(i);
  __builtin_amdgcn_s_setprio(0);
  SCHED_FENCE();
  const int thf = w >> 2, nbf = (w >> 1) & 1, hff = w & 1;
  const int tok = 32 * thf + (l & 31);
  const int col = 64 * jj + 32 * nbf + 16 * (l >> 5) + 8 * hff;
  u4 x1v, gbv;
#pragma unroll
  for (int th = 0; th < 2; ++th) {
    if (th == 1) {
      BAR();
      x1v = __builtin_amdgcn_raw_buffer_load_b128(rs, (int)OFF_X1 + ((64 * g + tok) * 64 + (col >> 3)) * 16, 0, AUX_SC1);
      gbv = __builtin_amdgcn_raw_buffer_load_b128(rs, (int)OFF_GB + col * 2, 0, 0);
      SCHED_FENCE();
    }
    reload_until_written<14>(av + th * 14, rs, aro + th * 14 * 1024, 1024);
    SCHED_FENCE();
    f4* red = (f4*)(smem + (th == 0 ? L_R01 : L_H2)) + w * 512 + l;
#pragma unroll
    for (int nb = 0; nb < 2; ++nb) {
      f16v acc;
#pragma unroll
      for (int i = 0; i < 16; ++i) acc[i] = 0.f;
#pragma unroll
      for (int k = 0; k < 14; ++k) acc = mfma32(uv[2 * k + nb], av[th * 14 + k], acc);
      if (th == 0 && nb == 1) {
        SCHED_FENCE();
        if (CLS == 2) { __builtin_amdgcn_s_barrier(); __builtin_amdgcn_s_sleep(S_DELAY2); SCHED_FENCE(); }
        __builtin_amdgcn_s_setprio(3);
#pragma unroll
        for (int i = 14; i < 28; ++i) LDA(i);
        __builtin_amdgcn_s_setprio(0);
        SCHED_FENCE();
      }
#pragma unroll
      for (int q4 = 0; q4 < 4; ++q4)
        red[(nb * 4 + q4) * 64] = f4{acc[4 * q4], acc[4 * q4 + 1], acc[4 * q4 + 2], acc[4 * q4 + 3]};
      SCHED_FENCE();
    }
  }
  BAR();
  f4 s0 = f4{0.f, 0.f, 0.f, 0.f}, s1 = s0;
  {
    const f4* red = (const f4*)(smem + (thf == 0 ? L_R01 : L_H2)) + (nbf * 4 + 2 * hff) * 64 + l;
#pragma unroll
    for (int ww = 0; ww < 8; ++ww) {
      s0 += red[ww * 512];
      s1 += red[ww * 512 + 64];
    }
  }
  u4 o;
  o[0] = pk(unpk(x1v[0]) + unpk(gbv[0]) * s0.xy);
  o[1] = pk(unpk(x1v[1]) + unpk(gbv[1]) * s0.zw);
  o[2] = pk(unpk(x1v[2]) + unpk(gbv[2]) * s1.xy);
  o[3] = pk(unpk(x1v[3]) + unpk(gbv[3]) * s1.zw);
  const int srow = 8 * g + (tok >> 3) + 256 * (tok & 7);
  *(u4*)(out + (size_t)srow * D + col) = o;
}

__global__ __launch_bounds__(512, 2) void k3_fused(const unsigned* __restrict__ x32, unsigned char* ws,
                                                   unsigned short* __restrict__ out) {
  extern __shared__ __attribute__((aligned(16))) unsigned char smem[];
  const int w = __builtin_amdgcn_readfirstlane(threadIdx.x >> 6);
  if (w < 4) k3_body<0>(x32, ws, out, smem, w);
  else if (w < 7) k3_body<1>(x32, ws, out, smem, w);
  else k3_body<2>(x32, ws, out, smem, w);
}

__global__ void kz_end() { asm volatile("gf_code_end:"); }

}

extern "C" void kernel_launch(void* const* d_in, const int* in_sizes, int n_in, void* d_out, int out_size,
                              void* d_ws, size_t ws_size, hipStream_t stream) {
  const unsigned short* x = (const unsigned short*)d_in[0];
  const unsigned short* wr = (const unsigned short*)d_in[1];
  const unsigned short* wi = (const unsigned short*)d_in[2];
  const unsigned short* w1 = (const unsigned short*)d_in[4];
  const unsigned short* w2 = (const unsigned short*)d_in[5];
  const unsigned short* cv = (const unsigned short*)d_in[6];
  const unsigned short* wm = (const unsigned short*)d_in[7];
  unsigned char* ws = (unsigned char*)d_ws;
  (void)hipFuncSetAttribute((const void*)k3_fused, hipFuncAttributeMaxDynamicSharedMemorySize, LDS3_BYTES);
  k0_prep<<<K0_BLOCKS, 256, 0, stream>>>(cv, wm, wr, wi, (const u4*)x, ws);
  k1_rows<<<256, 640, 0, stream>>>((const unsigned*)x, w1, w2, ws);
  k3_fused<<<256, 512, LDS3_BYTES, stream>>>((const unsigned*)x, ws, (unsigned short*)d_out);
}
```
